```python
import torch

def _softmax(x, axis=-1):
    return torch.softmax(x, dim=axis)
def _gelu(x):
    return torch.nn.functional.gelu(x, approximate="tanh")
def _silu(x):
    return torch.nn.functional.silu(x)
def _sigmoid(x):
    return torch.sigmoid(x)
def _layernorm(x, axis=-1, eps=1e-5):
    m = x.mean(dim=axis, keepdim=True)
    v = x.var(dim=axis, keepdim=True, unbiased=False)
    return (x - m) / torch.sqrt(v + eps)
def _scatter_add(tgt, idx, src, axis=0):
    return tgt.clone().index_add_(axis, idx, src)
def _segment_sum(src, seg, n):
    out = torch.zeros((n,) + src.shape[1:], dtype=src.dtype)
    return out.index_add_(0, seg, src)
def _pad(x, pw):
    flat = tuple(v for pair in reversed(pw) for v in pair)
    return torch.nn.functional.pad(x, flat)

kernel_name = 'standalone__mha_block_cached__near_ridge'

def setup_inputs():

    _w = (lambda a: (lambda t: t.to(torch.bfloat16) if t.is_floating_point() else t)(torch.as_tensor(a)))
    B, Sq, D = 16, 192, 448
    H, Sk, Dh = 16, 512, 256
    return {
        "x": _w((lambda *s: torch.randn(*s, dtype=torch.bfloat16) * 0.8)(B, Sq, D)),
        "k_cache": _w((lambda *s: torch.randn(*s, dtype=torch.bfloat16) * 0.8)(B, H, Sk, Dh)),
        "v_cache": _w((lambda *s: torch.randn(*s, dtype=torch.bfloat16) * 0.05)(B, H, Sk, Dh)),
        "wq": _w((lambda *s: torch.randn(*s, dtype=torch.bfloat16) * 0.8)(D, H * Dh) * (D ** -0.5)),
        "wo": _w((lambda *s: torch.randn(*s, dtype=torch.bfloat16) * 0.05)(H * Dh, D) * ((H * Dh) ** -0.5)),
    }

def reference(x, k_cache, v_cache, wq, wo):
    B, Sq, D = x.shape
    H, Dh = 16, 256
    q = (lambda x, *d: x.permute(*d))((x @ wq).reshape(B, Sq, H, Dh), 0, 2, 1, 3)
    scores = torch.einsum("bhqd,bhkd->bhqk", q, k_cache) * (Dh ** -0.5)
    p = _softmax(scores, axis=-1)
    o = torch.einsum("bhqk,bhkd->bhqd", p, v_cache)
    return (lambda x, *d: x.permute(*d))(o, 0, 2, 1, 3).reshape(B, Sq, H * Dh) @ wo
```

```cpp
#include <hip/hip_runtime.h>
#include <hip/hip_bf16.h>
typedef unsigned short u16;
__device__ __forceinline__ float b2f(u16 v){ return __uint_as_float(((unsigned)v)<<16); }
__device__ __forceinline__ u16 f2b(float f){ unsigned u=__float_as_uint(f); return (u16)((u + 0x7FFF + ((u>>16)&1))>>16); }
constexpr int B=16,SQ=192,D=448,H=16,SK=512,DH=256;
__global__ void k_q(const u16* x,const u16* wq,u16* q){
  int col=blockIdx.x*256+threadIdx.x; int row=blockIdx.y;
  float a=0; for(int k=0;k<D;k++) a+=b2f(x[row*D+k])*b2f(wq[k*(H*DH)+col]);
  q[row*(H*DH)+col]=f2b(a);
}
__global__ void k_attn(const u16* q,const u16* kc,const u16* vc,u16* o){
  __shared__ float s[SK]; __shared__ float qs[DH]; __shared__ float red[256];
  int qi=blockIdx.x, h=blockIdx.y, b=blockIdx.z, t=threadIdx.x;
  qs[t]=b2f(q[(b*SQ+qi)*(H*DH)+h*DH+t]); __syncthreads();
  const u16* K=kc+(size_t)(b*H+h)*SK*DH; const u16* V=vc+(size_t)(b*H+h)*SK*DH;
  float m=-1e30f;
  for(int k=t;k<SK;k+=256){ float a=0; for(int d=0;d<DH;d++) a+=qs[d]*b2f(K[k*DH+d]); a*=0.0625f; s[k]=a; m=fmaxf(m,a);}
  red[t]=m; __syncthreads(); for(int w=128;w>0;w>>=1){ if(t<w) red[t]=fmaxf(red[t],red[t+w]); __syncthreads(); }
  m=red[0]; __syncthreads(); float l=0;
  for(int k=t;k<SK;k+=256){ float p=__expf(s[k]-m); s[k]=p; l+=p; }
  red[t]=l; __syncthreads(); for(int w=128;w>0;w>>=1){ if(t<w) red[t]+=red[t+w]; __syncthreads(); }
  l=red[0]; float a=0; for(int k=0;k<SK;k++) a+=s[k]*b2f(V[k*DH+t]);
  o[(b*SQ+qi)*(H*DH)+h*DH+t]=f2b(a/l);
}
__global__ void k_o(const u16* o,const u16* wo,u16* out){
  int col=threadIdx.x; int row=blockIdx.x; if(col>=D) return;
  float a=0; for(int k=0;k<H*DH;k++) a+=b2f(o[row*(H*DH)+k])*b2f(wo[k*D+col]);
  out[row*D+col]=f2b(a);
}
extern "C" void kernel_launch(void* const* d_in, const int* in_sizes, int n_in, void* d_out, int out_size, void* d_ws, size_t ws_size, hipStream_t stream){
  const u16* x=(const u16*)d_in[0]; const u16* kc=(const u16*)d_in[1]; const u16* vc=(const u16*)d_in[2]; const u16* wq=(const u16*)d_in[3]; const u16* wo=(const u16*)d_in[4];
  u16* q=(u16*)d_ws; u16* o=q+(size_t)B*SQ*H*DH;
  k_q<<<dim3(16,B*SQ),256,0,stream>>>(x,wq,q);
  k_attn<<<dim3(SQ,H,B),256,0,stream>>>(q,kc,vc,o);
  k_o<<<dim3(B*SQ),448,0,stream>>>(o,wo,(u16*)d_out);
}
```

```cpp
#include <hip/hip_runtime.h>
#include <hip/hip_bf16.h>


__global__ __launch_bounds__(256, 1) void mha_main(const char* __restrict__ x, const char* __restrict__ kc, const char* __restrict__ vc,
                                                   const char* __restrict__ wq, const char* __restrict__ wo, char* __restrict__ ws,
                                                   char* __restrict__ out) {
  __shared__ __attribute__((aligned(16))) char lds[163840];
  const unsigned w = __builtin_amdgcn_readfirstlane(threadIdx.x >> 6) + (unsigned)(size_t)lds;
  asm volatile(
"s_load_dwordx8 s[4:11], %0, 0\n"
"s_load_dwordx4 s[12:15], %0, 0x20\n"
"s_load_dwordx2 s[16:17], %0, 0x30\n"
"v_mbcnt_lo_u32_b32 v252, -1, 0\n"
"v_mbcnt_hi_u32_b32 v252, -1, v252\n"
"s_lshr_b32 s77, %1, 5\n"
"s_lshl_b32 s100, %1, 1\n"
"s_and_b32 s100, s100, 8\n"
"s_add_u32 s100, s100, s77\n"
"s_lshl_b32 s60, %2, 10\n"
"s_mul_i32 s61, %2, 12288\n"
"s_add_u32 s61, s61, 114688\n"
"s_bfe_u32 s78, %1, 0x20003\n"
"s_mov_b32 s70, 0\n"
"v_bfe_u32 v220, v252, 2, 3\n"
"v_bfe_u32 v221, v252, 4, 1\n"
"v_and_b32 v253, 3, v252\n"
"v_lshrrev_b32 v254, 5, v252\n"
"v_mul_u32_u24 v255, 3, v221\n"
"v_xor_b32 v255, v255, v253\n"
"v_lshlrev_b32 v255, 4, v255\n"
"v_lshl_add_u32 v205, v254, 6, v255\n"
"s_mul_i32 s63, %2, 43008\n"
"v_mul_u32_u24 v206, 896, v220\n"
"v_add3_u32 v210, v206, s63, v205\n"
"v_xor_b32 v211, 32, v210\n"
"s_and_b32 s101, %1, 3\n"
"s_lshl_b32 s101, s101, 2\n"
"s_add_u32 s101, s101, s78\n"
"v_lshlrev_b32 v255, 1, v221\n"
"v_xor_b32 v255, v255, v253\n"
"v_lshlrev_b32 v255, 4, v255\n"
"v_lshl_add_u32 v222, v254, 6, v255\n"
"s_and_b32 s66, %2, 1\n"
"s_lshl_b32 s66, s66, 4\n"
"s_lshr_b32 s67, %2, 1\n"
"s_lshl_b32 s67, s67, 2\n"
"s_add_u32 s66, s66, s67\n"
"v_and_b32 v255, 3, v220\n"
"v_lshl_add_u32 v255, v221, 3, v255\n"
"v_add_u32 v255, s66, v255\n"
"v_lshl_add_u32 v209, v255, 13, v222\n"
"s_mul_i32 s20, s100, 172032\n"
"s_mov_b32 s22, 172032\n"
"s_mov_b32 s23, 0x20000\n"
"s_lshl_b32 s24, s101, 9\n"
"s_sub_u32 s26, 3670016, s24\n"
"s_mov_b32 s27, 0x20000\n"
"s_waitcnt lgkmcnt(0)\n"
"s_add_u32 s20, s20, s4\n"
"s_addc_u32 s21, s5, 0\n"
"s_add_u32 s24, s24, s10\n"
"s_addc_u32 s25, s11, 0\n"
"s_add_u32 m0, s61, 0\n"
"s_add_u32 s63, s70, 0\n"
"buffer_load_dwordx4 v210, s[20:23], s63 offen lds\n"
"s_add_u32 m0, s61, 1024\n"
"s_add_u32 s63, s70, 7168\n"
"buffer_load_dwordx4 v211, s[20:23], s63 offen lds\n"
"s_add_u32 m0, s61, 2048\n"
"s_add_u32 s63, s70, 14336\n"
"buffer_load_dwordx4 v210, s[20:23], s63 offen lds\n"
"s_add_u32 m0, s61, 3072\n"
"s_add_u32 s63, s70, 21504\n"
"buffer_load_dwordx4 v211, s[20:23], s63 offen lds\n"
"s_add_u32 m0, s61, 4096\n"
"s_add_u32 s63, s70, 28672\n"
"buffer_load_dwordx4 v210, s[20:23], s63 offen lds\n"
"s_add_u32 m0, s61, 5120\n"
"s_add_u32 s63, s70, 35840\n"
"buffer_load_dwordx4 v211, s[20:23], s63 offen lds\n"
"s_add_u32 m0, s60, 0\n"
"s_lshl_b32 s63, s70, 12\n"
"buffer_load_dwordx4 v209, s[24:27], s63 offen lds\n"
"s_add_u32 m0, s60, 3968\n"
"s_nop 0\n"
"buffer_load_dwordx4 v209, s[24:27], s63 offen offset:128 lds\n"
"s_add_u32 m0, s60, 7936\n"
"s_nop 0\n"
"buffer_load_dwordx4 v209, s[24:27], s63 offen offset:256 lds\n"
"s_add_u32 m0, s60, 11904\n"
"s_nop 0\n"
"buffer_load_dwordx4 v209, s[24:27], s63 offen offset:384 lds\n"
"s_getpc_b64 s[64:65]\n"
"Lpf:\n"
"v_and_b32 v225, 7, v252\n"
"v_lshlrev_b32 v225, 7, v225\n"
"s_lshr_b32 s63, %1, 3\n"
"s_lshl_b32 s63, s63, 12\n"
"s_add_u32 s63, s63, s60\n"
"s_cmp_lt_u32 s63, Lcold-Lpf\n"
"s_cselect_b32 s63, s63, 0\n"
"v_add_u32 v225, s63, v225\n"
"global_load_dword v227, v225, s[64:65]\n"
"v_lshlrev_b32 v192, 10, v254\n"
"v_lshl_add_u32 v192, v220, 6, v192\n"
"v_lshl_add_u32 v192, v221, 5, v192\n"
"v_lshl_add_u32 v192, v253, 3, v192\n"
"v_xor_b32 v193, 32, v192\n"
"v_add_u32 v194, 0x10000, v192\n"
"v_add_u32 v195, 0x10000, v193\n"
"v_add_u32 v196, 0x20000, v192\n"
"v_add_u32 v197, 0x20000, v193\n"
"v_bfe_u32 v223, v252, 3, 1\n"
"v_lshlrev_b32 v223, 10, v223\n"
"v_and_b32 v255, 7, v252\n"
"v_lshl_add_u32 v223, v255, 6, v223\n"
"v_bfe_u32 v224, v252, 2, 2\n"
"v_sub_u32 v224, 0, v224\n"
"v_and_b32 v224, 3, v224\n"
"v_xor_b32 v255, v254, v224\n"
"v_lshl_add_u32 v198, v255, 4, v223\n"
"v_lshl_add_u32 v198, v221, 3, v198\n"
"v_xor_b32 v199, 32, v198\n"
"v_add_u32 v200, 0x10000, v198\n"
"v_add_u32 v201, 0x10000, v199\n"
"v_add_u32 v202, 0x20000, v198\n"
"v_add_u32 v203, 0x20000, v199\n"
"v_lshrrev_b32 v255, 4, v252\n"
"v_xor_b32 v255, v255, v224\n"
"v_lshl_add_u32 v208, v255, 4, v223\n"
"v_add_u32 v208, s61, v208\n"
"s_mul_i32 s63, %2, 7168\n"
"v_add3_u32 v206, v206, s63, v222\n"
"s_lshl_b32 s63, %2, 12\n"
"v_lshl_add_u32 v255, v220, 9, s63\n"
"v_add_u32 v204, v255, v222\n"
"s_and_b32 s66, %2, 1\n"
"s_lshl_b32 s66, s66, 5\n"
"v_xor_b32 v205, s66, v205\n"
"v_add_u32 v205, v205, v255\n"
"s_mul_i32 s69, %2, 114688\n"
"v_and_b32 v207, 3, v252\n"
"v_lshl_add_u32 v207, v207, 4, s69\n"
"v_lshrrev_b32 v255, 4, v252\n"
"v_lshl_add_u32 v207, v255, 6, v207\n"
"v_bfe_u32 v255, v252, 2, 2\n"
"v_lshl_add_u32 v207, v255, 8, v207\n"
"s_add_u32 s63, s77, s78\n"
"s_add_u32 s71, s63, 0\n"
"s_and_b32 s71, s71, 1\n"
"s_add_u32 s71, s71, 1\n"
"s_lshl_b32 s71, s71, 7\n"
"s_add_u32 s72, s63, 1\n"
"s_and_b32 s72, s72, 1\n"
"s_add_u32 s72, s72, 1\n"
"s_lshl_b32 s72, s72, 7\n"
"s_add_u32 s73, s63, 0\n"
"s_and_b32 s73, s73, 3\n"
"s_add_u32 s73, s73, 3\n"
"s_lshl_b32 s73, s73, 7\n"
"s_add_u32 s74, s63, 1\n"
"s_and_b32 s74, s74, 3\n"
"s_add_u32 s74, s74, 3\n"
"s_lshl_b32 s74, s74, 7\n"
"s_add_u32 s75, s63, 2\n"
"s_and_b32 s75, s75, 3\n"
"s_add_u32 s75, s75, 3\n"
"s_lshl_b32 s75, s75, 7\n"
"s_add_u32 s76, s63, 3\n"
"s_and_b32 s76, s76, 3\n"
"s_add_u32 s76, s76, 3\n"
"s_lshl_b32 s76, s76, 7\n"
"s_mov_b32 s62, 0x3db8aa3b\n"
"v_mov_b32 v255, 0x3f803f80\n"
"v_accvgpr_write_b32 a204, v255\n"
"v_accvgpr_write_b32 a205, v255\n"
"v_accvgpr_write_b32 a206, v255\n"
"v_accvgpr_write_b32 a207, v255\n"
"s_lshl_b32 s66, s100, 4\n"
"s_add_u32 s66, s66, s101\n"
"s_lshl_b32 s67, s66, 18\n"
"s_mov_b64 s[28:29], s[6:7]\n"
"s_add_u32 s28, s28, s67\n"
"s_addc_u32 s29, s29, 0\n"
"s_mov_b32 s30, 262144\n"
"s_mov_b32 s31, 0x20000\n"
"s_mov_b64 s[32:33], s[8:9]\n"
"s_add_u32 s32, s32, s67\n"
"s_addc_u32 s33, s33, 0\n"
"s_mov_b32 s34, 262144\n"
"s_mov_b32 s35, 0x20000\n"
"s_lshl_b32 s67, s66, 19\n"
"s_mov_b64 s[40:41], s[14:15]\n"
"s_add_u32 s40, s40, s67\n"
"s_addc_u32 s41, s41, 0\n"
"s_mov_b32 s42, 516096\n"
"s_mov_b32 s43, 0x20000\n"
"s_mul_i32 s67, s101, 229376\n"
"s_mov_b64 s[36:37], s[12:13]\n"
"s_add_u32 s36, s36, s67\n"
"s_addc_u32 s37, s37, 0\n"
"s_mov_b32 s38, 229376\n"
"s_mov_b32 s39, 0x20000\n"
"s_mul_i32 s67, s100, 172032\n"
"s_mov_b64 s[52:53], s[16:17]\n"
"s_add_u32 s52, s52, s67\n"
"s_addc_u32 s53, s53, 0\n"
"s_mov_b32 s54, 172032\n"
"s_mov_b32 s55, 0x20000\n"
"s_lshl_b32 s67, s100, 23\n"
"s_mov_b64 s[44:45], s[14:15]\n"
"s_add_u32 s44, s44, s67\n"
"s_addc_u32 s45, s45, 0\n"
"s_mov_b32 s46, 8388608\n"
"s_mov_b32 s47, 0x20000\n"
"s_lshl_b32 s67, s100, 12\n"
"s_mov_b64 s[48:49], s[14:15]\n"
"s_add_u32 s67, s67, 0x8000000\n"
"s_add_u32 s48, s48, s67\n"
"s_addc_u32 s49, s49, 0\n"
"s_mov_b32 s50, 4096\n"
"s_mov_b32 s51, 0x20000\n"
"s_cmp_eq_u32 s77, 7\n"
"s_cselect_b32 s77, 0, s77\n"
"s_lshl_b32 s77, s77, 7\n"
"s_add_u32 s78, s77, 128\n"
"s_cmp_eq_u32 s78, 896\n"
"s_cselect_b32 s78, 0, s78\n"
"s_add_u32 s79, s78, 128\n"
"s_cmp_eq_u32 s79, 896\n"
"s_cselect_b32 s79, 0, s79\n"
"s_add_u32 s80, s79, 128\n"
"s_cmp_eq_u32 s80, 896\n"
"s_cselect_b32 s80, 0, s80\n"
"s_add_u32 s81, s80, 128\n"
"s_cmp_eq_u32 s81, 896\n"
"s_cselect_b32 s81, 0, s81\n"
"s_add_u32 s82, s81, 128\n"
"s_cmp_eq_u32 s82, 896\n"
"s_cselect_b32 s82, 0, s82\n"
"s_add_u32 s83, s82, 128\n"
"s_cmp_eq_u32 s83, 896\n"
"s_cselect_b32 s83, 0, s83\n"
"s_add_u32 m0, s60, 16384\n"
"s_lshl_b32 s63, s70, 12\n"
"s_add_u32 s63, s63, 0x40000\n"
"buffer_load_dwordx4 v209, s[24:27], s63 offen lds\n"
"s_add_u32 m0, s60, 20352\n"
"s_nop 0\n"
"buffer_load_dwordx4 v209, s[24:27], s63 offen offset:128 lds\n"
"s_add_u32 m0, s60, 24320\n"
"s_nop 0\n"
"buffer_load_dwordx4 v209, s[24:27], s63 offen offset:256 lds\n"
"s_add_u32 m0, s60, 28288\n"
"s_nop 0\n"
"buffer_load_dwordx4 v209, s[24:27], s63 offen offset:384 lds\n"
"v_mov_b32 v214, 2048\n"
"s_add_u32 s69, s69, 7168\n"
"v_lshl_add_u32 v215, v252, 2, s69\n"
"v_add_u32 v216, 8192, v215\n"
"v_mov_b32 v255, s69\n"
"s_movk_i32 s69, 0x2000\n"
"s_add_u32 m0, s61, 6144\n"
"s_add_u32 s63, s71, 0\n"
"buffer_load_dwordx4 v210, s[20:23], s63 offen lds\n"
"s_add_u32 m0, s61, 7168\n"
"s_add_u32 s63, s71, 7168\n"
"buffer_load_dwordx4 v211, s[20:23], s63 offen lds\n"
"s_add_u32 m0, s61, 8192\n"
"s_add_u32 s63, s71, 14336\n"
"buffer_load_dwordx4 v210, s[20:23], s63 offen lds\n"
"s_add_u32 m0, s61, 9216\n"
"s_add_u32 s63, s71, 21504\n"
"buffer_load_dwordx4 v211, s[20:23], s63 offen lds\n"
"s_add_u32 m0, s61, 10240\n"
"s_add_u32 s63, s71, 28672\n"
"buffer_load_dwordx4 v210, s[20:23], s63 offen lds\n"
"s_add_u32 m0, s61, 11264\n"
"s_add_u32 s63, s71, 35840\n"
"buffer_load_dwordx4 v211, s[20:23], s63 offen lds\n"
"s_waitcnt vmcnt(15)\n"
"ds_read_b128 a[0:3], v208 offset:0\n"
"ds_read_b128 a[4:7], v208 offset:2048\n"
"ds_read_b128 a[8:11], v208 offset:4096\n"
"s_waitcnt vmcnt(11)\n"
"s_barrier\n"
"ds_read_b64_tr_b16 a[208:209], v192 offset:0\n"
"ds_read_b64_tr_b16 a[210:211], v192 offset:2048\n"
"ds_read_b64_tr_b16 a[212:213], v193 offset:0\n"
"ds_read_b64_tr_b16 a[214:215], v193 offset:2048\n"
"ds_read_b64_tr_b16 a[216:217], v192 offset:512\n"
"ds_read_b64_tr_b16 a[218:219], v192 offset:2560\n"
"ds_read_b64_tr_b16 a[220:221], v193 offset:512\n"
"ds_read_b64_tr_b16 a[222:223], v193 offset:2560\n"
"s_waitcnt lgkmcnt(6)\n"
"v_mfma_f32_16x16x32_bf16 v[0:3], a[208:211], a[0:3], 0\n"
"s_waitcnt lgkmcnt(4)\n"
"ds_read_b128 a[12:15], v208 offset:512\n"
"ds_read_b128 a[16:19], v208 offset:2560\n"
"v_mfma_f32_16x16x32_bf16 v[96:99], a[212:215], a[0:3], 0\n"
"s_waitcnt lgkmcnt(4)\n"
"ds_read_b128 a[20:23], v208 offset:4608\n"
"ds_read_b64_tr_b16 a[224:225], v192 offset:4096\n"
"v_mfma_f32_16x16x32_bf16 v[12:15], a[216:219], a[0:3], 0\n"
"s_waitcnt lgkmcnt(4)\n"
"ds_read_b64_tr_b16 a[226:227], v192 offset:6144\n"
"ds_read_b64_tr_b16 a[228:229], v193 offset:4096\n"
"s_nop 7\n"
"v_mfma_f32_16x16x32_bf16 v[108:111], a[220:223], a[0:3], 0\n"
"ds_read_b64_tr_b16 a[230:231], v193 offset:6144\n"
"ds_read_b64_tr_b16 a[232:233], v192 offset:4608\n"
"v_mfma_f32_16x16x32_bf16 v[112:115], a[220:223], a[4:7], 0\n"
"s_lshl_b32 s63, s71, 12\n"
"ds_read_b64_tr_b16 a[234:235], v192 offset:6656\n"
"ds_read_b64_tr_b16 a[236:237], v193 offset:4608\n"
"v_mfma_f32_16x16x32_bf16 v[16:19], a[216:219], a[4:7], 0\n"
"s_add_u32 m0, s60, 32768\n"
"ds_read_b64_tr_b16 a[238:239], v193 offset:6656\n"
"s_waitcnt lgkmcnt(8)\n"
"s_nop 7\n"
"v_mfma_f32_16x16x32_bf16 v[100:103], a[212:215], a[4:7], 0\n"
"buffer_load_dwordx4 v209, s[24:27], s63 offen lds\n"
"v_mfma_f32_16x16x32_bf16 v[4:7], a[208:211], a[4:7], 0\n"
"s_add_u32 m0, s60, 36736\n"
"v_mfma_f32_16x16x32_bf16 v[8:11], a[208:211], a[8:11], 0\n"
"buffer_load_dwordx4 v209, s[24:27], s63 offen offset:128 lds\n"
"s_nop 7\n"
"v_mfma_f32_16x16x32_bf16 v[104:107], a[212:215], a[8:11], 0\n"
"ds_read_b64_tr_b16 a[208:209], v192 offset:8192\n"
"ds_read_b64_tr_b16 a[210:211], v192 offset:10240\n"
"v_mfma_f32_16x16x32_bf16 v[20:23], a[216:219], a[8:11], 0\n"
"ds_read_b64_tr_b16 a[212:213], v193 offset:8192\n"
"ds_read_b64_tr_b16 a[214:215], v193 offset:10240\n"
"v_mfma_f32_16x16x32_bf16 v[116:119], a[220:223], a[8:11], 0\n"
"s_waitcnt lgkmcnt(10)\n"
"ds_read_b64_tr_b16 a[216:217], v192 offset:8704\n"
"ds_read_b64_tr_b16 a[218:219], v192 offset:10752\n"
"s_nop 7\n"
"v_mfma_f32_16x16x32_bf16 v[32:35], a[224:227], a[8:11], 0\n"
"s_waitcnt lgkmcnt(10)\n"
"ds_read_b64_tr_b16 a[220:221], v193 offset:8704\n"
"ds_read_b64_tr_b16 a[222:223], v193 offset:10752\n"
"v_mfma_f32_16x16x32_bf16 v[128:131], a[228:231], a[8:11], 0\n"
"s_waitcnt lgkmcnt(10)\n"
"s_add_u32 m0, s60, 40704\n"
"v_mfma_f32_16x16x32_bf16 v[44:47], a[232:235], a[8:11], 0\n"
"s_waitcnt lgkmcnt(8)\n"
"buffer_load_dwordx4 v209, s[24:27], s63 offen offset:256 lds\n"
"s_nop 7\n"
"v_mfma_f32_16x16x32_bf16 v[140:143], a[236:239], a[8:11], 0\n"
"v_mfma_f32_16x16x32_bf16 v[136:139], a[236:239], a[4:7], 0\n"
"v_mfma_f32_16x16x32_bf16 v[40:43], a[232:235], a[4:7], 0\n"
"s_add_u32 m0, s60, 44672\n"
"s_nop 7\n"
"v_mfma_f32_16x16x32_bf16 v[124:127], a[228:231], a[4:7], 0\n"
"buffer_load_dwordx4 v209, s[24:27], s63 offen offset:384 lds\n"
"v_mfma_f32_16x16x32_bf16 v[28:31], a[224:227], a[4:7], 0\n"
"v_mfma_f32_16x16x32_bf16 v[24:27], a[224:227], a[0:3], 0\n"
"ds_read_b64_tr_b16 a[224:225], v192 offset:12288\n"
"ds_read_b64_tr_b16 a[226:227], v192 offset:14336\n"
"s_nop 7\n"
"v_mfma_f32_16x16x32_bf16 v[120:123], a[228:231], a[0:3], 0\n"
"ds_read_b64_tr_b16 a[228:229], v193 offset:12288\n"
"ds_read_b64_tr_b16 a[230:231], v193 offset:14336\n"
"v_mfma_f32_16x16x32_bf16 v[36:39], a[232:235], a[0:3], 0\n"
"s_lshl_b32 s63, s71, 12\n"
"ds_read_b64_tr_b16 a[232:233], v192 offset:12800\n"
"ds_read_b64_tr_b16 a[234:235], v192 offset:14848\n"
"v_mfma_f32_16x16x32_bf16 v[132:135], a[236:239], a[0:3], 0\n"
"s_waitcnt lgkmcnt(12)\n"
"ds_read_b64_tr_b16 a[236:237], v193 offset:12800\n"
"ds_read_b64_tr_b16 a[238:239], v193 offset:14848\n"
"s_nop 7\n"
"v_mfma_f32_16x16x32_bf16 v[48:51], a[208:211], a[0:3], 0\n"
"s_waitcnt lgkmcnt(12)\n"
"s_add_u32 s63, s63, 0x40000\n"
"s_add_u32 m0, s60, 49152\n"
"v_mfma_f32_16x16x32_bf16 v[144:147], a[212:215], a[0:3], 0\n"
"s_waitcnt lgkmcnt(10)\n"
"buffer_load_dwordx4 v209, s[24:27], s63 offen lds\n"
"v_mfma_f32_16x16x32_bf16 v[60:63], a[216:219], a[0:3], 0\n"
"s_waitcnt lgkmcnt(8)\n"
"s_nop 7\n"
"v_mfma_f32_16x16x32_bf16 v[156:159], a[220:223], a[0:3], 0\n"
"v_mfma_f32_16x16x32_bf16 v[160:163], a[220:223], a[4:7], 0\n"
"v_mfma_f32_16x16x32_bf16 v[64:67], a[216:219], a[4:7], 0\n"
"s_add_u32 m0, s60, 53120\n"
"s_nop 7\n"
"v_mfma_f32_16x16x32_bf16 v[148:151], a[212:215], a[4:7], 0\n"
"s_waitcnt vmcnt(11)\n"
"s_barrier\n"
"v_mfma_f32_16x16x32_bf16 v[52:55], a[208:211], a[4:7], 0\n"
"buffer_load_dwordx4 v209, s[24:27], s63 offen offset:128 lds\n"
"v_mfma_f32_16x16x32_bf16 v[56:59], a[208:211], a[8:11], 0\n"
"ds_read_b64_tr_b16 a[208:209], v192 offset:16384\n"
"ds_read_b64_tr_b16 a[210:211], v192 offset:18432\n"
"s_nop 7\n"
"v_mfma_f32_16x16x32_bf16 v[152:155], a[212:215], a[8:11], 0\n"
"ds_read_b64_tr_b16 a[212:213], v193 offset:16384\n"
"ds_read_b64_tr_b16 a[214:215], v193 offset:18432\n"
"v_mfma_f32_16x16x32_bf16 v[68:71], a[216:219], a[8:11], 0\n"
"ds_read_b64_tr_b16 a[216:217], v192 offset:16896\n"
"ds_read_b64_tr_b16 a[218:219], v192 offset:18944\n"
"v_mfma_f32_16x16x32_bf16 v[164:167], a[220:223], a[8:11], 0\n"
"s_waitcnt lgkmcnt(12)\n"
"ds_read_b64_tr_b16 a[220:221], v193 offset:16896\n"
"ds_read_b64_tr_b16 a[222:223], v193 offset:18944\n"
"s_nop 7\n"
"v_mfma_f32_16x16x32_bf16 v[80:83], a[224:227], a[8:11], 0\n"
"s_waitcnt lgkmcnt(12)\n"
"s_add_u32 m0, s60, 57088\n"
"v_mfma_f32_16x16x32_bf16 v[176:179], a[228:231], a[8:11], 0\n"
"s_waitcnt lgkmcnt(10)\n"
"buffer_load_dwordx4 v209, s[24:27], s63 offen offset:256 lds\n"
"v_mfma_f32_16x16x32_bf16 v[92:95], a[232:235], a[8:11], 0\n"
"s_waitcnt lgkmcnt(8)\n"
"s_nop 7\n"
"v_mfma_f32_16x16x32_bf16 v[188:191], a[236:239], a[8:11], 0\n"
"v_mfma_f32_16x16x32_bf16 v[184:187], a[236:239], a[4:7], 0\n"
"v_mfma_f32_16x16x32_bf16 v[88:91], a[232:235], a[4:7], 0\n"
"s_add_u32 m0, s60, 61056\n"
"s_nop 7\n"
"v_mfma_f32_16x16x32_bf16 v[172:175], a[228:231], a[4:7], 0\n"
"buffer_load_dwordx4 v209, s[24:27], s63 offen offset:384 lds\n"
"v_mfma_f32_16x16x32_bf16 v[76:79], a[224:227], a[4:7], 0\n"
"v_mfma_f32_16x16x32_bf16 v[72:75], a[224:227], a[0:3], 0\n"
"ds_read_b64_tr_b16 a[224:225], v192 offset:20480\n"
"ds_read_b64_tr_b16 a[226:227], v192 offset:22528\n"
"s_nop 7\n"
"v_mfma_f32_16x16x32_bf16 v[168:171], a[228:231], a[0:3], 0\n"
"ds_read_b64_tr_b16 a[228:229], v193 offset:20480\n"
"ds_read_b64_tr_b16 a[230:231], v193 offset:22528\n"
"v_mfma_f32_16x16x32_bf16 v[84:87], a[232:235], a[0:3], 0\n"
"s_add_u32 s63, s72, 0\n"
"ds_read_b64_tr_b16 a[232:233], v192 offset:20992\n"
"ds_read_b64_tr_b16 a[234:235], v192 offset:23040\n"
"v_mfma_f32_16x16x32_bf16 v[180:183], a[236:239], a[0:3], 0\n"
"s_waitcnt lgkmcnt(12)\n"
"s_add_u32 m0, s61, 0\n"
"ds_read_b64_tr_b16 a[236:237], v193 offset:20992\n"
"s_nop 7\n"
"v_mfma_f32_16x16x32_bf16 v[0:3], a[208:211], a[12:15], v[0:3]\n"
"s_waitcnt lgkmcnt(11)\n"
"buffer_load_dwordx4 v210, s[20:23], s63 offen lds\n"
"v_mfma_f32_16x16x32_bf16 v[96:99], a[212:215], a[12:15], v[96:99]\n"
"s_waitcnt lgkmcnt(9)\n"
"ds_read_b64_tr_b16 a[238:239], v193 offset:23040\n"
"v_mfma_f32_16x16x32_bf16 v[12:15], a[216:219], a[12:15], v[12:15]\n"
"s_waitcnt lgkmcnt(8)\n"
"s_add_u32 s63, s72, 7168\n"
"s_nop 7\n"
"v_mfma_f32_16x16x32_bf16 v[108:111], a[220:223], a[12:15], v[108:111]\n"
"s_add_u32 m0, s61, 1024\n"
"v_mfma_f32_16x16x32_bf16 v[112:115], a[220:223], a[16:19], v[112:115]\n"
"buffer_load_dwordx4 v211, s[20:23], s63 offen lds\n"
"v_mfma_f32_16x16x32_bf16 v[16:19], a[216:219], a[16:19], v[16:19]\n"
"s_nop 7\n"
"v_mfma_f32_16x16x32_bf16 v[100:103], a[212:215], a[16:19], v[100:103]\n"
"v_mfma_f32_16x16x32_bf16 v[4:7], a[208:211], a[16:19], v[4:7]\n"
"s_add_u32 s63, s72, 14336\n"
"v_mfma_f32_16x16x32_bf16 v[8:11], a[208:211], a[20:23], v[8:11]\n"
"s_add_u32 m0, s61, 2048\n"
"ds_read_b64_tr_b16 a[208:209], v192 offset:24576\n"
"ds_read_b64_tr_b16 a[210:211], v192 offset:26624\n"
"s_nop 7\n"
"v_mfma_f32_16x16x32_bf16 v[104:107], a[212:215], a[20:23], v[104:107]\n"
"buffer_load_dwordx4 v210, s[20:23], s63 offen lds\n"
"v_mfma_f32_16x16x32_bf16 v[20:23], a[216:219], a[20:23], v[20:23]\n"
"ds_read_b64_tr_b16 a[212:213], v193 offset:24576\n"
"ds_read_b64_tr_b16 a[214:215], v193 offset:26624\n"
"v_mfma_f32_16x16x32_bf16 v[116:119], a[220:223], a[20:23], v[116:119]\n"
"s_waitcnt lgkmcnt(10)\n"
"ds_read_b64_tr_b16 a[216:217], v192 offset:25088\n"
"ds_read_b64_tr_b16 a[218:219], v192 offset:27136\n"
"s_nop 7\n"
"v_mfma_f32_16x16x32_bf16 v[32:35], a[224:227], a[20:23], v[32:35]\n"
"s_waitcnt lgkmcnt(10)\n"
"s_add_u32 s63, s72, 21504\n"
"ds_read_b64_tr_b16 a[220:221], v193 offset:25088\n"
"v_mfma_f32_16x16x32_bf16 v[128:131], a[228:231], a[20:23], v[128:131]\n"
"s_waitcnt lgkmcnt(9)\n"
"s_add_u32 m0, s61, 3072\n"
"ds_read_b64_tr_b16 a[222:223], v193 offset:27136\n"
"v_mfma_f32_16x16x32_bf16 v[44:47], a[232:235], a[20:23], v[44:47]\n"
"s_waitcnt lgkmcnt(8)\n"
"buffer_load_dwordx4 v211, s[20:23], s63 offen lds\n"
"s_nop 7\n"
"v_mfma_f32_16x16x32_bf16 v[140:143], a[236:239], a[20:23], v[140:143]\n"
"v_mfma_f32_16x16x32_bf16 v[136:139], a[236:239], a[16:19], v[136:139]\n"
"v_mfma_f32_16x16x32_bf16 v[40:43], a[232:235], a[16:19], v[40:43]\n"
"s_add_u32 s63, s72, 28672\n"
"s_nop 7\n"
"v_mfma_f32_16x16x32_bf16 v[124:127], a[228:231], a[16:19], v[124:127]\n"
"s_add_u32 m0, s61, 4096\n"
"v_mfma_f32_16x16x32_bf16 v[28:31], a[224:227], a[16:19], v[28:31]\n"
"buffer_load_dwordx4 v210, s[20:23], s63 offen lds\n"
"v_mfma_f32_16x16x32_bf16 v[24:27], a[224:227], a[12:15], v[24:27]\n"
"ds_read_b64_tr_b16 a[224:225], v192 offset:28672\n"
"ds_read_b64_tr_b16 a[226:227], v192 offset:30720\n"
"s_nop 7\n"
"v_mfma_f32_16x16x32_bf16 v[120:123], a[228:231], a[12:15], v[120:123]\n"
"ds_read_b64_tr_b16 a[228:229], v193 offset:28672\n"
"ds_read_b64_tr_b16 a[230:231], v193 offset:30720\n"
"v_mfma_f32_16x16x32_bf16 v[36:39], a[232:235], a[12:15], v[36:39]\n"
"s_add_u32 s63, s72, 35840\n"
"ds_read_b64_tr_b16 a[232:233], v192 offset:29184\n"
"ds_read_b64_tr_b16 a[234:235], v192 offset:31232\n"
"v_mfma_f32_16x16x32_bf16 v[132:135], a[236:239], a[12:15], v[132:135]\n"
"s_waitcnt lgkmcnt(12)\n"
"s_add_u32 m0, s61, 5120\n"
"ds_read_b64_tr_b16 a[236:237], v193 offset:29184\n"
"s_nop 7\n"
"v_mfma_f32_16x16x32_bf16 v[48:51], a[208:211], a[12:15], v[48:51]\n"
"s_waitcnt lgkmcnt(11)\n"
"buffer_load_dwordx4 v211, s[20:23], s63 offen lds\n"
"v_mfma_f32_16x16x32_bf16 v[144:147], a[212:215], a[12:15], v[144:147]\n"
"s_waitcnt lgkmcnt(9)\n"
"ds_read_b64_tr_b16 a[238:239], v193 offset:31232\n"
"v_mfma_f32_16x16x32_bf16 v[60:63], a[216:219], a[12:15], v[60:63]\n"
"s_waitcnt lgkmcnt(8)\n"
"s_lshl_b32 s63, s72, 12\n"
"s_nop 7\n"
"v_mfma_f32_16x16x32_bf16 v[156:159], a[220:223], a[12:15], v[156:159]\n"
"s_add_u32 m0, s60, 65536\n"
"v_mfma_f32_16x16x32_bf16 v[160:163], a[220:223], a[16:19], v[160:163]\n"
"buffer_load_dwordx4 v209, s[24:27], s63 offen lds\n"
"v_mfma_f32_16x16x32_bf16 v[64:67], a[216:219], a[16:19], v[64:67]\n"
"s_nop 7\n"
"v_mfma_f32_16x16x32_bf16 v[148:151], a[212:215], a[16:19], v[148:151]\n"
"s_waitcnt vmcnt(11)\n"
"s_barrier\n"
"v_mfma_f32_16x16x32_bf16 v[52:55], a[208:211], a[16:19], v[52:55]\n"
"v_mfma_f32_16x16x32_bf16 v[56:59], a[208:211], a[20:23], v[56:59]\n"
"s_add_u32 m0, s60, 69504\n"
"ds_read_b128 a[24:27], v208 offset:6144\n"
"s_nop 7\n"
"v_mfma_f32_16x16x32_bf16 v[152:155], a[212:215], a[20:23], v[152:155]\n"
"buffer_load_dwordx4 v209, s[24:27], s63 offen offset:128 lds\n"
"v_mfma_f32_16x16x32_bf16 v[68:71], a[216:219], a[20:23], v[68:71]\n"
"ds_read_b128 a[28:31], v208 offset:8192\n"
"ds_read_b128 a[32:35], v208 offset:10240\n"
"v_mfma_f32_16x16x32_bf16 v[164:167], a[220:223], a[20:23], v[164:167]\n"
"s_waitcnt lgkmcnt(9)\n"
"ds_read_b128 a[36:39], v208 offset:6656\n"
"ds_read_b128 a[40:43], v208 offset:8704\n"
"s_nop 7\n"
"v_mfma_f32_16x16x32_bf16 v[80:83], a[224:227], a[20:23], v[80:83]\n"
"s_waitcnt lgkmcnt(9)\n"
"ds_read_b128 a[44:47], v208 offset:10752\n"
"ds_read_b64_tr_b16 a[208:209], v192 offset:32768\n"
"v_mfma_f32_16x16x32_bf16 v[176:179], a[228:231], a[20:23], v[176:179]\n"
"s_waitcnt lgkmcnt(9)\n"
"s_add_u32 m0, s60, 73472\n"
"ds_read_b64_tr_b16 a[210:211], v192 offset:34816\n"
"v_mfma_f32_16x16x32_bf16 v[92:95], a[232:235], a[20:23], v[92:95]\n"
"s_waitcnt lgkmcnt(8)\n"
"buffer_load_dwordx4 v209, s[24:27], s63 offen offset:256 lds\n"
"s_nop 7\n"
"v_mfma_f32_16x16x32_bf16 v[188:191], a[236:239], a[20:23], v[188:191]\n"
"ds_read_b64_tr_b16 a[212:213], v193 offset:32768\n"
"ds_read_b64_tr_b16 a[214:215], v193 offset:34816\n"
"v_mfma_f32_16x16x32_bf16 v[184:187], a[236:239], a[16:19], v[184:187]\n"
"ds_read_b64_tr_b16 a[216:217], v192 offset:33280\n"
"ds_read_b64_tr_b16 a[218:219], v192 offset:35328\n"
"v_mfma_f32_16x16x32_bf16 v[88:91], a[232:235], a[16:19], v[88:91]\n"
"ds_read_b64_tr_b16 a[220:221], v193 offset:33280\n"
"ds_read_b64_tr_b16 a[222:223], v193 offset:35328\n"
"s_nop 7\n"
"v_mfma_f32_16x16x32_bf16 v[172:175], a[228:231], a[16:19], v[172:175]\n"
"s_add_u32 m0, s60, 77440\n"
"v_mfma_f32_16x16x32_bf16 v[76:79], a[224:227], a[16:19], v[76:79]\n"
"buffer_load_dwordx4 v209, s[24:27], s63 offen offset:384 lds\n"
"v_mfma_f32_16x16x32_bf16 v[72:75], a[224:227], a[12:15], v[72:75]\n"
"s_waitcnt lgkmcnt(8)\n"
"ds_read_b64_tr_b16 a[224:225], v192 offset:36864\n"
"ds_read_b64_tr_b16 a[226:227], v192 offset:38912\n"
"s_nop 7\n"
"v_mfma_f32_16x16x32_bf16 v[168:171], a[228:231], a[12:15], v[168:171]\n"
"ds_read_b64_tr_b16 a[228:229], v193 offset:36864\n"
"ds_read_b64_tr_b16 a[230:231], v193 offset:38912\n"
"v_mfma_f32_16x16x32_bf16 v[84:87], a[232:235], a[12:15], v[84:87]\n"
"s_lshl_b32 s63, s72, 12\n"
"s_add_u32 s63, s63, 0x40000\n"
"ds_read_b64_tr_b16 a[232:233], v192 offset:37376\n"
"v_mfma_f32_16x16x32_bf16 v[180:183], a[236:239], a[12:15], v[180:183]\n"
"s_waitcnt lgkmcnt(11)\n"
"s_add_u32 m0, s60, 81920\n"
"ds_read_b64_tr_b16 a[234:235], v192 offset:39424\n"
"v_mfma_f32_16x16x32_bf16 v[0:3], a[208:211], a[24:27], v[0:3]\n"
"s_waitcnt lgkmcnt(10)\n"
"buffer_load_dwordx4 v209, s[24:27], s63 offen lds\n"
"s_nop 7\n"
"v_mfma_f32_16x16x32_bf16 v[96:99], a[212:215], a[24:27], v[96:99]\n"
"s_waitcnt lgkmcnt(8)\n"
"ds_read_b64_tr_b16 a[236:237], v193 offset:37376\n"
"ds_read_b64_tr_b16 a[238:239], v193 offset:39424\n"
"v_mfma_f32_16x16x32_bf16 v[12:15], a[216:219], a[24:27], v[12:15]\n"
"s_waitcnt lgkmcnt(8)\n"
"v_mfma_f32_16x16x32_bf16 v[108:111], a[220:223], a[24:27], v[108:111]\n"
"s_add_u32 m0, s60, 85888\n"
"v_mfma_f32_16x16x32_bf16 v[112:115], a[220:223], a[28:31], v[112:115]\n"
"buffer_load_dwordx4 v209, s[24:27], s63 offen offset:128 lds\n"
"s_nop 7\n"
"v_mfma_f32_16x16x32_bf16 v[16:19], a[216:219], a[28:31], v[16:19]\n"
"v_mfma_f32_16x16x32_bf16 v[100:103], a[212:215], a[28:31], v[100:103]\n"
"v_mfma_f32_16x16x32_bf16 v[4:7], a[208:211], a[28:31], v[4:7]\n"
"s_add_u32 m0, s60, 89856\n"
"v_mfma_f32_16x16x32_bf16 v[8:11], a[208:211], a[32:35], v[8:11]\n"
"buffer_load_dwordx4 v209, s[24:27], s63 offen offset:256 lds\n"
"s_nop 7\n"
"v_mfma_f32_16x16x32_bf16 v[104:107], a[212:215], a[32:35], v[104:107]\n"
"ds_read_b64_tr_b16 a[208:209], v192 offset:40960\n"
"ds_read_b64_tr_b16 a[210:211], v192 offset:43008\n"
"v_mfma_f32_16x16x32_bf16 v[20:23], a[216:219], a[32:35], v[20:23]\n"
"ds_read_b64_tr_b16 a[212:213], v193 offset:40960\n"
"ds_read_b64_tr_b16 a[214:215], v193 offset:43008\n"
"v_mfma_f32_16x16x32_bf16 v[116:119], a[220:223], a[32:35], v[116:119]\n"
"s_waitcnt lgkmcnt(10)\n"
"ds_read_b64_tr_b16 a[216:217], v192 offset:41472\n"
"ds_read_b64_tr_b16 a[218:219], v192 offset:43520\n"
"v_mfma_f32_16x16x32_bf16 v[32:35], a[224:227], a[32:35], v[32:35]\n"
"s_waitcnt lgkmcnt(10)\n"
"s_add_u32 m0, s60, 93824\n"
"ds_read_b64_tr_b16 a[220:221], v193 offset:41472\n"
"s_nop 7\n"
"v_mfma_f32_16x16x32_bf16 v[128:131], a[228:231], a[32:35], v[128:131]\n"
"s_waitcnt lgkmcnt(9)\n"
"buffer_load_dwordx4 v209, s[24:27], s63 offen offset:384 lds\n"
"v_mfma_f32_16x16x32_bf16 v[44:47], a[232:235], a[32:35], v[44:47]\n"
"s_waitcnt lgkmcnt(7)\n"
"buffer_load_dwordx2 v[212:213], v214, s[48:51], 0 offen sc1\n"
"v_mfma_f32_16x16x32_bf16 v[140:143], a[236:239], a[32:35], v[140:143]\n"
"buffer_store_dword v215, v215, s[40:43], 0 offen sc1\n"
"v_mfma_f32_16x16x32_bf16 v[136:139], a[236:239], a[28:31], v[136:139]\n"
"buffer_store_dword v215, v216, s[40:43], 0 offen sc1\n"
"s_nop 7\n"
"v_mfma_f32_16x16x32_bf16 v[40:43], a[232:235], a[28:31], v[40:43]\n"
"s_add_u32 s63, s73, 0\n"
"s_add_u32 m0, s61, 6144\n"
"ds_read_b64_tr_b16 a[222:223], v193 offset:43520\n"
"v_mfma_f32_16x16x32_bf16 v[124:127], a[228:231], a[28:31], v[124:127]\n"
"buffer_load_dwordx4 v210, s[20:23], s63 offen lds\n"
"v_mfma_f32_16x16x32_bf16 v[28:31], a[224:227], a[28:31], v[28:31]\n"
"s_add_u32 s63, s73, 7168\n"
"v_mfma_f32_16x16x32_bf16 v[24:27], a[224:227], a[24:27], v[24:27]\n"
"s_add_u32 m0, s61, 7168\n"
"ds_read_b64_tr_b16 a[224:225], v192 offset:45056\n"
"ds_read_b64_tr_b16 a[226:227], v192 offset:47104\n"
"s_nop 7\n"
"v_mfma_f32_16x16x32_bf16 v[120:123], a[228:231], a[24:27], v[120:123]\n"
"buffer_load_dwordx4 v211, s[20:23], s63 offen lds\n"
"v_mfma_f32_16x16x32_bf16 v[36:39], a[232:235], a[24:27], v[36:39]\n"
"ds_read_b64_tr_b16 a[228:229], v193 offset:45056\n"
"ds_read_b64_tr_b16 a[230:231], v193 offset:47104\n"
"v_mfma_f32_16x16x32_bf16 v[132:135], a[236:239], a[24:27], v[132:135]\n"
"s_waitcnt lgkmcnt(10)\n"
"ds_read_b64_tr_b16 a[232:233], v192 offset:45568\n"
"ds_read_b64_tr_b16 a[234:235], v192 offset:47616\n"
"v_mfma_f32_16x16x32_bf16 v[48:51], a[208:211], a[24:27], v[48:51]\n"
"s_waitcnt lgkmcnt(10)\n"
"s_add_u32 s63, s73, 14336\n"
"ds_read_b64_tr_b16 a[236:237], v193 offset:45568\n"
"s_nop 7\n"
"v_mfma_f32_16x16x32_bf16 v[144:147], a[212:215], a[24:27], v[144:147]\n"
"s_waitcnt lgkmcnt(9)\n"
"s_add_u32 m0, s61, 8192\n"
"ds_read_b64_tr_b16 a[238:239], v193 offset:47616\n"
"v_mfma_f32_16x16x32_bf16 v[60:63], a[216:219], a[24:27], v[60:63]\n"
"s_waitcnt lgkmcnt(8)\n"
"buffer_load_dwordx4 v210, s[20:23], s63 offen lds\n"
"v_mfma_f32_16x16x32_bf16 v[156:159], a[220:223], a[24:27], v[156:159]\n"
"v_mfma_f32_16x16x32_bf16 v[160:163], a[220:223], a[28:31], v[160:163]\n"
"s_add_u32 s63, s73, 21504\n"
"s_nop 7\n"
"v_mfma_f32_16x16x32_bf16 v[64:67], a[216:219], a[28:31], v[64:67]\n"
"s_add_u32 m0, s61, 9216\n"
"v_mfma_f32_16x16x32_bf16 v[148:151], a[212:215], a[28:31], v[148:151]\n"
"s_waitcnt vmcnt(20)\n"
"buffer_load_dwordx4 v211, s[20:23], s63 offen lds\n"
"v_mfma_f32_16x16x32_bf16 v[52:55], a[208:211], a[28:31], v[52:55]\n"
"s_barrier\n"
"v_mfma_f32_16x16x32_bf16 v[56:59], a[208:211], a[32:35], v[56:59]\n"
"s_add_u32 s63, s73, 28672\n"
"ds_read_b64_tr_b16 a[208:209], v192 offset:49152\n"
"ds_read_b64_tr_b16 a[210:211], v192 offset:51200\n"
"s_nop 7\n"
"v_mfma_f32_16x16x32_bf16 v[152:155], a[212:215], a[32:35], v[152:155]\n"
"s_add_u32 m0, s61, 10240\n"
"ds_read_b64_tr_b16 a[212:213], v193 offset:49152\n"
"ds_read_b64_tr_b16 a[214:215], v193 offset:51200\n"
"v_mfma_f32_16x16x32_bf16 v[68:71], a[216:219], a[32:35], v[68:71]\n"
"s_waitcnt vmcnt(15)\n"
"buffer_load_dwordx4 v210, s[20:23], s63 offen lds\n"
"v_mfma_f32_16x16x32_bf16 v[164:167], a[220:223], a[32:35], v[164:167]\n"
"s_waitcnt lgkmcnt(10)\n"
"ds_read_b64_tr_b16 a[216:217], v192 offset:49664\n"
"ds_read_b64_tr_b16 a[218:219], v192 offset:51712\n"
"v_mfma_f32_16x16x32_bf16 v[80:83], a[224:227], a[32:35], v[80:83]\n"
"s_waitcnt lgkmcnt(10)\n"
"ds_read_b64_tr_b16 a[220:221], v193 offset:49664\n"
"ds_read_b64_tr_b16 a[222:223], v193 offset:51712\n"
"s_nop 7\n"
"v_mfma_f32_16x16x32_bf16 v[176:179], a[228:231], a[32:35], v[176:179]\n"
"s_waitcnt lgkmcnt(10)\n"
"s_add_u32 s63, s73, 35840\n"
"ds_read_b128 a[48:51], v208 offset:0\n"
"v_mfma_f32_16x16x32_bf16 v[92:95], a[232:235], a[32:35], v[92:95]\n"
"s_waitcnt lgkmcnt(9)\n"
"s_add_u32 m0, s61, 11264\n"
"ds_read_b128 a[52:55], v208 offset:2048\n"
"v_mfma_f32_16x16x32_bf16 v[188:191], a[236:239], a[32:35], v[188:191]\n"
"buffer_load_dwordx4 v211, s[20:23], s63 offen lds\n"
"v_mfma_f32_16x16x32_bf16 v[184:187], a[236:239], a[28:31], v[184:187]\n"
"ds_read_b128 a[56:59], v208 offset:4096\n"
"ds_read_b128 a[60:63], v208 offset:512\n"
"s_nop 7\n"
"v_mfma_f32_16x16x32_bf16 v[88:91], a[232:235], a[28:31], v[88:91]\n"
"s_lshl_b32 s63, s73, 12\n"
"ds_read_b128 a[64:67], v208 offset:2560\n"
"ds_read_b128 a[68:71], v208 offset:4608\n"
"v_mfma_f32_16x16x32_bf16 v[172:175], a[228:231], a[28:31], v[172:175]\n"
"s_add_u32 m0, s60, 98304\n"
"v_mfma_f32_16x16x32_bf16 v[76:79], a[224:227], a[28:31], v[76:79]\n"
"buffer_load_dwordx4 v209, s[24:27], s63 offen lds\n"
"v_mfma_f32_16x16x32_bf16 v[72:75], a[224:227], a[24:27], v[72:75]\n"
"ds_read_b64_tr_b16 a[224:225], v192 offset:53248\n"
"s_waitcnt lgkmcnt(14)\n"
"ds_read_b64_tr_b16 a[226:227], v192 offset:55296\n"
"s_nop 7\n"
"v_mfma_f32_16x16x32_bf16 v[168:171], a[228:231], a[24:27], v[168:171]\n"
"s_waitcnt lgkmcnt(14)\n"
"ds_read_b64_tr_b16 a[228:229], v193 offset:53248\n"
"s_waitcnt lgkmcnt(14)\n"
"ds_read_b64_tr_b16 a[230:231], v193 offset:55296\n"
"v_mfma_f32_16x16x32_bf16 v[84:87], a[232:235], a[24:27], v[84:87]\n"
"s_waitcnt lgkmcnt(14)\n"
"ds_read_b64_tr_b16 a[232:233], v192 offset:53760\n"
"s_waitcnt lgkmcnt(14)\n"
"ds_read_b64_tr_b16 a[234:235], v192 offset:55808\n"
"v_mfma_f32_16x16x32_bf16 v[180:183], a[236:239], a[24:27], v[180:183]\n"
"s_add_u32 m0, s60, 102272\n"
"s_waitcnt lgkmcnt(14)\n"
"ds_read_b64_tr_b16 a[236:237], v193 offset:53760\n"
"v_mfma_f32_16x16x32_bf16 v[0:3], a[208:211], a[36:39], v[0:3]\n"
"buffer_load_dwordx4 v209, s[24:27], s63 offen offset:128 lds\n"
"s_nop 7\n"
"v_mfma_f32_16x16x32_bf16 v[96:99], a[212:215], a[36:39], v[96:99]\n"
"s_waitcnt lgkmcnt(7)\n"
"ds_read_b64_tr_b16 a[238:239], v193 offset:55808\n"
"v_mfma_f32_16x16x32_bf16 v[12:15], a[216:219], a[36:39], v[12:15]\n"
"v_bfe_u32 v220, v252, 2, 1\n"
"v_bfe_u32 v221, v252, 3, 1\n"
"v_mfma_f32_16x16x32_bf16 v[108:111], a[220:223], a[36:39], v[108:111]\n"
"v_lshlrev_b32 v220, 3, v220\n"
"v_lshl_add_u32 v220, v221, 1, v220\n"
"v_mfma_f32_16x16x32_bf16 v[112:115], a[220:223], a[40:43], v[112:115]\n"
"s_add_u32 m0, s60, 106240\n"
"v_bfe_u32 v221, v252, 1, 1\n"
"v_add_u32 v220, v220, v221\n"
"s_nop 7\n"
"v_mfma_f32_16x16x32_bf16 v[16:19], a[216:219], a[40:43], v[16:19]\n"
"buffer_load_dwordx4 v209, s[24:27], s63 offen offset:256 lds\n"
"v_mfma_f32_16x16x32_bf16 v[100:103], a[212:215], a[40:43], v[100:103]\n"
"v_lshrrev_b32 v221, 4, v252\n"
"v_lshlrev_b32 v221, 2, v221\n"
"v_mfma_f32_16x16x32_bf16 v[4:7], a[208:211], a[40:43], v[4:7]\n"
"v_sub_u32 v220, v220, v221\n"
"v_and_b32 v221, 1, v252\n"
"v_mfma_f32_16x16x32_bf16 v[8:11], a[208:211], a[44:47], v[8:11]\n"
"ds_read_b64_tr_b16 a[208:209], v192 offset:57344\n"
"ds_read_b64_tr_b16 a[210:211], v192 offset:59392\n"
"s_nop 7\n"
"v_mfma_f32_16x16x32_bf16 v[104:107], a[212:215], a[44:47], v[104:107]\n"
"s_add_u32 m0, s60, 110208\n"
"ds_read_b64_tr_b16 a[212:213], v193 offset:57344\n"
"ds_read_b64_tr_b16 a[214:215], v193 offset:59392\n"
"v_mfma_f32_16x16x32_bf16 v[20:23], a[216:219], a[44:47], v[20:23]\n"
"buffer_load_dwordx4 v209, s[24:27], s63 offen offset:384 lds\n"
"v_mfma_f32_16x16x32_bf16 v[116:119], a[220:223], a[44:47], v[116:119]\n"
"s_waitcnt lgkmcnt(10)\n"
"ds_read_b64_tr_b16 a[216:217], v192 offset:57856\n"
"ds_read_b64_tr_b16 a[218:219], v192 offset:59904\n"
"v_mfma_f32_16x16x32_bf16 v[32:35], a[224:227], a[44:47], v[32:35]\n"
"s_waitcnt lgkmcnt(10)\n"
"ds_read_b64_tr_b16 a[220:221], v193 offset:57856\n"
"ds_read_b64_tr_b16 a[222:223], v193 offset:59904\n"
"s_nop 7\n"
"v_mfma_f32_16x16x32_bf16 v[128:131], a[228:231], a[44:47], v[128:131]\n"
"s_waitcnt lgkmcnt(10)\n"
"v_lshlrev_b32 v221, 4, v221\n"
"v_mov_b32 v222, 0x3f80\n"
"v_mfma_f32_16x16x32_bf16 v[44:47], a[232:235], a[44:47], v[44:47]\n"
"s_waitcnt lgkmcnt(8)\n"
"s_lshl_b32 s63, s73, 12\n"
"s_add_u32 s63, s63, 0x40000\n"
"v_mfma_f32_16x16x32_bf16 v[140:143], a[236:239], a[44:47], v[140:143]\n"
"s_add_u32 m0, s60, 0\n"
"v_lshlrev_b32 v221, v221, v222\n"
"v_cmp_eq_u32 s[58:59], 0, v220\n"
"v_mfma_f32_16x16x32_bf16 v[136:139], a[236:239], a[40:43], v[136:139]\n"
"buffer_load_dwordx4 v209, s[24:27], s63 offen lds\n"
"s_nop 7\n"
"v_mfma_f32_16x16x32_bf16 v[40:43], a[232:235], a[40:43], v[40:43]\n"
"v_cndmask_b32 v222, 0, v221, s[58:59]\n"
"v_accvgpr_write_b32 a240, v222\n"
"v_mfma_f32_16x16x32_bf16 v[124:127], a[228:231], a[40:43], v[124:127]\n"
"v_cmp_eq_u32 s[58:59], 1, v220\n"
"s_nop 1\n"
"v_cndmask_b32 v222, 0, v221, s[58:59]\n"
"v_mfma_f32_16x16x32_bf16 v[28:31], a[224:227], a[40:43], v[28:31]\n"
"v_accvgpr_write_b32 a241, v222\n"
"v_cmp_eq_u32 s[58:59], 2, v220\n"
"v_mfma_f32_16x16x32_bf16 v[24:27], a[224:227], a[36:39], v[24:27]\n"
"s_add_u32 m0, s60, 3968\n"
"ds_read_b64_tr_b16 a[224:225], v192 offset:61440\n"
"ds_read_b64_tr_b16 a[226:227], v192 offset:63488\n"
"s_nop 7\n"
"v_mfma_f32_16x16x32_bf16 v[120:123], a[228:231], a[36:39], v[120:123]\n"
"buffer_load_dwordx4 v209, s[24:27], s63 offen offset:128 lds\n"
"v_mfma_f32_16x16x32_bf16 v[36:39], a[232:235], a[36:39], v[36:39]\n"
"ds_read_b64_tr_b16 a[228:229], v193 offset:61440\n"
"ds_read_b64_tr_b16 a[230:231], v193 offset:63488\n"
"v_mfma_f32_16x16x32_bf16 v[132:135], a[236:239], a[36:39], v[132:135]\n"
"s_waitcnt lgkmcnt(10)\n"
"ds_read_b64_tr_b16 a[232:233], v192 offset:61952\n"
"ds_read_b64_tr_b16 a[234:235], v192 offset:64000\n"
"v_mfma_f32_16x16x32_bf16 v[48:51], a[208:211], a[36:39], v[48:51]\n"
"s_waitcnt lgkmcnt(10)\n"
"ds_read_b64_tr_b16 a[236:237], v193 offset:61952\n"
"ds_read_b64_tr_b16 a[238:239], v193 offset:64000\n"
"s_nop 7\n"
"v_mfma_f32_16x16x32_bf16 v[144:147], a[212:215], a[36:39], v[144:147]\n"
"s_waitcnt lgkmcnt(10)\n"
"s_add_u32 m0, s60, 7936\n"
"v_cndmask_b32 v222, 0, v221, s[58:59]\n"
"v_mfma_f32_16x16x32_bf16 v[60:63], a[216:219], a[36:39], v[60:63]\n"
"s_waitcnt lgkmcnt(8)\n"
"buffer_load_dwordx4 v209, s[24:27], s63 offen offset:256 lds\n"
"v_mfma_f32_16x16x32_bf16 v[156:159], a[220:223], a[36:39], v[156:159]\n"
"v_accvgpr_write_b32 a242, v222\n"
"v_cmp_eq_u32 s[58:59], 3, v220\n"
"v_mfma_f32_16x16x32_bf16 v[160:163], a[220:223], a[40:43], v[160:163]\n"
"s_nop 0\n"
"v_cndmask_b32 v222, 0, v221, s[58:59]\n"
"v_accvgpr_write_b32 a243, v222\n"
"s_nop 7\n"
"v_mfma_f32_16x16x32_bf16 v[64:67], a[216:219], a[40:43], v[64:67]\n"
"v_cmp_eq_u32 s[58:59], -4, v220\n"
"s_nop 1\n"
"v_cndmask_b32 v222, 0, v221, s[58:59]\n"
"v_mfma_f32_16x16x32_bf16 v[148:151], a[212:215], a[40:43], v[148:151]\n"
"s_waitcnt vmcnt(20)\n"
"s_barrier\n"
"v_accvgpr_write_b32 a244, v222\n"
"v_mfma_f32_16x16x32_bf16 v[52:55], a[208:211], a[40:43], v[52:55]\n"
"s_add_u32 m0, s60, 11904\n"
"v_cmp_eq_u32 s[58:59], -3, v220\n"
"s_nop 1\n"
"v_cndmask_b32 v222, 0, v221, s[58:59]\n"
"v_mfma_f32_16x16x32_bf16 v[56:59], a[208:211], a[44:47], v[56:59]\n"
"buffer_load_dwordx4 v209, s[24:27], s63 offen offset:384 lds\n"
"s_nop 7\n"
"v_mfma_f32_16x16x32_bf16 v[152:155], a[212:215], a[44:47], v[152:155]\n"
"ds_read_b64_tr_b16 a[208:209], v194 offset:0\n"
"ds_read_b64_tr_b16 a[210:211], v194 offset:2048\n"
"v_mfma_f32_16x16x32_bf16 v[68:71], a[216:219], a[44:47], v[68:71]\n"
"ds_read_b64_tr_b16 a[212:213], v195 offset:0\n"
"ds_read_b64_tr_b16 a[214:215], v195 offset:2048\n"
"v_mfma_f32_16x16x32_bf16 v[164:167], a[220:223], a[44:47], v[164:167]\n"
"s_waitcnt lgkmcnt(10)\n"
"s_add_u32 s63, s74, 0\n"
"ds_read_b64_tr_b16 a[216:217], v194 offset:512\n"
"v_mfma_f32_16x16x32_bf16 v[80:83], a[224:227], a[44:47], v[80:83]\n"
"s_waitcnt lgkmcnt(9)\n"
"s_add_u32 m0, s61, 0\n"
"ds_read_b64_tr_b16 a[218:219], v194 offset:2560\n"
"s_nop 7\n"
"v_mfma_f32_16x16x32_bf16 v[176:179], a[228:231], a[44:47], v[176:179]\n"
"s_waitcnt lgkmcnt(8)\n"
"buffer_load_dwordx4 v210, s[20:23], s63 offen lds\n"
"v_mfma_f32_16x16x32_bf16 v[92:95], a[232:235], a[44:47], v[92:95]\n"
"s_waitcnt lgkmcnt(6)\n"
"ds_read_b64_tr_b16 a[220:221], v195 offset:512\n"
"ds_read_b64_tr_b16 a[222:223], v195 offset:2560\n"
"v_mfma_f32_16x16x32_bf16 v[188:191], a[236:239], a[44:47], v[188:191]\n"
"v_accvgpr_write_b32 a245, v222\n"
"v_cmp_eq_u32 s[58:59], -2, v220\n"
"v_mfma_f32_16x16x32_bf16 v[184:187], a[236:239], a[40:43], v[184:187]\n"
"s_add_u32 s63, s74, 7168\n"
"v_cndmask_b32 v222, 0, v221, s[58:59]\n"
"v_accvgpr_write_b32 a246, v222\n"
"s_nop 7\n"
"v_mfma_f32_16x16x32_bf16 v[88:91], a[232:235], a[40:43], v[88:91]\n"
"s_add_u32 m0, s61, 1024\n"
"v_cmp_eq_u32 s[58:59], -1, v220\n"
"s_nop 1\n"
"v_cndmask_b32 v222, 0, v221, s[58:59]\n"
"v_mfma_f32_16x16x32_bf16 v[172:175], a[228:231], a[40:43], v[172:175]\n"
"buffer_load_dwordx4 v211, s[20:23], s63 offen lds\n"
"v_mfma_f32_16x16x32_bf16 v[76:79], a[224:227], a[40:43], v[76:79]\n"
"v_accvgpr_write_b32 a247, v222\n"
"v_mfma_f32_16x16x32_bf16 v[72:75], a[224:227], a[36:39], v[72:75]\n"
"ds_read_b64_tr_b16 a[224:225], v194 offset:4096\n"
"ds_read_b64_tr_b16 a[226:227], v194 offset:6144\n"
"s_nop 7\n"
"v_mfma_f32_16x16x32_bf16 v[168:171], a[228:231], a[36:39], v[168:171]\n"
"ds_read_b64_tr_b16 a[228:229], v195 offset:4096\n"
"ds_read_b64_tr_b16 a[230:231], v195 offset:6144\n"
"v_mfma_f32_16x16x32_bf16 v[84:87], a[232:235], a[36:39], v[84:87]\n"
"s_add_u32 s63, s74, 14336\n"
"ds_read_b64_tr_b16 a[232:233], v194 offset:4608\n"
"ds_read_b64_tr_b16 a[234:235], v194 offset:6656\n"
"v_mfma_f32_16x16x32_bf16 v[180:183], a[236:239], a[36:39], v[180:183]\n"
"s_waitcnt lgkmcnt(12)\n"
"s_add_u32 m0, s61, 2048\n"
"ds_read_b64_tr_b16 a[236:237], v195 offset:4608\n"
"v_mfma_f32_16x16x32_bf16 v[0:3], a[208:211], a[48:51], v[0:3]\n"
"s_waitcnt lgkmcnt(11)\n"
"buffer_load_dwordx4 v210, s[20:23], s63 offen lds\n"
"v_mfma_f32_16x16x32_bf16 v[96:99], a[212:215], a[48:51], v[96:99]\n"
"s_waitcnt lgkmcnt(9)\n"
"ds_read_b64_tr_b16 a[238:239], v195 offset:6656\n"
"v_mfma_f32_16x16x32_bf16 v[12:15], a[216:219], a[48:51], v[12:15]\n"
"s_waitcnt lgkmcnt(8)\n"
"v_mfma_f32_16x16x32_bf16 v[108:111], a[220:223], a[48:51], v[108:111]\n"
"s_nop 7\n"
"v_mfma_f32_16x16x32_bf16 v[112:115], a[220:223], a[52:55], v[112:115]\n"
"s_add_u32 s63, s74, 21504\n"
"v_mfma_f32_16x16x32_bf16 v[16:19], a[216:219], a[52:55], v[16:19]\n"
"s_add_u32 m0, s61, 3072\n"
"v_mfma_f32_16x16x32_bf16 v[100:103], a[212:215], a[52:55], v[100:103]\n"
"buffer_load_dwordx4 v211, s[20:23], s63 offen lds\n"
"v_mfma_f32_16x16x32_bf16 v[4:7], a[208:211], a[52:55], v[4:7]\n"
"v_mfma_f32_16x16x32_bf16 v[8:11], a[208:211], a[56:59], v[8:11]\n"
"ds_read_b64_tr_b16 a[208:209], v194 offset:8192\n"
"ds_read_b64_tr_b16 a[210:211], v194 offset:10240\n"
"v_mfma_f32_16x16x32_bf16 v[104:107], a[212:215], a[56:59], v[104:107]\n"
"ds_read_b64_tr_b16 a[212:213], v195 offset:8192\n"
"ds_read_b64_tr_b16 a[214:215], v195 offset:10240\n"
"v_mfma_f32_16x16x32_bf16 v[20:23], a[216:219], a[56:59], v[20:23]\n"
"ds_read_b64_tr_b16 a[216:217], v194 offset:8704\n"
"ds_read_b64_tr_b16 a[218:219], v194 offset:10752\n"
"s_nop 7\n"
"v_mfma_f32_16x16x32_bf16 v[116:119], a[220:223], a[56:59], v[116:119]\n"
"s_waitcnt lgkmcnt(12)\n"
"s_add_u32 s63, s74, 28672\n"
"ds_read_b64_tr_b16 a[220:221], v195 offset:8704\n"
"v_mfma_f32_16x16x32_bf16 v[32:35], a[224:227], a[56:59], v[32:35]\n"
"s_waitcnt lgkmcnt(11)\n"
"s_add_u32 m0, s61, 4096\n"
"ds_read_b64_tr_b16 a[222:223], v195 offset:10752\n"
"v_mfma_f32_16x16x32_bf16 v[128:131], a[228:231], a[56:59], v[128:131]\n"
"s_waitcnt lgkmcnt(10)\n"
"buffer_load_dwordx4 v210, s[20:23], s63 offen lds\n"
"v_mfma_f32_16x16x32_bf16 v[44:47], a[232:235], a[56:59], v[44:47]\n"
"s_waitcnt lgkmcnt(8)\n"
"v_mfma_f32_16x16x32_bf16 v[140:143], a[236:239], a[56:59], v[140:143]\n"
"v_mfma_f32_16x16x32_bf16 v[136:139], a[236:239], a[52:55], v[136:139]\n"
"v_mfma_f32_16x16x32_bf16 v[40:43], a[232:235], a[52:55], v[40:43]\n"
"s_nop 7\n"
"v_mfma_f32_16x16x32_bf16 v[124:127], a[228:231], a[52:55], v[124:127]\n"
"s_add_u32 s63, s74, 35840\n"
"v_mfma_f32_16x16x32_bf16 v[28:31], a[224:227], a[52:55], v[28:31]\n"
"s_add_u32 m0, s61, 5120\n"
"v_mfma_f32_16x16x32_bf16 v[24:27], a[224:227], a[48:51], v[24:27]\n"
"buffer_load_dwordx4 v211, s[20:23], s63 offen lds\n"
"v_mfma_f32_16x16x32_bf16 v[120:123], a[228:231], a[48:51], v[120:123]\n"
"ds_read_b64_tr_b16 a[224:225], v194 offset:12288\n"
"ds_read_b64_tr_b16 a[226:227], v194 offset:14336\n"
"v_mfma_f32_16x16x32_bf16 v[36:39], a[232:235], a[48:51], v[36:39]\n"
"ds_read_b64_tr_b16 a[228:229], v195 offset:12288\n"
"ds_read_b64_tr_b16 a[230:231], v195 offset:14336\n"
"v_mfma_f32_16x16x32_bf16 v[132:135], a[236:239], a[48:51], v[132:135]\n"
"s_waitcnt lgkmcnt(10)\n"
"ds_read_b64_tr_b16 a[232:233], v194 offset:12800\n"
"ds_read_b64_tr_b16 a[234:235], v194 offset:14848\n"
"v_mfma_f32_16x16x32_bf16 v[48:51], a[208:211], a[48:51], v[48:51]\n"
"s_waitcnt lgkmcnt(10)\n"
"ds_read_b64_tr_b16 a[236:237], v195 offset:12800\n"
"ds_read_b64_tr_b16 a[238:239], v195 offset:14848\n"
"s_nop 7\n"
"v_mfma_f32_16x16x32_bf16 v[144:147], a[212:215], a[48:51], v[144:147]\n"
"s_waitcnt lgkmcnt(10)\n"
"s_lshl_b32 s63, s74, 12\n"
"v_mfma_f32_16x16x32_bf16 v[60:63], a[216:219], a[48:51], v[60:63]\n"
"s_waitcnt lgkmcnt(8)\n"
"s_add_u32 m0, s60, 16384\n"
"v_mfma_f32_16x16x32_bf16 v[156:159], a[220:223], a[48:51], v[156:159]\n"
"buffer_load_dwordx4 v209, s[24:27], s63 offen lds\n"
"v_mfma_f32_16x16x32_bf16 v[160:163], a[220:223], a[52:55], v[160:163]\n"
"v_mfma_f32_16x16x32_bf16 v[64:67], a[216:219], a[52:55], v[64:67]\n"
"v_mfma_f32_16x16x32_bf16 v[148:151], a[212:215], a[52:55], v[148:151]\n"
"s_waitcnt vmcnt(24)\n"
"s_barrier\n"
"v_mfma_f32_16x16x32_bf16 v[52:55], a[208:211], a[52:55], v[52:55]\n"
"s_nop 7\n"
"v_mfma_f32_16x16x32_bf16 v[56:59], a[208:211], a[56:59], v[56:59]\n"
"ds_read_b64_tr_b16 a[208:209], v194 offset:16384\n"
"ds_read_b64_tr_b16 a[210:211], v194 offset:18432\n"
"v_mfma_f32_16x16x32_bf16 v[152:155], a[212:215], a[56:59], v[152:155]\n"
"s_add_u32 m0, s60, 20352\n"
"ds_read_b64_tr_b16 a[212:213], v195 offset:16384\n"
"ds_read_b64_tr_b16 a[214:215], v195 offset:18432\n"
"v_mfma_f32_16x16x32_bf16 v[68:71], a[216:219], a[56:59], v[68:71]\n"
"buffer_load_dwordx4 v209, s[24:27], s63 offen offset:128 lds\n"
"v_mfma_f32_16x16x32_bf16 v[164:167], a[220:223], a[56:59], v[164:167]\n"
"s_waitcnt lgkmcnt(10)\n"
"ds_read_b64_tr_b16 a[216:217], v194 offset:16896\n"
"ds_read_b64_tr_b16 a[218:219], v194 offset:18944\n"
"v_mfma_f32_16x16x32_bf16 v[80:83], a[224:227], a[56:59], v[80:83]\n"
"s_waitcnt lgkmcnt(10)\n"
"ds_read_b64_tr_b16 a[220:221], v195 offset:16896\n"
"ds_read_b64_tr_b16 a[222:223], v195 offset:18944\n"
"v_mfma_f32_16x16x32_bf16 v[176:179], a[228:231], a[56:59], v[176:179]\n"
"s_waitcnt lgkmcnt(10)\n"
"v_mfma_f32_16x16x32_bf16 v[92:95], a[232:235], a[56:59], v[92:95]\n"
"s_waitcnt lgkmcnt(8)\n"
"s_nop 7\n"
"v_mfma_f32_16x16x32_bf16 v[188:191], a[236:239], a[56:59], v[188:191]\n"
"v_mfma_f32_16x16x32_bf16 v[184:187], a[236:239], a[52:55], v[184:187]\n"
"s_add_u32 m0, s60, 24320\n"
"v_mfma_f32_16x16x32_bf16 v[88:91], a[232:235], a[52:55], v[88:91]\n"
"buffer_load_dwordx4 v209, s[24:27], s63 offen offset:256 lds\n"
"v_mfma_f32_16x16x32_bf16 v[172:175], a[228:231], a[52:55], v[172:175]\n"
"v_mfma_f32_16x16x32_bf16 v[76:79], a[224:227], a[52:55], v[76:79]\n"
"v_mfma_f32_16x16x32_bf16 v[72:75], a[224:227], a[48:51], v[72:75]\n"
"ds_read_b64_tr_b16 a[224:225], v194 offset:20480\n"
"ds_read_b64_tr_b16 a[226:227], v194 offset:22528\n"
"v_mfma_f32_16x16x32_bf16 v[168:171], a[228:231], a[48:51], v[168:171]\n"
"ds_read_b64_tr_b16 a[228:229], v195 offset:20480\n"
"ds_read_b64_tr_b16 a[230:231], v195 offset:22528\n"
"s_nop 7\n"
"v_mfma_f32_16x16x32_bf16 v[84:87], a[232:235], a[48:51], v[84:87]\n"
"ds_read_b64_tr_b16 a[232:233], v194 offset:20992\n"
"ds_read_b64_tr_b16 a[234:235], v194 offset:23040\n"
"v_mfma_f32_16x16x32_bf16 v[180:183], a[236:239], a[48:51], v[180:183]\n"
"s_waitcnt lgkmcnt(12)\n"
"s_add_u32 m0, s60, 28288\n"
"ds_read_b64_tr_b16 a[236:237], v195 offset:20992\n"
"v_mfma_f32_16x16x32_bf16 v[0:3], a[208:211], a[60:63], v[0:3]\n"
"s_waitcnt lgkmcnt(11)\n"
"buffer_load_dwordx4 v209, s[24:27], s63 offen offset:384 lds\n"
"v_mfma_f32_16x16x32_bf16 v[96:99], a[212:215], a[60:63], v[96:99]\n"
"s_waitcnt lgkmcnt(9)\n"
"ds_read_b64_tr_b16 a[238:239], v195 offset:23040\n"
"v_mfma_f32_16x16x32_bf16 v[12:15], a[216:219], a[60:63], v[12:15]\n"
"s_waitcnt lgkmcnt(8)\n"
"s_lshl_b32 s63, s74, 12\n"
"s_add_u32 s63, s63, 0x40000\n"
"v_mfma_f32_16x16x32_bf16 v[108:111], a[220:223], a[60:63], v[108:111]\n"
"s_add_u32 m0, s60, 32768\n"
"v_mfma_f32_16x16x32_bf16 v[112:115], a[220:223], a[64:67], v[112:115]\n"
"buffer_load_dwordx4 v209, s[24:27], s63 offen lds\n"
"s_nop 7\n"
"v_mfma_f32_16x16x32_bf16 v[16:19], a[216:219], a[64:67], v[16:19]\n"
"s_waitcnt vmcnt(19)\n"
"ds_read_b128 a[72:75], v208 offset:6144\n"
"ds_read_b128 a[76:79], v208 offset:8192\n"
"v_mfma_f32_16x16x32_bf16 v[100:103], a[212:215], a[64:67], v[100:103]\n"
"ds_read_b128 a[80:83], v208 offset:10240\n"
"ds_read_b128 a[84:87], v208 offset:6656\n"
"v_mfma_f32_16x16x32_bf16 v[4:7], a[208:211], a[64:67], v[4:7]\n"
"ds_read_b128 a[88:91], v208 offset:8704\n"
"ds_read_b128 a[92:95], v208 offset:10752\n"
"v_mfma_f32_16x16x32_bf16 v[8:11], a[208:211], a[68:71], v[8:11]\n"
"s_add_u32 m0, s60, 36736\n"
"ds_read_b64_tr_b16 a[208:209], v194 offset:24576\n"
"s_waitcnt lgkmcnt(14)\n"
"ds_read_b64_tr_b16 a[210:211], v194 offset:26624\n"
"v_mfma_f32_16x16x32_bf16 v[104:107], a[212:215], a[68:71], v[104:107]\n"
"buffer_load_dwordx4 v209, s[24:27], s63 offen offset:128 lds\n"
"v_mfma_f32_16x16x32_bf16 v[20:23], a[216:219], a[68:71], v[20:23]\n"
"s_waitcnt lgkmcnt(14)\n"
"ds_read_b64_tr_b16 a[212:213], v195 offset:24576\n"
"s_waitcnt lgkmcnt(14)\n"
"ds_read_b64_tr_b16 a[214:215], v195 offset:26624\n"
"v_mfma_f32_16x16x32_bf16 v[116:119], a[220:223], a[68:71], v[116:119]\n"
"s_waitcnt lgkmcnt(14)\n"
"ds_read_b64_tr_b16 a[216:217], v194 offset:25088\n"
"s_waitcnt lgkmcnt(14)\n"
"ds_read_b64_tr_b16 a[218:219], v194 offset:27136\n"
"s_nop 7\n"
"v_mfma_f32_16x16x32_bf16 v[32:35], a[224:227], a[68:71], v[32:35]\n"
"s_waitcnt lgkmcnt(14)\n"
"ds_read_b64_tr_b16 a[220:221], v195 offset:25088\n"
"s_waitcnt lgkmcnt(14)\n"
"ds_read_b64_tr_b16 a[222:223], v195 offset:27136\n"
"v_mfma_f32_16x16x32_bf16 v[128:131], a[228:231], a[68:71], v[128:131]\n"
"s_add_u32 m0, s60, 40704\n"
"v_mfma_f32_16x16x32_bf16 v[44:47], a[232:235], a[68:71], v[44:47]\n"
"s_waitcnt lgkmcnt(14)\n"
"buffer_load_dwordx4 v209, s[24:27], s63 offen offset:256 lds\n"
"v_mfma_f32_16x16x32_bf16 v[140:143], a[236:239], a[68:71], v[140:143]\n"
"s_waitcnt lgkmcnt(8)\n"
"v_mfma_f32_16x16x32_bf16 v[136:139], a[236:239], a[64:67], v[136:139]\n"
"v_mfma_f32_16x16x32_bf16 v[40:43], a[232:235], a[64:67], v[40:43]\n"
"v_mfma_f32_16x16x32_bf16 v[124:127], a[228:231], a[64:67], v[124:127]\n"
"s_add_u32 m0, s60, 44672\n"
"s_nop 7\n"
"v_mfma_f32_16x16x32_bf16 v[28:31], a[224:227], a[64:67], v[28:31]\n"
"buffer_load_dwordx4 v209, s[24:27], s63 offen offset:384 lds\n"
"v_mfma_f32_16x16x32_bf16 v[24:27], a[224:227], a[60:63], v[24:27]\n"
"ds_read_b64_tr_b16 a[224:225], v194 offset:28672\n"
"ds_read_b64_tr_b16 a[226:227], v194 offset:30720\n"
"v_mfma_f32_16x16x32_bf16 v[120:123], a[228:231], a[60:63], v[120:123]\n"
"ds_read_b64_tr_b16 a[228:229], v195 offset:28672\n"
"ds_read_b64_tr_b16 a[230:231], v195 offset:30720\n"
"v_mfma_f32_16x16x32_bf16 v[36:39], a[232:235], a[60:63], v[36:39]\n"
"s_add_u32 s63, s75, 0\n"
"ds_read_b64_tr_b16 a[232:233], v194 offset:29184\n"
"ds_read_b64_tr_b16 a[234:235], v194 offset:31232\n"
"v_mfma_f32_16x16x32_bf16 v[132:135], a[236:239], a[60:63], v[132:135]\n"
"s_waitcnt lgkmcnt(12)\n"
"s_add_u32 m0, s61, 6144\n"
"ds_read_b64_tr_b16 a[236:237], v195 offset:29184\n"
"v_mfma_f32_16x16x32_bf16 v[48:51], a[208:211], a[60:63], v[48:51]\n"
"s_waitcnt lgkmcnt(11)\n"
"buffer_load_dwordx4 v210, s[20:23], s63 offen lds\n"
"v_mfma_f32_16x16x32_bf16 v[144:147], a[212:215], a[60:63], v[144:147]\n"
"s_waitcnt lgkmcnt(9)\n"
"ds_read_b64_tr_b16 a[238:239], v195 offset:31232\n"
"s_nop 7\n"
"v_mfma_f32_16x16x32_bf16 v[60:63], a[216:219], a[60:63], v[60:63]\n"
"s_waitcnt lgkmcnt(8)\n"
"s_add_u32 s63, s75, 7168\n"
"v_mfma_f32_16x16x32_bf16 v[156:159], a[220:223], a[60:63], v[156:159]\n"
"s_add_u32 m0, s61, 7168\n"
"v_mfma_f32_16x16x32_bf16 v[160:163], a[220:223], a[64:67], v[160:163]\n"
"s_waitcnt vmcnt(9)\n"
"buffer_load_dwordx4 v211, s[20:23], s63 offen lds\n"
"v_mfma_f32_16x16x32_bf16 v[64:67], a[216:219], a[64:67], v[64:67]\n"
"ds_read_b128 a[96:99], v208 offset:0\n"
"ds_read_b128 a[100:103], v208 offset:2048\n"
"v_mfma_f32_16x16x32_bf16 v[148:151], a[212:215], a[64:67], v[148:151]\n"
"ds_read_b128 a[104:107], v208 offset:4096\n"
"ds_read_b128 a[108:111], v208 offset:512\n"
"v_mfma_f32_16x16x32_bf16 v[52:55], a[208:211], a[64:67], v[52:55]\n"
"s_add_u32 s63, s75, 14336\n"
"ds_read_b128 a[112:115], v208 offset:2560\n"
"ds_read_b128 a[116:119], v208 offset:4608\n"
"v_mfma_f32_16x16x32_bf16 v[56:59], a[208:211], a[68:71], v[56:59]\n"
"s_add_u32 m0, s61, 8192\n"
"s_barrier\n"
"ds_read_b64_tr_b16 a[208:209], v194 offset:32768\n"
"s_nop 7\n"
"v_mfma_f32_16x16x32_bf16 v[152:155], a[212:215], a[68:71], v[152:155]\n"
"buffer_load_dwordx4 v210, s[20:23], s63 offen lds\n"
"v_mfma_f32_16x16x32_bf16 v[68:71], a[216:219], a[68:71], v[68:71]\n"
"s_waitcnt lgkmcnt(14)\n"
"ds_read_b64_tr_b16 a[210:211], v194 offset:34816\n"
"s_waitcnt lgkmcnt(14)\n"
"ds_read_b64_tr_b16 a[212:213], v195 offset:32768\n"
"v_mfma_f32_16x16x32_bf16 v[164:167], a[220:223], a[68:71], v[164:167]\n"
"s_waitcnt lgkmcnt(14)\n"
"ds_read_b64_tr_b16 a[214:215], v195 offset:34816\n"
"s_waitcnt lgkmcnt(14)\n"
"ds_read_b64_tr_b16 a[216:217], v194 offset:33280\n"
"v_mfma_f32_16x16x32_bf16 v[80:83], a[224:227], a[68:71], v[80:83]\n"
"s_add_u32 s63, s75, 21504\n"
"s_waitcnt lgkmcnt(14)\n"
"ds_read_b64_tr_b16 a[218:219], v194 offset:35328\n"
"v_mfma_f32_16x16x32_bf16 v[176:179], a[228:231], a[68:71], v[176:179]\n"
"s_waitcnt lgkmcnt(14)\n"
"s_add_u32 m0, s61, 9216\n"
"ds_read_b64_tr_b16 a[220:221], v195 offset:33280\n"
"v_mfma_f32_16x16x32_bf16 v[92:95], a[232:235], a[68:71], v[92:95]\n"
"s_waitcnt lgkmcnt(13)\n"
"buffer_load_dwordx4 v211, s[20:23], s63 offen lds\n"
"v_mfma_f32_16x16x32_bf16 v[188:191], a[236:239], a[68:71], v[188:191]\n"
"s_waitcnt lgkmcnt(7)\n"
"ds_read_b64_tr_b16 a[222:223], v195 offset:35328\n"
"s_nop 7\n"
"v_mfma_f32_16x16x32_bf16 v[184:187], a[236:239], a[64:67], v[184:187]\n"
"v_mfma_f32_16x16x32_bf16 v[88:91], a[232:235], a[64:67], v[88:91]\n"
"s_add_u32 s63, s75, 28672\n"
"v_mfma_f32_16x16x32_bf16 v[172:175], a[228:231], a[64:67], v[172:175]\n"
"s_add_u32 m0, s61, 10240\n"
"v_mfma_f32_16x16x32_bf16 v[76:79], a[224:227], a[64:67], v[76:79]\n"
"buffer_load_dwordx4 v210, s[20:23], s63 offen lds\n"
"v_mfma_f32_16x16x32_bf16 v[72:75], a[224:227], a[60:63], v[72:75]\n"
"ds_read_b64_tr_b16 a[224:225], v194 offset:36864\n"
"ds_read_b64_tr_b16 a[226:227], v194 offset:38912\n"
"v_mfma_f32_16x16x32_bf16 v[168:171], a[228:231], a[60:63], v[168:171]\n"
"ds_read_b64_tr_b16 a[228:229], v195 offset:36864\n"
"ds_read_b64_tr_b16 a[230:231], v195 offset:38912\n"
"v_mfma_f32_16x16x32_bf16 v[84:87], a[232:235], a[60:63], v[84:87]\n"
"s_add_u32 s63, s75, 35840\n"
"ds_read_b64_tr_b16 a[232:233], v194 offset:37376\n"
"ds_read_b64_tr_b16 a[234:235], v194 offset:39424\n"
"s_nop 7\n"
"v_mfma_f32_16x16x32_bf16 v[180:183], a[236:239], a[60:63], v[180:183]\n"
"s_waitcnt lgkmcnt(12)\n"
"s_add_u32 m0, s61, 11264\n"
"ds_read_b64_tr_b16 a[236:237], v195 offset:37376\n"
"v_mfma_f32_16x16x32_bf16 v[0:3], a[208:211], a[72:75], v[0:3]\n"
"s_waitcnt lgkmcnt(11)\n"
"buffer_load_dwordx4 v211, s[20:23], s63 offen lds\n"
"v_mfma_f32_16x16x32_bf16 v[96:99], a[212:215], a[72:75], v[96:99]\n"
"s_waitcnt lgkmcnt(9)\n"
"s_lshl_b32 s63, s75, 12\n"
"ds_read_b64_tr_b16 a[238:239], v195 offset:39424\n"
"v_mfma_f32_16x16x32_bf16 v[12:15], a[216:219], a[72:75], v[12:15]\n"
"s_waitcnt lgkmcnt(8)\n"
"s_add_u32 m0, s60, 49152\n"
"v_mfma_f32_16x16x32_bf16 v[108:111], a[220:223], a[72:75], v[108:111]\n"
"buffer_load_dwordx4 v209, s[24:27], s63 offen lds\n"
"v_mfma_f32_16x16x32_bf16 v[112:115], a[220:223], a[76:79], v[112:115]\n"
"v_mfma_f32_16x16x32_bf16 v[16:19], a[216:219], a[76:79], v[16:19]\n"
"v_mfma_f32_16x16x32_bf16 v[100:103], a[212:215], a[76:79], v[100:103]\n"
"s_add_u32 m0, s60, 53120\n"
"v_mfma_f32_16x16x32_bf16 v[4:7], a[208:211], a[76:79], v[4:7]\n"
"buffer_load_dwordx4 v209, s[24:27], s63 offen offset:128 lds\n"
"v_mfma_f32_16x16x32_bf16 v[8:11], a[208:211], a[80:83], v[8:11]\n"
"ds_read_b64_tr_b16 a[208:209], v194 offset:40960\n"
"ds_read_b64_tr_b16 a[210:211], v194 offset:43008\n"
"v_mfma_f32_16x16x32_bf16 v[104:107], a[212:215], a[80:83], v[104:107]\n"
"ds_read_b64_tr_b16 a[212:213], v195 offset:40960\n"
"ds_read_b64_tr_b16 a[214:215], v195 offset:43008\n"
"v_mfma_f32_16x16x32_bf16 v[20:23], a[216:219], a[80:83], v[20:23]\n"
"s_add_u32 m0, s60, 57088\n"
"ds_read_b64_tr_b16 a[216:217], v194 offset:41472\n"
"ds_read_b64_tr_b16 a[218:219], v194 offset:43520\n"
"v_mfma_f32_16x16x32_bf16 v[116:119], a[220:223], a[80:83], v[116:119]\n"
"s_waitcnt lgkmcnt(12)\n"
"buffer_load_dwordx4 v209, s[24:27], s63 offen offset:256 lds\n"
"v_mfma_f32_16x16x32_bf16 v[32:35], a[224:227], a[80:83], v[32:35]\n"
"s_waitcnt lgkmcnt(10)\n"
"ds_read_b64_tr_b16 a[220:221], v195 offset:41472\n"
"ds_read_b64_tr_b16 a[222:223], v195 offset:43520\n"
"s_nop 7\n"
"v_mfma_f32_16x16x32_bf16 v[128:131], a[228:231], a[80:83], v[128:131]\n"
"s_waitcnt lgkmcnt(10)\n"
"s_add_u32 m0, s60, 61056\n"
"v_mfma_f32_16x16x32_bf16 v[44:47], a[232:235], a[80:83], v[44:47]\n"
"s_waitcnt lgkmcnt(8)\n"
"buffer_load_dwordx4 v209, s[24:27], s63 offen offset:384 lds\n"
"v_mfma_f32_16x16x32_bf16 v[140:143], a[236:239], a[80:83], v[140:143]\n"
"v_mfma_f32_16x16x32_bf16 v[136:139], a[236:239], a[76:79], v[136:139]\n"
"s_lshl_b32 s63, s75, 12\n"
"s_add_u32 s63, s63, 0x40000\n"
"v_mfma_f32_16x16x32_bf16 v[40:43], a[232:235], a[76:79], v[40:43]\n"
"s_add_u32 m0, s60, 65536\n"
"v_mfma_f32_16x16x32_bf16 v[124:127], a[228:231], a[76:79], v[124:127]\n"
"buffer_load_dwordx4 v209, s[24:27], s63 offen lds\n"
"v_mfma_f32_16x16x32_bf16 v[28:31], a[224:227], a[76:79], v[28:31]\n"
"v_mfma_f32_16x16x32_bf16 v[24:27], a[224:227], a[72:75], v[24:27]\n"
"ds_read_b64_tr_b16 a[224:225], v194 offset:45056\n"
"ds_read_b64_tr_b16 a[226:227], v194 offset:47104\n"
"v_mfma_f32_16x16x32_bf16 v[120:123], a[228:231], a[72:75], v[120:123]\n"
"s_add_u32 m0, s60, 69504\n"
"ds_read_b64_tr_b16 a[228:229], v195 offset:45056\n"
"ds_read_b64_tr_b16 a[230:231], v195 offset:47104\n"
"v_mfma_f32_16x16x32_bf16 v[36:39], a[232:235], a[72:75], v[36:39]\n"
"buffer_load_dwordx4 v209, s[24:27], s63 offen offset:128 lds\n"
"v_mfma_f32_16x16x32_bf16 v[132:135], a[236:239], a[72:75], v[132:135]\n"
"s_waitcnt lgkmcnt(10)\n"
"ds_read_b64_tr_b16 a[232:233], v194 offset:45568\n"
"ds_read_b64_tr_b16 a[234:235], v194 offset:47616\n"
"v_mfma_f32_16x16x32_bf16 v[48:51], a[208:211], a[72:75], v[48:51]\n"
"s_waitcnt lgkmcnt(10)\n"
"s_add_u32 m0, s60, 73472\n"
"s_mov_b32 s66, 0\n"
"v_mfma_f32_16x16x32_bf16 v[144:147], a[212:215], a[72:75], v[144:147]\n"
"s_waitcnt lgkmcnt(8)\n"
"buffer_load_dwordx4 v209, s[24:27], s63 offen offset:256 lds\n"
"v_mfma_f32_16x16x32_bf16 v[60:63], a[216:219], a[72:75], v[60:63]\n"
"s_waitcnt lgkmcnt(6)\n"
"buffer_load_dword a248, v255, s[40:43], 0 offen offset:128 sc1\n"
"s_nop 7\n"
"v_mfma_f32_16x16x32_bf16 v[156:159], a[220:223], a[72:75], v[156:159]\n"
"s_bfe_u32 s70, %2, 0x10000\n"
"s_lshl_b32 s70, s70, 13\n"
"s_xor_b32 s69, s69, s70\n"
"ds_read_b64_tr_b16 a[236:237], v195 offset:45568\n"
"v_mfma_f32_16x16x32_bf16 v[160:163], a[220:223], a[76:79], v[160:163]\n"
"s_add_u32 m0, s60, 77440\n"
"ds_read_b64_tr_b16 a[238:239], v195 offset:47616\n"
"v_mfma_f32_16x16x32_bf16 v[64:67], a[216:219], a[76:79], v[64:67]\n"
"buffer_load_dwordx4 v209, s[24:27], s63 offen offset:384 lds\n"
"v_mfma_f32_16x16x32_bf16 v[148:151], a[212:215], a[76:79], v[148:151]\n"
"s_barrier\n"
"v_mfma_f32_16x16x32_bf16 v[52:55], a[208:211], a[76:79], v[52:55]\n"
"s_add_u32 s63, s76, 0\n"
"v_mfma_f32_16x16x32_bf16 v[56:59], a[208:211], a[80:83], v[56:59]\n"
"s_add_u32 m0, s61, 0\n"
"ds_read_b64_tr_b16 a[208:209], v192 offset:0\n"
"ds_read_b64_tr_b16 a[210:211], v192 offset:2048\n"
"v_mfma_f32_16x16x32_bf16 v[152:155], a[212:215], a[80:83], v[152:155]\n"
"buffer_load_dwordx4 v210, s[20:23], s63 offen lds\n"
"v_mfma_f32_16x16x32_bf16 v[68:71], a[216:219], a[80:83], v[68:71]\n"
"s_add_u32 s63, s76, 7168\n"
"ds_read_b64_tr_b16 a[212:213], v193 offset:0\n"
"ds_read_b64_tr_b16 a[214:215], v193 offset:2048\n"
"v_mfma_f32_16x16x32_bf16 v[164:167], a[220:223], a[80:83], v[164:167]\n"
"s_waitcnt lgkmcnt(10)\n"
"s_add_u32 m0, s61, 1024\n"
"ds_read_b64_tr_b16 a[216:217], v192 offset:512\n"
"v_mfma_f32_16x16x32_bf16 v[80:83], a[224:227], a[80:83], v[80:83]\n"
"s_waitcnt lgkmcnt(9)\n"
"buffer_load_dwordx4 v211, s[20:23], s63 offen lds\n"
"v_mfma_f32_16x16x32_bf16 v[176:179], a[228:231], a[80:83], v[176:179]\n"
"s_waitcnt lgkmcnt(7)\n"
"ds_read_b64_tr_b16 a[218:219], v192 offset:2560\n"
"ds_read_b64_tr_b16 a[220:221], v193 offset:512\n"
"v_mfma_f32_16x16x32_bf16 v[92:95], a[232:235], a[80:83], v[92:95]\n"
"s_waitcnt lgkmcnt(7)\n"
"s_add_u32 s63, s76, 14336\n"
"ds_read_b64_tr_b16 a[222:223], v193 offset:2560\n"
"v_mfma_f32_16x16x32_bf16 v[188:191], a[236:239], a[80:83], v[188:191]\n"
"s_add_u32 m0, s61, 2048\n"
"v_mfma_f32_16x16x32_bf16 v[184:187], a[236:239], a[76:79], v[184:187]\n"
"buffer_load_dwordx4 v210, s[20:23], s63 offen lds\n"
"s_nop 7\n"
"v_mfma_f32_16x16x32_bf16 v[88:91], a[232:235], a[76:79], v[88:91]\n"
"v_mfma_f32_16x16x32_bf16 v[172:175], a[228:231], a[76:79], v[172:175]\n"
"s_add_u32 s63, s76, 21504\n"
"v_mfma_f32_16x16x32_bf16 v[76:79], a[224:227], a[76:79], v[76:79]\n"
"s_add_u32 m0, s61, 3072\n"
"v_mfma_f32_16x16x32_bf16 v[72:75], a[224:227], a[72:75], v[72:75]\n"
"buffer_load_dwordx4 v211, s[20:23], s63 offen lds\n"
"v_mfma_f32_16x16x32_bf16 v[168:171], a[228:231], a[72:75], v[168:171]\n"
"ds_read_b64_tr_b16 a[224:225], v192 offset:4096\n"
"ds_read_b64_tr_b16 a[226:227], v192 offset:6144\n"
"v_mfma_f32_16x16x32_bf16 v[84:87], a[232:235], a[72:75], v[84:87]\n"
"s_add_u32 s63, s76, 28672\n"
"ds_read_b64_tr_b16 a[228:229], v193 offset:4096\n"
"ds_read_b64_tr_b16 a[230:231], v193 offset:6144\n"
"v_mfma_f32_16x16x32_bf16 v[180:183], a[236:239], a[72:75], v[180:183]\n"
"s_waitcnt lgkmcnt(10)\n"
"s_add_u32 m0, s61, 4096\n"
"ds_read_b64_tr_b16 a[232:233], v192 offset:4608\n"
"v_mfma_f32_16x16x32_bf16 v[0:3], a[208:211], a[84:87], v[0:3]\n"
"s_waitcnt lgkmcnt(9)\n"
"buffer_load_dwordx4 v210, s[20:23], s63 offen lds\n"
"v_mfma_f32_16x16x32_bf16 v[96:99], a[212:215], a[84:87], v[96:99]\n"
"s_waitcnt lgkmcnt(7)\n"
"s_add_u32 s63, s76, 35840\n"
"ds_read_b64_tr_b16 a[234:235], v192 offset:6656\n"
"v_mfma_f32_16x16x32_bf16 v[12:15], a[216:219], a[84:87], v[12:15]\n"
"s_waitcnt lgkmcnt(6)\n"
"s_add_u32 m0, s61, 5120\n"
"ds_read_b64_tr_b16 a[236:237], v193 offset:4608\n"
"v_mfma_f32_16x16x32_bf16 v[108:111], a[220:223], a[84:87], v[108:111]\n"
"buffer_load_dwordx4 v211, s[20:23], s63 offen lds\n"
"v_mfma_f32_16x16x32_bf16 v[112:115], a[220:223], a[88:91], v[112:115]\n"
"ds_read_b64_tr_b16 a[238:239], v193 offset:6656\n"
"v_mfma_f32_16x16x32_bf16 v[16:19], a[216:219], a[88:91], v[16:19]\n"
"s_lshl_b32 s63, s76, 12\n"
"v_mfma_f32_16x16x32_bf16 v[100:103], a[212:215], a[88:91], v[100:103]\n"
"s_add_u32 m0, s60, 81920\n"
"s_nop 7\n"
"v_mfma_f32_16x16x32_bf16 v[4:7], a[208:211], a[88:91], v[4:7]\n"
"buffer_load_dwordx4 v209, s[24:27], s63 offen lds\n"
"v_mfma_f32_16x16x32_bf16 v[8:11], a[208:211], a[92:95], v[8:11]\n"
"s_waitcnt vmcnt(16)\n"
"ds_read_b128 a[120:123], v208 offset:6144\n"
"ds_read_b128 a[124:127], v208 offset:8192\n"
"v_mfma_f32_16x16x32_bf16 v[104:107], a[212:215], a[92:95], v[104:107]\n"
"ds_read_b128 a[128:131], v208 offset:10240\n"
"ds_read_b128 a[132:135], v208 offset:6656\n"
"v_mfma_f32_16x16x32_bf16 v[20:23], a[216:219], a[92:95], v[20:23]\n"
"s_add_u32 m0, s60, 85888\n"
"ds_read_b128 a[136:139], v208 offset:8704\n"
"ds_read_b128 a[140:143], v208 offset:10752\n"
"v_mfma_f32_16x16x32_bf16 v[116:119], a[220:223], a[92:95], v[116:119]\n"
"s_waitcnt lgkmcnt(12)\n"
"buffer_load_dwordx4 v209, s[24:27], s63 offen offset:128 lds\n"
"v_mfma_f32_16x16x32_bf16 v[32:35], a[224:227], a[92:95], v[32:35]\n"
"s_waitcnt lgkmcnt(10)\n"
"ds_read_b64_tr_b16 a[208:209], v192 offset:8192\n"
"ds_read_b64_tr_b16 a[210:211], v192 offset:10240\n"
"v_mfma_f32_16x16x32_bf16 v[128:131], a[228:231], a[92:95], v[128:131]\n"
"s_waitcnt lgkmcnt(10)\n"
"s_add_u32 m0, s60, 89856\n"
"ds_read_b64_tr_b16 a[212:213], v193 offset:8192\n"
"v_mfma_f32_16x16x32_bf16 v[44:47], a[232:235], a[92:95], v[44:47]\n"
"s_waitcnt lgkmcnt(9)\n"
"buffer_load_dwordx4 v209, s[24:27], s63 offen offset:256 lds\n"
"v_mfma_f32_16x16x32_bf16 v[140:143], a[236:239], a[92:95], v[140:143]\n"
"ds_read_b64_tr_b16 a[214:215], v193 offset:10240\n"
"ds_read_b64_tr_b16 a[216:217], v192 offset:8704\n"
"v_mfma_f32_16x16x32_bf16 v[136:139], a[236:239], a[88:91], v[136:139]\n"
"ds_read_b64_tr_b16 a[218:219], v192 offset:10752\n"
"ds_read_b64_tr_b16 a[220:221], v193 offset:8704\n"
"v_mfma_f32_16x16x32_bf16 v[40:43], a[232:235], a[88:91], v[40:43]\n"
"s_add_u32 m0, s60, 93824\n"
"ds_read_b64_tr_b16 a[222:223], v193 offset:10752\n"
"v_mfma_f32_16x16x32_bf16 v[124:127], a[228:231], a[88:91], v[124:127]\n"
"s_waitcnt lgkmcnt(8)\n"
"buffer_load_dwordx4 v209, s[24:27], s63 offen offset:384 lds\n"
"v_mfma_f32_16x16x32_bf16 v[28:31], a[224:227], a[88:91], v[28:31]\n"
"v_mfma_f32_16x16x32_bf16 v[24:27], a[224:227], a[84:87], v[24:27]\n"
"s_lshl_b32 s63, s76, 12\n"
"s_add_u32 s63, s63, 0x40000\n"
"ds_read_b64_tr_b16 a[224:225], v192 offset:12288\n"
"s_nop 7\n"
"v_mfma_f32_16x16x32_bf16 v[120:123], a[228:231], a[84:87], v[120:123]\n"
"s_add_u32 m0, s60, 98304\n"
"ds_read_b64_tr_b16 a[226:227], v192 offset:14336\n"
"ds_read_b64_tr_b16 a[228:229], v193 offset:12288\n"
"v_mfma_f32_16x16x32_bf16 v[36:39], a[232:235], a[84:87], v[36:39]\n"
"ds_read_b64_tr_b16 a[230:231], v193 offset:14336\n"
"ds_read_b64_tr_b16 a[232:233], v192 offset:12800\n"
"v_mfma_f32_16x16x32_bf16 v[132:135], a[236:239], a[84:87], v[132:135]\n"
"s_waitcnt lgkmcnt(11)\n"
"ds_read_b64_tr_b16 a[234:235], v192 offset:14848\n"
"ds_read_b64_tr_b16 a[236:237], v193 offset:12800\n"
"v_mfma_f32_16x16x32_bf16 v[48:51], a[208:211], a[84:87], v[48:51]\n"
"s_waitcnt lgkmcnt(11)\n"
"ds_read_b64_tr_b16 a[238:239], v193 offset:14848\n"
"v_mfma_f32_16x16x32_bf16 v[144:147], a[212:215], a[84:87], v[144:147]\n"
"s_waitcnt lgkmcnt(10)\n"
"v_mfma_f32_16x16x32_bf16 v[60:63], a[216:219], a[84:87], v[60:63]\n"
"s_waitcnt lgkmcnt(8)\n"
"v_mfma_f32_16x16x32_bf16 v[156:159], a[220:223], a[84:87], v[156:159]\n"
"v_mfma_f32_16x16x32_bf16 v[160:163], a[220:223], a[88:91], v[160:163]\n"
"v_mfma_f32_16x16x32_bf16 v[64:67], a[216:219], a[88:91], v[64:67]\n"
"v_mfma_f32_16x16x32_bf16 v[148:151], a[212:215], a[88:91], v[148:151]\n"
"s_barrier\n"
"v_mfma_f32_16x16x32_bf16 v[52:55], a[208:211], a[88:91], v[52:55]\n"
"buffer_load_dwordx4 v209, s[24:27], s63 offen lds\n"
"v_mfma_f32_16x16x32_bf16 v[56:59], a[208:211], a[92:95], v[56:59]\n"
"s_add_u32 m0, s60, 102272\n"
"ds_read_b64_tr_b16 a[208:209], v192 offset:16384\n"
"ds_read_b64_tr_b16 a[210:211], v192 offset:18432\n"
"v_mfma_f32_16x16x32_bf16 v[152:155], a[212:215], a[92:95], v[152:155]\n"
"buffer_load_dwordx4 v209, s[24:27], s63 offen offset:128 lds\n"
"v_mfma_f32_16x16x32_bf16 v[68:71], a[216:219], a[92:95], v[68:71]\n"
"s_add_u32 m0, s60, 106240\n"
"ds_read_b64_tr_b16 a[212:213], v193 offset:16384\n"
"ds_read_b64_tr_b16 a[214:215], v193 offset:18432\n"
"s_nop 7\n"
"v_mfma_f32_16x16x32_bf16 v[164:167], a[220:223], a[92:95], v[164:167]\n"
"s_waitcnt lgkmcnt(10)\n"
"buffer_load_dwordx4 v209, s[24:27], s63 offen offset:256 lds\n"
"v_mfma_f32_16x16x32_bf16 v[80:83], a[224:227], a[92:95], v[80:83]\n"
"s_waitcnt lgkmcnt(8)\n"
"s_add_u32 m0, s60, 110208\n"
"ds_read_b64_tr_b16 a[216:217], v192 offset:16896\n"
"v_mfma_f32_16x16x32_bf16 v[176:179], a[228:231], a[92:95], v[176:179]\n"
"s_waitcnt lgkmcnt(7)\n"
"buffer_load_dwordx4 v209, s[24:27], s63 offen offset:384 lds\n"
"v_mfma_f32_16x16x32_bf16 v[92:95], a[232:235], a[92:95], v[92:95]\n"
"s_waitcnt lgkmcnt(5)\n"
"ds_read_b64_tr_b16 a[218:219], v192 offset:18944\n"
"ds_read_b64_tr_b16 a[220:221], v193 offset:16896\n"
"v_mfma_f32_16x16x32_bf16 v[188:191], a[236:239], a[92:95], v[188:191]\n"
"ds_read_b64_tr_b16 a[222:223], v193 offset:18944\n"
"v_mfma_f32_16x16x32_bf16 v[184:187], a[236:239], a[88:91], v[184:187]\n"
"v_mfma_f32_16x16x32_bf16 v[88:91], a[232:235], a[88:91], v[88:91]\n"
"v_mfma_f32_16x16x32_bf16 v[172:175], a[228:231], a[88:91], v[172:175]\n"
"v_mfma_f32_16x16x32_bf16 v[76:79], a[224:227], a[88:91], v[76:79]\n"
"v_mfma_f32_16x16x32_bf16 v[72:75], a[224:227], a[84:87], v[72:75]\n"
"ds_read_b64_tr_b16 a[224:225], v192 offset:20480\n"
"ds_read_b64_tr_b16 a[226:227], v192 offset:22528\n"
"v_mfma_f32_16x16x32_bf16 v[168:171], a[228:231], a[84:87], v[168:171]\n"
"ds_read_b64_tr_b16 a[228:229], v193 offset:20480\n"
"ds_read_b64_tr_b16 a[230:231], v193 offset:22528\n"
"v_mfma_f32_16x16x32_bf16 v[84:87], a[232:235], a[84:87], v[84:87]\n"
"ds_read_b64_tr_b16 a[232:233], v192 offset:20992\n"
"ds_read_b64_tr_b16 a[234:235], v192 offset:23040\n"
"v_mfma_f32_16x16x32_bf16 v[180:183], a[236:239], a[84:87], v[180:183]\n"
"s_waitcnt lgkmcnt(12)\n"
"ds_read_b64_tr_b16 a[236:237], v193 offset:20992\n"
"ds_read_b64_tr_b16 a[238:239], v193 offset:23040\n"
"v_mfma_f32_16x16x32_bf16 v[0:3], a[208:211], a[96:99], v[0:3]\n"
"s_waitcnt lgkmcnt(12)\n"
"v_mfma_f32_16x16x32_bf16 v[96:99], a[212:215], a[96:99], v[96:99]\n"
"s_waitcnt lgkmcnt(10)\n"
"v_mfma_f32_16x16x32_bf16 v[12:15], a[216:219], a[96:99], v[12:15]\n"
"s_waitcnt lgkmcnt(8)\n"
"v_mfma_f32_16x16x32_bf16 v[108:111], a[220:223], a[96:99], v[108:111]\n"
"v_mfma_f32_16x16x32_bf16 v[112:115], a[220:223], a[100:103], v[112:115]\n"
"v_mfma_f32_16x16x32_bf16 v[16:19], a[216:219], a[100:103], v[16:19]\n"
"v_mfma_f32_16x16x32_bf16 v[100:103], a[212:215], a[100:103], v[100:103]\n"
"v_mfma_f32_16x16x32_bf16 v[4:7], a[208:211], a[100:103], v[4:7]\n"
"v_mfma_f32_16x16x32_bf16 v[8:11], a[208:211], a[104:107], v[8:11]\n"
"ds_read_b64_tr_b16 a[208:209], v192 offset:24576\n"
"ds_read_b64_tr_b16 a[210:211], v192 offset:26624\n"
"v_mfma_f32_16x16x32_bf16 v[104:107], a[212:215], a[104:107], v[104:107]\n"
"buffer_load_dword a249, v255, s[40:43], s70 offen sc1\n"
"v_mfma_f32_16x16x32_bf16 v[20:23], a[216:219], a[104:107], v[20:23]\n"
"ds_read_b64_tr_b16 a[212:213], v193 offset:24576\n"
"ds_read_b64_tr_b16 a[214:215], v193 offset:26624\n"
"v_mfma_f32_16x16x32_bf16 v[116:119], a[220:223], a[104:107], v[116:119]\n"
"s_waitcnt lgkmcnt(10)\n"
"s_waitcnt vmcnt(9)\n"
"ds_read_b128 a[144:147], v208 offset:0\n"
"v_mfma_f32_16x16x32_bf16 v[32:35], a[224:227], a[104:107], v[32:35]\n"
"s_waitcnt lgkmcnt(9)\n"
"ds_read_b128 a[148:151], v208 offset:2048\n"
"ds_read_b128 a[152:155], v208 offset:4096\n"
"v_mfma_f32_16x16x32_bf16 v[128:131], a[228:231], a[104:107], v[128:131]\n"
"s_waitcnt lgkmcnt(9)\n"
"ds_read_b128 a[156:159], v208 offset:512\n"
"ds_read_b128 a[160:163], v208 offset:2560\n"
"v_mfma_f32_16x16x32_bf16 v[44:47], a[232:235], a[104:107], v[44:47]\n"
"s_waitcnt lgkmcnt(9)\n"
"ds_read_b128 a[164:167], v208 offset:4608\n"
"ds_read_b64_tr_b16 a[216:217], v192 offset:25088\n"
"v_mfma_f32_16x16x32_bf16 v[140:143], a[236:239], a[104:107], v[140:143]\n"
"ds_read_b64_tr_b16 a[218:219], v192 offset:27136\n"
"ds_read_b64_tr_b16 a[220:221], v193 offset:25088\n"
"v_mfma_f32_16x16x32_bf16 v[136:139], a[236:239], a[100:103], v[136:139]\n"
"ds_read_b64_tr_b16 a[222:223], v193 offset:27136\n"
"v_mfma_f32_16x16x32_bf16 v[40:43], a[232:235], a[100:103], v[40:43]\n"
"s_getreg_b32 s67, hwreg(HW_REG_IB_STS, 0, 4)\n"
"s_add_u32 s66, s66, s67\n"
"v_mfma_f32_16x16x32_bf16 v[124:127], a[228:231], a[100:103], v[124:127]\n"
"v_mfma_f32_16x16x32_bf16 v[28:31], a[224:227], a[100:103], v[28:31]\n"
"s_getreg_b32 s67, hwreg(HW_REG_IB_STS, 0, 4)\n"
"s_add_u32 s66, s66, s67\n"
"v_mfma_f32_16x16x32_bf16 v[24:27], a[224:227], a[96:99], v[24:27]\n"
"ds_read_b64_tr_b16 a[224:225], v192 offset:28672\n"
"s_waitcnt lgkmcnt(14)\n"
"ds_read_b64_tr_b16 a[226:227], v192 offset:30720\n"
"v_mfma_f32_16x16x32_bf16 v[120:123], a[228:231], a[96:99], v[120:123]\n"
"s_getreg_b32 s67, hwreg(HW_REG_IB_STS, 0, 4)\n"
"s_add_u32 s66, s66, s67\n"
"s_waitcnt lgkmcnt(14)\n"
"ds_read_b64_tr_b16 a[228:229], v193 offset:28672\n"
"s_waitcnt lgkmcnt(14)\n"
"ds_read_b64_tr_b16 a[230:231], v193 offset:30720\n"
"v_mfma_f32_16x16x32_bf16 v[36:39], a[232:235], a[96:99], v[36:39]\n"
"s_waitcnt lgkmcnt(8)\n"
"ds_read_b64_tr_b16 a[232:233], v192 offset:29184\n"
"ds_read_b64_tr_b16 a[234:235], v192 offset:31232\n"
"v_mfma_f32_16x16x32_bf16 v[132:135], a[236:239], a[96:99], v[132:135]\n"
"s_getreg_b32 s67, hwreg(HW_REG_IB_STS, 0, 4)\n"
"s_add_u32 s66, s66, s67\n"
"ds_read_b64_tr_b16 a[236:237], v193 offset:29184\n"
"ds_read_b64_tr_b16 a[238:239], v193 offset:31232\n"
"v_mfma_f32_16x16x32_bf16 v[48:51], a[208:211], a[96:99], v[48:51]\n"
"s_getreg_b32 s67, hwreg(HW_REG_IB_STS, 0, 4)\n"
"s_add_u32 s66, s66, s67\n"
"v_mfma_f32_16x16x32_bf16 v[144:147], a[212:215], a[96:99], v[144:147]\n"
"s_waitcnt lgkmcnt(10)\n"
"v_mfma_f32_16x16x32_bf16 v[60:63], a[216:219], a[96:99], v[60:63]\n"
"s_waitcnt lgkmcnt(8)\n"
"s_getreg_b32 s67, hwreg(HW_REG_IB_STS, 0, 4)\n"
"s_add_u32 s66, s66, s67\n"
"v_mfma_f32_16x16x32_bf16 v[156:159], a[220:223], a[96:99], v[156:159]\n"
"v_mfma_f32_16x16x32_bf16 v[160:163], a[220:223], a[100:103], v[160:163]\n"
"s_getreg_b32 s67, hwreg(HW_REG_IB_STS, 0, 4)\n"
"s_add_u32 s66, s66, s67\n"
"s_mov_b32 s63, 0\n"
"v_mfma_f32_16x16x32_bf16 v[64:67], a[216:219], a[100:103], v[64:67]\n"
"s_add_u32 m0, s60, 114688\n"
"v_mfma_f32_16x16x32_bf16 v[148:151], a[212:215], a[100:103], v[148:151]\n"
"s_getreg_b32 s67, hwreg(HW_REG_IB_STS, 0, 4)\n"
"s_add_u32 s66, s66, s67\n"
"s_barrier\n"
"v_mfma_f32_16x16x32_bf16 v[52:55], a[208:211], a[100:103], v[52:55]\n"
"v_mfma_f32_16x16x32_bf16 v[56:59], a[208:211], a[104:107], v[56:59]\n"
"s_getreg_b32 s67, hwreg(HW_REG_IB_STS, 0, 4)\n"
"s_add_u32 s66, s66, s67\n"
"ds_read_b64_tr_b16 a[208:209], v192 offset:32768\n"
"ds_read_b64_tr_b16 a[210:211], v192 offset:34816\n"
"v_mfma_f32_16x16x32_bf16 v[152:155], a[212:215], a[104:107], v[152:155]\n"
"ds_read_b64_tr_b16 a[212:213], v193 offset:32768\n"
"ds_read_b64_tr_b16 a[214:215], v193 offset:34816\n"
"v_mfma_f32_16x16x32_bf16 v[68:71], a[216:219], a[104:107], v[68:71]\n"
"ds_read_b64_tr_b16 a[216:217], v192 offset:33280\n"
"ds_read_b64_tr_b16 a[218:219], v192 offset:35328\n"
"v_mfma_f32_16x16x32_bf16 v[164:167], a[220:223], a[104:107], v[164:167]\n"
"s_waitcnt lgkmcnt(12)\n"
"buffer_load_dword a250, v255, s[40:43], s69 offen sc1\n"
"v_mfma_f32_16x16x32_bf16 v[80:83], a[224:227], a[104:107], v[80:83]\n"
"s_waitcnt lgkmcnt(10)\n"
"ds_read_b64_tr_b16 a[220:221], v193 offset:33280\n"
"ds_read_b64_tr_b16 a[222:223], v193 offset:35328\n"
"v_mfma_f32_16x16x32_bf16 v[176:179], a[228:231], a[104:107], v[176:179]\n"
"s_waitcnt lgkmcnt(10)\n"
"v_mfma_f32_16x16x32_bf16 v[92:95], a[232:235], a[104:107], v[92:95]\n"
"s_waitcnt lgkmcnt(8)\n"
"v_mfma_f32_16x16x32_bf16 v[188:191], a[236:239], a[104:107], v[188:191]\n"
"v_mfma_f32_16x16x32_bf16 v[184:187], a[236:239], a[100:103], v[184:187]\n"
"v_mfma_f32_16x16x32_bf16 v[88:91], a[232:235], a[100:103], v[88:91]\n"
"v_mfma_f32_16x16x32_bf16 v[172:175], a[228:231], a[100:103], v[172:175]\n"
"v_mfma_f32_16x16x32_bf16 v[76:79], a[224:227], a[100:103], v[76:79]\n"
"s_getreg_b32 s67, hwreg(HW_REG_IB_STS, 0, 4)\n"
"s_sub_u32 s66, s66, s67\n"
"v_mfma_f32_16x16x32_bf16 v[72:75], a[224:227], a[96:99], v[72:75]\n"
"ds_read_b64_tr_b16 a[224:225], v192 offset:36864\n"
"ds_read_b64_tr_b16 a[226:227], v192 offset:38912\n"
"v_mfma_f32_16x16x32_bf16 v[168:171], a[228:231], a[96:99], v[168:171]\n"
"ds_read_b64_tr_b16 a[228:229], v193 offset:36864\n"
"ds_read_b64_tr_b16 a[230:231], v193 offset:38912\n"
"v_mfma_f32_16x16x32_bf16 v[84:87], a[232:235], a[96:99], v[84:87]\n"
"s_getreg_b32 s67, hwreg(HW_REG_IB_STS, 0, 4)\n"
"s_sub_u32 s66, s66, s67\n"
"ds_read_b64_tr_b16 a[232:233], v192 offset:37376\n"
"ds_read_b64_tr_b16 a[234:235], v192 offset:39424\n"
"v_mfma_f32_16x16x32_bf16 v[180:183], a[236:239], a[96:99], v[180:183]\n"
"s_waitcnt lgkmcnt(12)\n"
"s_getreg_b32 s67, hwreg(HW_REG_IB_STS, 0, 4)\n"
"s_sub_u32 s66, s66, s67\n"
"ds_read_b64_tr_b16 a[236:237], v193 offset:37376\n"
"ds_read_b64_tr_b16 a[238:239], v193 offset:39424\n"
"v_mfma_f32_16x16x32_bf16 v[0:3], a[208:211], a[108:111], v[0:3]\n"
"s_waitcnt lgkmcnt(12)\n"
"v_mfma_f32_16x16x32_bf16 v[96:99], a[212:215], a[108:111], v[96:99]\n"
"s_waitcnt lgkmcnt(10)\n"
"s_getreg_b32 s67, hwreg(HW_REG_IB_STS, 0, 4)\n"
"s_sub_u32 s66, s66, s67\n"
"v_mfma_f32_16x16x32_bf16 v[12:15], a[216:219], a[108:111], v[12:15]\n"
"s_waitcnt lgkmcnt(8)\n"
"s_getreg_b32 s67, hwreg(HW_REG_IB_STS, 0, 4)\n"
"s_sub_u32 s66, s66, s67\n"
"v_mfma_f32_16x16x32_bf16 v[108:111], a[220:223], a[108:111], v[108:111]\n"
"v_mfma_f32_16x16x32_bf16 v[112:115], a[220:223], a[112:115], v[112:115]\n"
"s_getreg_b32 s67, hwreg(HW_REG_IB_STS, 0, 4)\n"
"s_sub_u32 s66, s66, s67\n"
"v_mfma_f32_16x16x32_bf16 v[16:19], a[216:219], a[112:115], v[16:19]\n"
"v_mfma_f32_16x16x32_bf16 v[100:103], a[212:215], a[112:115], v[100:103]\n"
"s_getreg_b32 s67, hwreg(HW_REG_IB_STS, 0, 4)\n"
"s_sub_u32 s66, s66, s67\n"
"v_mfma_f32_16x16x32_bf16 v[4:7], a[208:211], a[112:115], v[4:7]\n"
"v_mfma_f32_16x16x32_bf16 v[8:11], a[208:211], a[116:119], v[8:11]\n"
"s_getreg_b32 s67, hwreg(HW_REG_IB_STS, 0, 4)\n"
"s_sub_u32 s66, s66, s67\n"
"ds_read_b64_tr_b16 a[208:209], v192 offset:40960\n"
"ds_read_b64_tr_b16 a[210:211], v192 offset:43008\n"
"v_mfma_f32_16x16x32_bf16 v[104:107], a[212:215], a[116:119], v[104:107]\n"
"ds_read_b64_tr_b16 a[212:213], v193 offset:40960\n"
"ds_read_b64_tr_b16 a[214:215], v193 offset:43008\n"
"v_mfma_f32_16x16x32_bf16 v[20:23], a[216:219], a[116:119], v[20:23]\n"
"s_getreg_b32 s67, hwreg(HW_REG_IB_STS, 0, 4)\n"
"s_sub_u32 s66, s66, s67\n"
"ds_read_b64_tr_b16 a[216:217], v192 offset:41472\n"
"ds_read_b64_tr_b16 a[218:219], v192 offset:43520\n"
"v_mfma_f32_16x16x32_bf16 v[116:119], a[220:223], a[116:119], v[116:119]\n"
"s_waitcnt lgkmcnt(12)\n"
"ds_read_b64_tr_b16 a[220:221], v193 offset:41472\n"
"ds_read_b64_tr_b16 a[222:223], v193 offset:43520\n"
"v_mfma_f32_16x16x32_bf16 v[32:35], a[224:227], a[116:119], v[32:35]\n"
"s_waitcnt lgkmcnt(12)\n"
"v_mfma_f32_16x16x32_bf16 v[128:131], a[228:231], a[116:119], v[128:131]\n"
"s_waitcnt lgkmcnt(10)\n"
"s_nop 0\n"
"s_cmp_eq_u32 s70, 0\n"
"s_cselect_b32 s70, 1, -1\n"
"s_mul_i32 s66, s66, s70\n"
"v_mfma_f32_16x16x32_bf16 v[44:47], a[232:235], a[116:119], v[44:47]\n"
"s_waitcnt lgkmcnt(8)\n"
"buffer_load_dwordx4 v205, s[28:31], s63 offen nt lds\n"
"v_mfma_f32_16x16x32_bf16 v[140:143], a[236:239], a[116:119], v[140:143]\n"
"s_add_u32 m0, s60, 118656\n"
"s_lshl_b32 s67, %2, 2\n"
"s_add_u32 s67, s67, 16384\n"
"v_mov_b32 v232, s67\n"
"v_mfma_f32_16x16x32_bf16 v[136:139], a[236:239], a[112:115], v[136:139]\n"
"buffer_load_dwordx4 v205, s[28:31], s63 offen offset:128 nt lds\n"
"v_mfma_f32_16x16x32_bf16 v[40:43], a[232:235], a[112:115], v[40:43]\n"
"s_add_u32 m0, s60, 122624\n"
"v_mov_b32 v233, s66\n"
"v_mfma_f32_16x16x32_bf16 v[124:127], a[228:231], a[112:115], v[124:127]\n"
"buffer_load_dwordx4 v205, s[28:31], s63 offen offset:256 nt lds\n"
"v_mfma_f32_16x16x32_bf16 v[28:31], a[224:227], a[112:115], v[28:31]\n"
"s_add_u32 m0, s60, 126592\n"
"v_mfma_f32_16x16x32_bf16 v[24:27], a[224:227], a[108:111], v[24:27]\n"
"buffer_load_dwordx4 v205, s[28:31], s63 offen offset:384 nt lds\n"
"v_mfma_f32_16x16x32_bf16 v[120:123], a[228:231], a[108:111], v[120:123]\n"
"ds_read_b64_tr_b16 a[224:225], v192 offset:45056\n"
"ds_read_b64_tr_b16 a[226:227], v192 offset:47104\n"
"v_mfma_f32_16x16x32_bf16 v[36:39], a[232:235], a[108:111], v[36:39]\n"
"ds_read_b64_tr_b16 a[228:229], v193 offset:45056\n"
"ds_read_b64_tr_b16 a[230:231], v193 offset:47104\n"
"v_mfma_f32_16x16x32_bf16 v[132:135], a[236:239], a[108:111], v[132:135]\n"
"s_waitcnt lgkmcnt(10)\n"
"ds_read_b64_tr_b16 a[232:233], v192 offset:45568\n"
"ds_read_b64_tr_b16 a[234:235], v192 offset:47616\n"
"v_mfma_f32_16x16x32_bf16 v[48:51], a[208:211], a[108:111], v[48:51]\n"
"s_waitcnt lgkmcnt(10)\n"
"ds_read_b64_tr_b16 a[236:237], v193 offset:45568\n"
"ds_read_b64_tr_b16 a[238:239], v193 offset:47616\n"
"v_mfma_f32_16x16x32_bf16 v[144:147], a[212:215], a[108:111], v[144:147]\n"
"s_waitcnt lgkmcnt(10)\n"
"v_mfma_f32_16x16x32_bf16 v[60:63], a[216:219], a[108:111], v[60:63]\n"
"s_waitcnt lgkmcnt(8)\n"
"v_mfma_f32_16x16x32_bf16 v[156:159], a[220:223], a[108:111], v[156:159]\n"
"v_mfma_f32_16x16x32_bf16 v[160:163], a[220:223], a[112:115], v[160:163]\n"
"v_mfma_f32_16x16x32_bf16 v[64:67], a[216:219], a[112:115], v[64:67]\n"
"v_mfma_f32_16x16x32_bf16 v[148:151], a[212:215], a[112:115], v[148:151]\n"
"s_waitcnt vmcnt(6)\n"
"s_barrier\n"
"ds_write_b32 v232, v233\n"
"v_mfma_f32_16x16x32_bf16 v[52:55], a[208:211], a[112:115], v[52:55]\n"
"s_mov_b32 s63, 16384\n"
"v_mfma_f32_16x16x32_bf16 v[56:59], a[208:211], a[116:119], v[56:59]\n"
"s_add_u32 m0, s60, 131072\n"
"ds_read_b64_tr_b16 a[208:209], v192 offset:49152\n"
"ds_read_b64_tr_b16 a[210:211], v192 offset:51200\n"
"v_mfma_f32_16x16x32_bf16 v[152:155], a[212:215], a[116:119], v[152:155]\n"
"buffer_load_dwordx4 v205, s[28:31], s63 offen nt lds\n"
"v_mfma_f32_16x16x32_bf16 v[68:71], a[216:219], a[116:119], v[68:71]\n"
"ds_read_b64_tr_b16 a[212:213], v194 offset:0\n"
"ds_read_b64_tr_b16 a[214:215], v194 offset:2048\n"
"v_mfma_f32_16x16x32_bf16 v[164:167], a[220:223], a[116:119], v[164:167]\n"
"s_waitcnt lgkmcnt(11)\n"
"ds_read_b64_tr_b16 a[216:217], v194 offset:16384\n"
"ds_read_b64_tr_b16 a[218:219], v194 offset:18432\n"
"v_mfma_f32_16x16x32_bf16 v[80:83], a[224:227], a[116:119], v[80:83]\n"
"s_waitcnt lgkmcnt(11)\n"
"s_waitcnt lgkmcnt(6)\n"
"v_mov_b32 v232, 16384\n"
"v_mfma_f32_16x16x32_bf16 v[176:179], a[228:231], a[116:119], v[176:179]\n"
"ds_read_b64_tr_b16 a[220:221], v194 offset:32768\n"
"ds_read_b64_tr_b16 a[222:223], v194 offset:34816\n"
"v_mfma_f32_16x16x32_bf16 v[92:95], a[232:235], a[116:119], v[92:95]\n"
"v_mfma_f32_16x16x32_bf16 v[188:191], a[236:239], a[116:119], v[188:191]\n"
"v_mfma_f32_16x16x32_bf16 v[184:187], a[236:239], a[112:115], v[184:187]\n"
"v_mfma_f32_16x16x32_bf16 v[88:91], a[232:235], a[112:115], v[88:91]\n"
"v_mfma_f32_16x16x32_bf16 v[172:175], a[228:231], a[112:115], v[172:175]\n"
"v_mfma_f32_16x16x32_bf16 v[76:79], a[224:227], a[112:115], v[76:79]\n"
"v_mfma_f32_16x16x32_bf16 v[72:75], a[224:227], a[108:111], v[72:75]\n"
"s_add_u32 m0, s60, 135040\n"
"ds_read_b64_tr_b16 a[224:225], v195 offset:32768\n"
"ds_read_b64_tr_b16 a[226:227], v195 offset:34816\n"
"v_mfma_f32_16x16x32_bf16 v[168:171], a[228:231], a[108:111], v[168:171]\n"
"buffer_load_dwordx4 v205, s[28:31], s63 offen offset:128 nt lds\n"
"v_mfma_f32_16x16x32_bf16 v[84:87], a[232:235], a[108:111], v[84:87]\n"
"ds_read_b64_tr_b16 a[228:229], v195 offset:16384\n"
"ds_read_b64_tr_b16 a[230:231], v195 offset:18432\n"
"v_mfma_f32_16x16x32_bf16 v[180:183], a[236:239], a[108:111], v[180:183]\n"
"s_waitcnt lgkmcnt(10)\n"
"ds_read_b64_tr_b16 a[232:233], v195 offset:0\n"
"ds_read_b64_tr_b16 a[234:235], v195 offset:2048\n"
"v_mfma_f32_16x16x32_bf16 v[0:3], a[208:211], a[120:123], v[0:3]\n"
"s_waitcnt lgkmcnt(10)\n"
"ds_read_b64_tr_b16 a[236:237], v193 offset:49152\n"
"ds_read_b64_tr_b16 a[238:239], v193 offset:51200\n"
"v_mfma_f32_16x16x32_bf16 v[0:3], a[212:215], a[132:135], v[0:3]\n"
"s_waitcnt lgkmcnt(10)\n"
"v_mfma_f32_16x16x32_bf16 v[0:3], a[216:219], a[144:147], v[0:3]\n"
"s_waitcnt lgkmcnt(8)\n"
"v_mfma_f32_16x16x32_bf16 v[0:3], a[220:223], a[156:159], v[0:3]\n"
"v_mfma_f32_16x16x32_bf16 v[4:7], a[220:223], a[160:163], v[4:7]\n"
"v_mfma_f32_16x16x32_bf16 v[4:7], a[216:219], a[148:151], v[4:7]\n"
"v_mfma_f32_16x16x32_bf16 v[4:7], a[212:215], a[136:139], v[4:7]\n"
"s_nop 4\n"
"v_mul_f32 v0, s62, v0\n"
"v_mul_f32 v1, s62, v1\n"
"v_mfma_f32_16x16x32_bf16 v[4:7], a[208:211], a[124:127], v[4:7]\n"
"v_mul_f32 v2, s62, v2\n"
"v_mul_f32 v3, s62, v3\n"
"v_mfma_f32_16x16x32_bf16 v[8:11], a[208:211], a[128:131], v[8:11]\n"
"ds_read_b64_tr_b16 a[208:209], v192 offset:49664\n"
"ds_read_b64_tr_b16 a[210:211], v192 offset:51712\n"
"v_mfma_f32_16x16x32_bf16 v[8:11], a[212:215], a[140:143], v[8:11]\n"
"s_add_u32 m0, s60, 139008\n"
"ds_read_b64_tr_b16 a[212:213], v194 offset:512\n"
"v_mfma_f32_16x16x32_bf16 v[8:11], a[216:219], a[152:155], v[8:11]\n"
"buffer_load_dwordx4 v205, s[28:31], s63 offen offset:256 nt lds\n"
"v_mfma_f32_16x16x32_bf16 v[8:11], a[220:223], a[164:167], v[8:11]\n"
"s_waitcnt lgkmcnt(9)\n"
"ds_read_b64_tr_b16 a[214:215], v194 offset:2560\n"
"ds_read_b64_tr_b16 a[216:217], v194 offset:16896\n"
"v_mfma_f32_16x16x32_bf16 v[104:107], a[224:227], a[164:167], v[104:107]\n"
"s_waitcnt lgkmcnt(9)\n"
"ds_read_b64_tr_b16 a[218:219], v194 offset:18944\n"
"ds_read_b64_tr_b16 a[220:221], v194 offset:33280\n"
"v_mfma_f32_16x16x32_bf16 v[104:107], a[228:231], a[152:155], v[104:107]\n"
"s_waitcnt lgkmcnt(9)\n"
"ds_read_b64_tr_b16 a[222:223], v194 offset:35328\n"
"v_cvt_pk_bf16_f32 v0, v0, v1\n"
"v_mfma_f32_16x16x32_bf16 v[104:107], a[232:235], a[140:143], v[104:107]\n"
"s_waitcnt lgkmcnt(8)\n"
"v_cvt_pk_bf16_f32 v1, v2, v3\n"
"v_mfma_f32_16x16x32_bf16 v[104:107], a[236:239], a[128:131], v[104:107]\n"
"v_mfma_f32_16x16x32_bf16 v[100:103], a[236:239], a[124:127], v[100:103]\n"
"v_mfma_f32_16x16x32_bf16 v[100:103], a[232:235], a[136:139], v[100:103]\n"
"v_mfma_f32_16x16x32_bf16 v[100:103], a[228:231], a[148:151], v[100:103]\n"
"v_mfma_f32_16x16x32_bf16 v[100:103], a[224:227], a[160:163], v[100:103]\n"
"v_mfma_f32_16x16x32_bf16 v[96:99], a[224:227], a[156:159], v[96:99]\n"
"ds_read_b64_tr_b16 a[224:225], v195 offset:33280\n"
"ds_read_b64_tr_b16 a[226:227], v195 offset:35328\n"
"v_mfma_f32_16x16x32_bf16 v[96:99], a[228:231], a[144:147], v[96:99]\n"
"ds_read_b64_tr_b16 a[228:229], v195 offset:16896\n"
"ds_read_b64_tr_b16 a[230:231], v195 offset:18944\n"
"v_mfma_f32_16x16x32_bf16 v[96:99], a[232:235], a[132:135], v[96:99]\n"
"s_add_u32 m0, s60, 142976\n"
"ds_read_b64_tr_b16 a[232:233], v195 offset:512\n"
"ds_read_b64_tr_b16 a[234:235], v195 offset:2560\n"
"v_mfma_f32_16x16x32_bf16 v[96:99], a[236:239], a[120:123], v[96:99]\n"
"s_waitcnt lgkmcnt(12)\n"
"buffer_load_dwordx4 v205, s[28:31], s63 offen offset:384 nt lds\n"
"v_mfma_f32_16x16x32_bf16 v[12:15], a[208:211], a[120:123], v[12:15]\n"
"s_waitcnt lgkmcnt(10)\n"
"ds_read_b64_tr_b16 a[236:237], v193 offset:49664\n"
"ds_read_b64_tr_b16 a[238:239], v193 offset:51712\n"
"v_mfma_f32_16x16x32_bf16 v[12:15], a[212:215], a[132:135], v[12:15]\n"
"s_waitcnt lgkmcnt(10)\n"
"v_mfma_f32_16x16x32_bf16 v[12:15], a[216:219], a[144:147], v[12:15]\n"
"s_waitcnt lgkmcnt(8)\n"
"v_mul_f32 v96, s62, v96\n"
"v_mul_f32 v97, s62, v97\n"
"v_mfma_f32_16x16x32_bf16 v[12:15], a[220:223], a[156:159], v[12:15]\n"
"v_mul_f32 v98, s62, v98\n"
"v_mul_f32 v99, s62, v99\n"
"v_mfma_f32_16x16x32_bf16 v[16:19], a[220:223], a[160:163], v[16:19]\n"
"v_cvt_pk_bf16_f32 v2, v96, v97\n"
"v_cvt_pk_bf16_f32 v3, v98, v99\n"
"v_mfma_f32_16x16x32_bf16 v[16:19], a[216:219], a[148:151], v[16:19]\n"
"v_mul_f32 v4, s62, v4\n"
"v_mul_f32 v5, s62, v5\n"
"v_mfma_f32_16x16x32_bf16 v[16:19], a[212:215], a[136:139], v[16:19]\n"
"s_barrier\n"
"ds_read_b128 v[234:237], v232\n"
"v_mfma_f32_16x16x32_bf16 v[16:19], a[208:211], a[124:127], v[16:19]\n"
"v_mul_f32 v6, s62, v6\n"
"v_mul_f32 v7, s62, v7\n"
"v_mfma_f32_16x16x32_bf16 v[20:23], a[208:211], a[128:131], v[20:23]\n"
"ds_read_b64_tr_b16 a[208:209], v192 offset:53248\n"
"ds_read_b64_tr_b16 a[210:211], v192 offset:55296\n"
"v_mfma_f32_16x16x32_bf16 v[20:23], a[212:215], a[140:143], v[20:23]\n"
"ds_read_b64_tr_b16 a[212:213], v194 offset:4096\n"
"ds_read_b64_tr_b16 a[214:215], v194 offset:6144\n"
"v_mfma_f32_16x16x32_bf16 v[20:23], a[216:219], a[152:155], v[20:23]\n"
"s_mov_b32 s63, 0\n"
"ds_read_b64_tr_b16 a[216:217], v194 offset:20480\n"
"ds_read_b64_tr_b16 a[218:219], v194 offset:22528\n"
"v_mfma_f32_16x16x32_bf16 v[20:23], a[220:223], a[164:167], v[20:23]\n"
"s_waitcnt lgkmcnt(13)\n"
"s_add_u32 m0, s60, 147456\n"
"ds_read_b64_tr_b16 a[220:221], v194 offset:36864\n"
"v_mfma_f32_16x16x32_bf16 v[116:119], a[224:227], a[164:167], v[116:119]\n"
"s_waitcnt lgkmcnt(12)\n"
"s_waitcnt lgkmcnt(7)\n"
"v_add3_u32 v234, v234, v235, v236\n"
"v_add_u32 v234, v234, v237\n"
"v_mfma_f32_16x16x32_bf16 v[116:119], a[228:231], a[152:155], v[116:119]\n"
"v_cmp_lt_i32 s[58:59], 0, v234\n"
"s_nop 0\n"
"s_and_b32 s68, s58, 1\n"
"v_mfma_f32_16x16x32_bf16 v[116:119], a[232:235], a[140:143], v[116:119]\n"
"buffer_load_dwordx4 v204, s[32:35], s63 offen nt lds\n"
"v_mfma_f32_16x16x32_bf16 v[116:119], a[236:239], a[128:131], v[116:119]\n"
"ds_read_b64_tr_b16 a[222:223], v194 offset:38912\n"
"v_cvt_pk_bf16_f32 v4, v4, v5\n"
"v_mfma_f32_16x16x32_bf16 v[112:115], a[236:239], a[124:127], v[112:115]\n"
"v_cvt_pk_bf16_f32 v5, v6, v7\n"
"v_mul_f32 v100, s62, v100\n"
"v_mfma_f32_16x16x32_bf16 v[112:115], a[232:235], a[136:139], v[112:115]\n"
"v_mul_f32 v101, s62, v101\n"
"v_mul_f32 v102, s62, v102\n"
"v_mfma_f32_16x16x32_bf16 v[112:115], a[228:231], a[148:151], v[112:115]\n"
"v_mul_f32 v103, s62, v103\n"
"v_cvt_pk_bf16_f32 v6, v100, v101\n"
"v_mfma_f32_16x16x32_bf16 v[112:115], a[224:227], a[160:163], v[112:115]\n"
"v_cvt_pk_bf16_f32 v7, v102, v103\n"
"v_mul_f32 v8, s62, v8\n"
"v_mfma_f32_16x16x32_bf16 v[108:111], a[224:227], a[156:159], v[108:111]\n"
"ds_read_b64_tr_b16 a[224:225], v195 offset:36864\n"
"ds_read_b64_tr_b16 a[226:227], v195 offset:38912\n"
"v_mfma_f32_16x16x32_bf16 v[108:111], a[228:231], a[144:147], v[108:111]\n"
"ds_read_b64_tr_b16 a[228:229], v195 offset:20480\n"
"ds_read_b64_tr_b16 a[230:231], v195 offset:22528\n"
"v_mfma_f32_16x16x32_bf16 v[108:111], a[232:235], a[132:135], v[108:111]\n"
"ds_read_b64_tr_b16 a[232:233], v195 offset:4096\n"
"ds_read_b64_tr_b16 a[234:235], v195 offset:6144\n"
"v_mfma_f32_16x16x32_bf16 v[108:111], a[236:239], a[120:123], v[108:111]\n"
"s_waitcnt lgkmcnt(12)\n"
"s_add_u32 m0, s60, 151424\n"
"ds_read_b64_tr_b16 a[236:237], v193 offset:53248\n"
"v_mfma_f32_16x16x32_bf16 v[24:27], a[208:211], a[120:123], v[24:27]\n"
"s_waitcnt lgkmcnt(11)\n"
"buffer_load_dwordx4 v204, s[32:35], s63 offen offset:128 nt lds\n"
"v_mfma_f32_16x16x32_bf16 v[24:27], a[212:215], a[132:135], v[24:27]\n"
"s_waitcnt lgkmcnt(9)\n"
"ds_read_b64_tr_b16 a[238:239], v193 offset:55296\n"
"v_mul_f32 v9, s62, v9\n"
"v_mfma_f32_16x16x32_bf16 v[24:27], a[216:219], a[144:147], v[24:27]\n"
"s_waitcnt lgkmcnt(8)\n"
"v_mul_f32 v10, s62, v10\n"
"v_mul_f32 v11, s62, v11\n"
"v_mfma_f32_16x16x32_bf16 v[24:27], a[220:223], a[156:159], v[24:27]\n"
"v_cvt_pk_bf16_f32 v8, v8, v9\n"
"v_cvt_pk_bf16_f32 v9, v10, v11\n"
"v_mfma_f32_16x16x32_bf16 v[28:31], a[220:223], a[160:163], v[28:31]\n"
"v_mul_f32 v104, s62, v104\n"
"v_mul_f32 v105, s62, v105\n"
"v_mfma_f32_16x16x32_bf16 v[28:31], a[216:219], a[148:151], v[28:31]\n"
"v_mul_f32 v106, s62, v106\n"
"v_mul_f32 v107, s62, v107\n"
"v_mfma_f32_16x16x32_bf16 v[28:31], a[212:215], a[136:139], v[28:31]\n"
"v_cvt_pk_bf16_f32 v10, v104, v105\n"
"v_cvt_pk_bf16_f32 v11, v106, v107\n"
"v_mfma_f32_16x16x32_bf16 v[28:31], a[208:211], a[124:127], v[28:31]\n"
"v_mul_f32 v12, s62, v12\n"
"v_mul_f32 v13, s62, v13\n"
"v_mfma_f32_16x16x32_bf16 v[32:35], a[208:211], a[128:131], v[32:35]\n"
"ds_read_b64_tr_b16 a[208:209], v192 offset:53760\n"
"ds_read_b64_tr_b16 a[210:211], v192 offset:55808\n"
"v_mfma_f32_16x16x32_bf16 v[32:35], a[212:215], a[140:143], v[32:35]\n"
"ds_read_b64_tr_b16 a[212:213], v194 offset:4608\n"
"ds_read_b64_tr_b16 a[214:215], v194 offset:6656\n"
"v_mfma_f32_16x16x32_bf16 v[32:35], a[216:219], a[152:155], v[32:35]\n"
"ds_read_b64_tr_b16 a[216:217], v194 offset:20992\n"
"ds_read_b64_tr_b16 a[218:219], v194 offset:23040\n"
"v_mfma_f32_16x16x32_bf16 v[32:35], a[220:223], a[164:167], v[32:35]\n"
"s_waitcnt lgkmcnt(12)\n"
"ds_read_b64_tr_b16 a[220:221], v194 offset:37376\n"
"ds_read_b64_tr_b16 a[222:223], v194 offset:39424\n"
"v_mfma_f32_16x16x32_bf16 v[128:131], a[224:227], a[164:167], v[128:131]\n"
"s_waitcnt lgkmcnt(12)\n"
"s_add_u32 m0, s60, 155392\n"
"v_mul_f32 v14, s62, v14\n"
"v_mfma_f32_16x16x32_bf16 v[128:131], a[228:231], a[152:155], v[128:131]\n"
"s_waitcnt lgkmcnt(10)\n"
"buffer_load_dwordx4 v204, s[32:35], s63 offen offset:256 nt lds\n"
"v_mfma_f32_16x16x32_bf16 v[128:131], a[232:235], a[140:143], v[128:131]\n"
"s_waitcnt lgkmcnt(8)\n"
"v_mul_f32 v15, s62, v15\n"
"v_cvt_pk_bf16_f32 v12, v12, v13\n"
"v_mfma_f32_16x16x32_bf16 v[128:131], a[236:239], a[128:131], v[128:131]\n"
"v_cvt_pk_bf16_f32 v13, v14, v15\n"
"v_mul_f32 v108, s62, v108\n"
"v_mfma_f32_16x16x32_bf16 v[124:127], a[236:239], a[124:127], v[124:127]\n"
"v_mul_f32 v109, s62, v109\n"
"v_mul_f32 v110, s62, v110\n"
"v_mfma_f32_16x16x32_bf16 v[124:127], a[232:235], a[136:139], v[124:127]\n"
"v_mul_f32 v111, s62, v111\n"
"v_cvt_pk_bf16_f32 v14, v108, v109\n"
"v_mfma_f32_16x16x32_bf16 v[124:127], a[228:231], a[148:151], v[124:127]\n"
"v_cvt_pk_bf16_f32 v15, v110, v111\n"
"v_mul_f32 v16, s62, v16\n"
"v_mfma_f32_16x16x32_bf16 v[124:127], a[224:227], a[160:163], v[124:127]\n"
"v_mul_f32 v17, s62, v17\n"
"v_mul_f32 v18, s62, v18\n"
"v_mfma_f32_16x16x32_bf16 v[120:123], a[224:227], a[156:159], v[120:123]\n"
"ds_read_b64_tr_b16 a[224:225], v195 offset:37376\n"
"ds_read_b64_tr_b16 a[226:227], v195 offset:39424\n"
"v_mfma_f32_16x16x32_bf16 v[120:123], a[228:231], a[144:147], v[120:123]\n"
"ds_read_b64_tr_b16 a[228:229], v195 offset:20992\n"
"ds_read_b64_tr_b16 a[230:231], v195 offset:23040\n"
"v_mfma_f32_16x16x32_bf16 v[120:123], a[232:235], a[132:135], v[120:123]\n"
"ds_read_b64_tr_b16 a[232:233], v195 offset:4608\n"
"ds_read_b64_tr_b16 a[234:235], v195 offset:6656\n"
"v_mfma_f32_16x16x32_bf16 v[120:123], a[236:239], a[120:123], v[120:123]\n"
"s_waitcnt lgkmcnt(12)\n"
"ds_read_b64_tr_b16 a[236:237], v193 offset:53760\n"
"ds_read_b64_tr_b16 a[238:239], v193 offset:55808\n"
"v_mfma_f32_16x16x32_bf16 v[36:39], a[208:211], a[120:123], v[36:39]\n"
"s_waitcnt lgkmcnt(12)\n"
"v_mul_f32 v19, s62, v19\n"
"v_cvt_pk_bf16_f32 v16, v16, v17\n"
"v_mfma_f32_16x16x32_bf16 v[36:39], a[212:215], a[132:135], v[36:39]\n"
"s_waitcnt lgkmcnt(10)\n"
"s_add_u32 m0, s60, 159360\n"
"v_cvt_pk_bf16_f32 v17, v18, v19\n"
"v_mfma_f32_16x16x32_bf16 v[36:39], a[216:219], a[144:147], v[36:39]\n"
"s_waitcnt lgkmcnt(8)\n"
"buffer_load_dwordx4 v204, s[32:35], s63 offen offset:384 nt lds\n"
"v_mfma_f32_16x16x32_bf16 v[36:39], a[220:223], a[156:159], v[36:39]\n"
"v_mul_f32 v112, s62, v112\n"
"v_mul_f32 v113, s62, v113\n"
"v_mfma_f32_16x16x32_bf16 v[40:43], a[220:223], a[160:163], v[40:43]\n"
"v_mul_f32 v114, s62, v114\n"
"v_mul_f32 v115, s62, v115\n"
"v_mfma_f32_16x16x32_bf16 v[40:43], a[216:219], a[148:151], v[40:43]\n"
"v_cvt_pk_bf16_f32 v18, v112, v113\n"
"v_cvt_pk_bf16_f32 v19, v114, v115\n"
"v_mfma_f32_16x16x32_bf16 v[40:43], a[212:215], a[136:139], v[40:43]\n"
"s_barrier\n"
"v_mul_f32 v20, s62, v20\n"
"v_mfma_f32_16x16x32_bf16 v[40:43], a[208:211], a[124:127], v[40:43]\n"
"v_mul_f32 v21, s62, v21\n"
"v_mul_f32 v22, s62, v22\n"
"v_mfma_f32_16x16x32_bf16 v[44:47], a[208:211], a[128:131], v[44:47]\n"
"ds_read_b64_tr_b16 a[208:209], v192 offset:57344\n"
"ds_read_b64_tr_b16 a[210:211], v192 offset:59392\n"
"v_mfma_f32_16x16x32_bf16 v[44:47], a[212:215], a[140:143], v[44:47]\n"
"ds_read_b64_tr_b16 a[212:213], v194 offset:8192\n"
"ds_read_b64_tr_b16 a[214:215], v194 offset:10240\n"
"v_mfma_f32_16x16x32_bf16 v[44:47], a[216:219], a[152:155], v[44:47]\n"
"ds_read_b64_tr_b16 a[216:217], v194 offset:24576\n"
"ds_read_b64_tr_b16 a[218:219], v194 offset:26624\n"
"v_mfma_f32_16x16x32_bf16 v[44:47], a[220:223], a[164:167], v[44:47]\n"
"s_waitcnt lgkmcnt(12)\n"
"ds_read_b64_tr_b16 a[220:221], v194 offset:40960\n"
"ds_read_b64_tr_b16 a[222:223], v194 offset:43008\n"
"v_mfma_f32_16x16x32_bf16 v[140:143], a[224:227], a[164:167], v[140:143]\n"
"s_waitcnt lgkmcnt(12)\n"
"v_mul_f32 v23, s62, v23\n"
"v_cvt_pk_bf16_f32 v20, v20, v21\n"
"v_mfma_f32_16x16x32_bf16 v[140:143], a[228:231], a[152:155], v[140:143]\n"
"s_waitcnt lgkmcnt(10)\n"
"s_mov_b32 s63, 32768\n"
"v_cvt_pk_bf16_f32 v21, v22, v23\n"
"v_mfma_f32_16x16x32_bf16 v[140:143], a[232:235], a[140:143], v[140:143]\n"
"s_waitcnt lgkmcnt(8)\n"
"s_add_u32 m0, s60, 0\n"
"v_mul_f32 v116, s62, v116\n"
"v_mfma_f32_16x16x32_bf16 v[140:143], a[236:239], a[128:131], v[140:143]\n"
"buffer_load_dwordx4 v205, s[28:31], s63 offen nt lds\n"
"v_mfma_f32_16x16x32_bf16 v[136:139], a[236:239], a[124:127], v[136:139]\n"
"v_mul_f32 v117, s62, v117\n"
"v_mul_f32 v118, s62, v118\n"
"v_mfma_f32_16x16x32_bf16 v[136:139], a[232:235], a[136:139], v[136:139]\n"
"v_mul_f32 v119, s62, v119\n"
"v_cvt_pk_bf16_f32 v22, v116, v117\n"
"v_mfma_f32_16x16x32_bf16 v[136:139], a[228:231], a[148:151], v[136:139]\n"
"v_cvt_pk_bf16_f32 v23, v118, v119\n"
"v_mul_f32 v24, s62, v24\n"
"v_mfma_f32_16x16x32_bf16 v[136:139], a[224:227], a[160:163], v[136:139]\n"
"v_mul_f32 v25, s62, v25\n"
"v_mul_f32 v26, s62, v26\n"
"v_mfma_f32_16x16x32_bf16 v[132:135], a[224:227], a[156:159], v[132:135]\n"
"ds_read_b64_tr_b16 a[224:225], v195 offset:40960\n"
"ds_read_b64_tr_b16 a[226:227], v195 offset:43008\n"
"v_mfma_f32_16x16x32_bf16 v[132:135], a[228:231], a[144:147], v[132:135]\n"
"ds_read_b64_tr_b16 a[228:229], v195 offset:24576\n"
"ds_read_b64_tr_b16 a[230:231], v195 offset:26624\n"
"v_mfma_f32_16x16x32_bf16 v[132:135], a[232:235], a[132:135], v[132:135]\n"
"ds_read_b64_tr_b16 a[232:233], v195 offset:8192\n"
"ds_read_b64_tr_b16 a[234:235], v195 offset:10240\n"
"v_mfma_f32_16x16x32_bf16 v[132:135], a[236:239], a[120:123], v[132:135]\n"
"s_waitcnt lgkmcnt(12)\n"
"ds_read_b64_tr_b16 a[236:237], v193 offset:57344\n"
"ds_read_b64_tr_b16 a[238:239], v193 offset:59392\n"
"v_mfma_f32_16x16x32_bf16 v[48:51], a[208:211], a[120:123], v[48:51]\n"
"s_waitcnt lgkmcnt(12)\n"
"v_mul_f32 v27, s62, v27\n"
"v_cvt_pk_bf16_f32 v24, v24, v25\n"
"v_mfma_f32_16x16x32_bf16 v[48:51], a[212:215], a[132:135], v[48:51]\n"
"s_waitcnt lgkmcnt(10)\n"
"v_cvt_pk_bf16_f32 v25, v26, v27\n"
"v_mul_f32 v120, s62, v120\n"
"v_mfma_f32_16x16x32_bf16 v[48:51], a[216:219], a[144:147], v[48:51]\n"
"s_waitcnt lgkmcnt(8)\n"
"s_add_u32 m0, s60, 3968\n"
"v_mul_f32 v121, s62, v121\n"
"v_mfma_f32_16x16x32_bf16 v[48:51], a[220:223], a[156:159], v[48:51]\n"
"buffer_load_dwordx4 v205, s[28:31], s63 offen offset:128 nt lds\n"
"v_mfma_f32_16x16x32_bf16 v[52:55], a[220:223], a[160:163], v[52:55]\n"
"v_mul_f32 v122, s62, v122\n"
"v_mul_f32 v123, s62, v123\n"
"v_mfma_f32_16x16x32_bf16 v[52:55], a[216:219], a[148:151], v[52:55]\n"
"v_cvt_pk_bf16_f32 v26, v120, v121\n"
"v_cvt_pk_bf16_f32 v27, v122, v123\n"
"v_mfma_f32_16x16x32_bf16 v[52:55], a[212:215], a[136:139], v[52:55]\n"
"v_mul_f32 v28, s62, v28\n"
"v_mul_f32 v29, s62, v29\n"
"v_mfma_f32_16x16x32_bf16 v[52:55], a[208:211], a[124:127], v[52:55]\n"
"v_mul_f32 v30, s62, v30\n"
"v_mul_f32 v31, s62, v31\n"
"v_mfma_f32_16x16x32_bf16 v[56:59], a[208:211], a[128:131], v[56:59]\n"
"ds_read_b64_tr_b16 a[208:209], v192 offset:57856\n"
"ds_read_b64_tr_b16 a[210:211], v192 offset:59904\n"
"v_mfma_f32_16x16x32_bf16 v[56:59], a[212:215], a[140:143], v[56:59]\n"
"ds_read_b64_tr_b16 a[212:213], v194 offset:8704\n"
"ds_read_b64_tr_b16 a[214:215], v194 offset:10752\n"
"v_mfma_f32_16x16x32_bf16 v[56:59], a[216:219], a[152:155], v[56:59]\n"
"ds_read_b64_tr_b16 a[216:217], v194 offset:25088\n"
"ds_read_b64_tr_b16 a[218:219], v194 offset:27136\n"
"v_mfma_f32_16x16x32_bf16 v[56:59], a[220:223], a[164:167], v[56:59]\n"
"s_waitcnt lgkmcnt(12)\n"
"ds_read_b64_tr_b16 a[220:221], v194 offset:41472\n"
"ds_read_b64_tr_b16 a[222:223], v194 offset:43520\n"
"v_mfma_f32_16x16x32_bf16 v[152:155], a[224:227], a[164:167], v[152:155]\n"
"s_waitcnt lgkmcnt(12)\n"
"v_cvt_pk_bf16_f32 v28, v28, v29\n"
"v_cvt_pk_bf16_f32 v29, v30, v31\n"
"v_mfma_f32_16x16x32_bf16 v[152:155], a[228:231], a[152:155], v[152:155]\n"
"s_waitcnt lgkmcnt(10)\n"
"v_mul_f32 v124, s62, v124\n"
"v_mul_f32 v125, s62, v125\n"
"v_mfma_f32_16x16x32_bf16 v[152:155], a[232:235], a[140:143], v[152:155]\n"
"s_waitcnt lgkmcnt(8)\n"
"v_mul_f32 v126, s62, v126\n"
"v_mul_f32 v127, s62, v127\n"
"v_mfma_f32_16x16x32_bf16 v[152:155], a[236:239], a[128:131], v[152:155]\n"
"s_add_u32 m0, s60, 7936\n"
"v_cvt_pk_bf16_f32 v30, v124, v125\n"
"v_cvt_pk_bf16_f32 v31, v126, v127\n"
"v_mfma_f32_16x16x32_bf16 v[148:151], a[236:239], a[124:127], v[148:151]\n"
"buffer_load_dwordx4 v205, s[28:31], s63 offen offset:256 nt lds\n"
"v_mfma_f32_16x16x32_bf16 v[148:151], a[232:235], a[136:139], v[148:151]\n"
"v_mul_f32 v32, s62, v32\n"
"v_mul_f32 v33, s62, v33\n"
"v_mfma_f32_16x16x32_bf16 v[148:151], a[228:231], a[148:151], v[148:151]\n"
"v_mul_f32 v34, s62, v34\n"
"v_mul_f32 v35, s62, v35\n"
"v_mfma_f32_16x16x32_bf16 v[148:151], a[224:227], a[160:163], v[148:151]\n"
"v_cvt_pk_bf16_f32 v32, v32, v33\n"
"v_cvt_pk_bf16_f32 v33, v34, v35\n"
"v_mfma_f32_16x16x32_bf16 v[144:147], a[224:227], a[156:159], v[144:147]\n"
"ds_read_b64_tr_b16 a[224:225], v195 offset:41472\n"
"v_mul_f32 v128, s62, v128\n"
"v_mfma_f32_16x16x32_bf16 v[144:147], a[228:231], a[144:147], v[144:147]\n"
"ds_read_b64_tr_b16 a[226:227], v195 offset:43520\n"
"v_mul_f32 v129, s62, v129\n"
"v_mfma_f32_16x16x32_bf16 v[144:147], a[232:235], a[132:135], v[144:147]\n"
"ds_read_b64_tr_b16 a[228:229], v195 offset:25088\n"
"v_mul_f32 v130, s62, v130\n"
"v_mfma_f32_16x16x32_bf16 v[144:147], a[236:239], a[120:123], v[144:147]\n"
"s_waitcnt lgkmcnt(9)\n"
"ds_read_b64_tr_b16 a[230:231], v195 offset:27136\n"
"v_mul_f32 v131, s62, v131\n"
"v_mfma_f32_16x16x32_bf16 v[60:63], a[208:211], a[120:123], v[60:63]\n"
"s_waitcnt lgkmcnt(8)\n"
"ds_read_b64_tr_b16 a[232:233], v195 offset:8704\n"
"v_cvt_pk_bf16_f32 v34, v128, v129\n"
"v_mfma_f32_16x16x32_bf16 v[60:63], a[212:215], a[132:135], v[60:63]\n"
"s_waitcnt lgkmcnt(7)\n"
"ds_read_b64_tr_b16 a[234:235], v195 offset:10752\n"
"v_cvt_pk_bf16_f32 v35, v130, v131\n"
"v_mfma_f32_16x16x32_bf16 v[60:63], a[216:219], a[144:147], v[60:63]\n"
"s_waitcnt lgkmcnt(6)\n"
"ds_read_b64_tr_b16 a[236:237], v193 offset:57856\n"
"v_mul_f32 v36, s62, v36\n"
"v_mfma_f32_16x16x32_bf16 v[60:63], a[220:223], a[156:159], v[60:63]\n"
"v_mul_f32 v37, s62, v37\n"
"ds_read_b64_tr_b16 a[238:239], v193 offset:59904\n"
"v_mfma_f32_16x16x32_bf16 v[64:67], a[220:223], a[160:163], v[64:67]\n"
"s_add_u32 m0, s60, 11904\n"
"v_mul_f32 v38, s62, v38\n"
"v_mul_f32 v39, s62, v39\n"
"v_mfma_f32_16x16x32_bf16 v[64:67], a[216:219], a[148:151], v[64:67]\n"
"buffer_load_dwordx4 v205, s[28:31], s63 offen offset:384 nt lds\n"
"v_mfma_f32_16x16x32_bf16 v[64:67], a[212:215], a[136:139], v[64:67]\n"
"v_cvt_pk_bf16_f32 v36, v36, v37\n"
"v_cvt_pk_bf16_f32 v37, v38, v39\n"
"v_mfma_f32_16x16x32_bf16 v[64:67], a[208:211], a[124:127], v[64:67]\n"
"v_mul_f32 v132, s62, v132\n"
"v_mul_f32 v133, s62, v133\n"
"v_mfma_f32_16x16x32_bf16 v[68:71], a[208:211], a[128:131], v[68:71]\n"
"v_mul_f32 v134, s62, v134\n"
"v_mul_f32 v135, s62, v135\n"
"v_mfma_f32_16x16x32_bf16 v[68:71], a[212:215], a[140:143], v[68:71]\n"
"v_cvt_pk_bf16_f32 v38, v132, v133\n"
"v_cvt_pk_bf16_f32 v39, v134, v135\n"
"v_mfma_f32_16x16x32_bf16 v[68:71], a[216:219], a[152:155], v[68:71]\n"
"v_mul_f32 v40, s62, v40\n"
"v_mul_f32 v41, s62, v41\n"
"v_mfma_f32_16x16x32_bf16 v[68:71], a[220:223], a[164:167], v[68:71]\n"
"s_waitcnt lgkmcnt(6)\n"
"v_mul_f32 v42, s62, v42\n"
"v_mul_f32 v43, s62, v43\n"
"v_mfma_f32_16x16x32_bf16 v[164:167], a[224:227], a[164:167], v[164:167]\n"
"s_waitcnt lgkmcnt(4)\n"
"v_cvt_pk_bf16_f32 v40, v40, v41\n"
"v_cvt_pk_bf16_f32 v41, v42, v43\n"
"v_mfma_f32_16x16x32_bf16 v[164:167], a[228:231], a[152:155], v[164:167]\n"
"s_waitcnt lgkmcnt(2)\n"
"ds_read_b64_tr_b16 a[208:209], v192 offset:61440\n"
"v_mul_f32 v136, s62, v136\n"
"v_mfma_f32_16x16x32_bf16 v[164:167], a[232:235], a[140:143], v[164:167]\n"
"s_waitcnt lgkmcnt(1)\n"
"ds_read_b64_tr_b16 a[210:211], v192 offset:63488\n"
"v_mul_f32 v137, s62, v137\n"
"v_mfma_f32_16x16x32_bf16 v[164:167], a[236:239], a[128:131], v[164:167]\n"
"ds_read_b64_tr_b16 a[212:213], v194 offset:12288\n"
"v_mul_f32 v138, s62, v138\n"
"v_mfma_f32_16x16x32_bf16 v[160:163], a[236:239], a[124:127], v[160:163]\n"
"s_mov_b32 s63, 16384\n"
"ds_read_b64_tr_b16 a[214:215], v194 offset:14336\n"
"v_mul_f32 v139, s62, v139\n"
"v_mfma_f32_16x16x32_bf16 v[160:163], a[232:235], a[136:139], v[160:163]\n"
"s_add_u32 m0, s60, 16384\n"
"ds_read_b64_tr_b16 a[216:217], v194 offset:28672\n"
"v_cvt_pk_bf16_f32 v42, v136, v137\n"
"v_mfma_f32_16x16x32_bf16 v[160:163], a[228:231], a[148:151], v[160:163]\n"
"buffer_load_dwordx4 v204, s[32:35], s63 offen nt lds\n"
"v_mfma_f32_16x16x32_bf16 v[160:163], a[224:227], a[160:163], v[160:163]\n"
"ds_read_b64_tr_b16 a[218:219], v194 offset:30720\n"
"ds_read_b64_tr_b16 a[220:221], v194 offset:45056\n"
"v_mfma_f32_16x16x32_bf16 v[156:159], a[224:227], a[156:159], v[156:159]\n"
"ds_read_b64_tr_b16 a[222:223], v194 offset:47104\n"
"v_cvt_pk_bf16_f32 v43, v138, v139\n"
"v_mfma_f32_16x16x32_bf16 v[156:159], a[228:231], a[144:147], v[156:159]\n"
"v_mul_f32 v44, s62, v44\n"
"v_mul_f32 v45, s62, v45\n"
"v_mfma_f32_16x16x32_bf16 v[156:159], a[232:235], a[132:135], v[156:159]\n"
"v_mul_f32 v46, s62, v46\n"
"v_mul_f32 v47, s62, v47\n"
"v_mfma_f32_16x16x32_bf16 v[156:159], a[236:239], a[120:123], v[156:159]\n"
"s_waitcnt lgkmcnt(6)\n"
"v_cvt_pk_bf16_f32 v44, v44, v45\n"
"v_cvt_pk_bf16_f32 v45, v46, v47\n"
"v_mfma_f32_16x16x32_bf16 v[72:75], a[208:211], a[120:123], v[72:75]\n"
"s_waitcnt lgkmcnt(4)\n"
"v_mul_f32 v140, s62, v140\n"
"v_mul_f32 v141, s62, v141\n"
"v_mfma_f32_16x16x32_bf16 v[72:75], a[212:215], a[132:135], v[72:75]\n"
"s_waitcnt lgkmcnt(2)\n"
"v_mul_f32 v142, s62, v142\n"
"v_mul_f32 v143, s62, v143\n"
"v_mfma_f32_16x16x32_bf16 v[72:75], a[216:219], a[144:147], v[72:75]\n"
"s_waitcnt lgkmcnt(0)\n"
"v_cvt_pk_bf16_f32 v46, v140, v141\n"
"v_cvt_pk_bf16_f32 v47, v142, v143\n"
"v_mfma_f32_16x16x32_bf16 v[72:75], a[220:223], a[156:159], v[72:75]\n"
"v_mul_f32 v48, s62, v48\n"
"v_mul_f32 v49, s62, v49\n"
"v_mfma_f32_16x16x32_bf16 v[76:79], a[220:223], a[160:163], v[76:79]\n"
"ds_read_b64_tr_b16 a[224:225], v195 offset:45056\n"
"v_mul_f32 v50, s62, v50\n"
"v_mfma_f32_16x16x32_bf16 v[76:79], a[216:219], a[148:151], v[76:79]\n"
"s_add_u32 m0, s60, 20352\n"
"ds_read_b64_tr_b16 a[226:227], v195 offset:47104\n"
"ds_read_b64_tr_b16 a[228:229], v195 offset:28672\n"
"v_mfma_f32_16x16x32_bf16 v[76:79], a[212:215], a[136:139], v[76:79]\n"
"ds_read_b64_tr_b16 a[230:231], v195 offset:30720\n"
"ds_read_b64_tr_b16 a[232:233], v195 offset:12288\n"
"v_mfma_f32_16x16x32_bf16 v[76:79], a[208:211], a[124:127], v[76:79]\n"
"ds_read_b64_tr_b16 a[234:235], v195 offset:14336\n"
"ds_read_b64_tr_b16 a[236:237], v193 offset:61440\n"
"v_mfma_f32_16x16x32_bf16 v[80:83], a[208:211], a[128:131], v[80:83]\n"
"ds_read_b64_tr_b16 a[238:239], v193 offset:63488\n"
"v_mul_f32 v51, s62, v51\n"
"v_mfma_f32_16x16x32_bf16 v[80:83], a[212:215], a[140:143], v[80:83]\n"
"buffer_load_dwordx4 v204, s[32:35], s63 offen offset:128 nt lds\n"
"v_mfma_f32_16x16x32_bf16 v[80:83], a[216:219], a[152:155], v[80:83]\n"
"v_cvt_pk_bf16_f32 v48, v48, v49\n"
"v_cvt_pk_bf16_f32 v49, v50, v51\n"
"v_mfma_f32_16x16x32_bf16 v[80:83], a[220:223], a[164:167], v[80:83]\n"
"s_waitcnt lgkmcnt(6)\n"
"v_mul_f32 v144, s62, v144\n"
"v_mul_f32 v145, s62, v145\n"
"v_mfma_f32_16x16x32_bf16 v[176:179], a[224:227], a[164:167], v[176:179]\n"
"s_waitcnt lgkmcnt(4)\n"
"v_mul_f32 v146, s62, v146\n"
"v_mul_f32 v147, s62, v147\n"
"v_mfma_f32_16x16x32_bf16 v[176:179], a[228:231], a[152:155], v[176:179]\n"
"s_waitcnt lgkmcnt(2)\n"
"v_cvt_pk_bf16_f32 v50, v144, v145\n"
"v_cvt_pk_bf16_f32 v51, v146, v147\n"
"v_mfma_f32_16x16x32_bf16 v[176:179], a[232:235], a[140:143], v[176:179]\n"
"s_waitcnt lgkmcnt(0)\n"
"v_mul_f32 v52, s62, v52\n"
"v_mul_f32 v53, s62, v53\n"
"v_mfma_f32_16x16x32_bf16 v[176:179], a[236:239], a[128:131], v[176:179]\n"
"v_mul_f32 v54, s62, v54\n"
"v_mul_f32 v55, s62, v55\n"
"v_mfma_f32_16x16x32_bf16 v[172:175], a[236:239], a[124:127], v[172:175]\n"
"ds_read_b64_tr_b16 a[208:209], v192 offset:61952\n"
"v_cvt_pk_bf16_f32 v52, v52, v53\n"
"v_mfma_f32_16x16x32_bf16 v[172:175], a[232:235], a[136:139], v[172:175]\n"
"ds_read_b64_tr_b16 a[210:211], v192 offset:64000\n"
"ds_read_b64_tr_b16 a[212:213], v194 offset:12800\n"
"v_mfma_f32_16x16x32_bf16 v[172:175], a[228:231], a[148:151], v[172:175]\n"
"s_add_u32 m0, s60, 24320\n"
"ds_read_b64_tr_b16 a[214:215], v194 offset:14848\n"
"ds_read_b64_tr_b16 a[216:217], v194 offset:29184\n"
"v_mfma_f32_16x16x32_bf16 v[172:175], a[224:227], a[160:163], v[172:175]\n"
"ds_read_b64_tr_b16 a[218:219], v194 offset:31232\n"
"ds_read_b64_tr_b16 a[220:221], v194 offset:45568\n"
"v_mfma_f32_16x16x32_bf16 v[168:171], a[224:227], a[156:159], v[168:171]\n"
"ds_read_b64_tr_b16 a[222:223], v194 offset:47616\n"
"v_cvt_pk_bf16_f32 v53, v54, v55\n"
"v_mfma_f32_16x16x32_bf16 v[168:171], a[228:231], a[144:147], v[168:171]\n"
"buffer_load_dwordx4 v204, s[32:35], s63 offen offset:256 nt lds\n"
"v_mfma_f32_16x16x32_bf16 v[168:171], a[232:235], a[132:135], v[168:171]\n"
"v_mul_f32 v148, s62, v148\n"
"v_mul_f32 v149, s62, v149\n"
"v_mfma_f32_16x16x32_bf16 v[168:171], a[236:239], a[120:123], v[168:171]\n"
"s_waitcnt lgkmcnt(6)\n"
"v_mul_f32 v150, s62, v150\n"
"v_mul_f32 v151, s62, v151\n"
"v_mfma_f32_16x16x32_bf16 v[84:87], a[208:211], a[120:123], v[84:87]\n"
"s_waitcnt lgkmcnt(4)\n"
"v_cvt_pk_bf16_f32 v54, v148, v149\n"
"v_cvt_pk_bf16_f32 v55, v150, v151\n"
"v_mfma_f32_16x16x32_bf16 v[84:87], a[212:215], a[132:135], v[84:87]\n"
"s_waitcnt lgkmcnt(2)\n"
"v_mul_f32 v56, s62, v56\n"
"v_mul_f32 v57, s62, v57\n"
"v_mfma_f32_16x16x32_bf16 v[84:87], a[216:219], a[144:147], v[84:87]\n"
"s_waitcnt lgkmcnt(0)\n"
"v_mul_f32 v58, s62, v58\n"
"v_mul_f32 v59, s62, v59\n"
"v_mfma_f32_16x16x32_bf16 v[84:87], a[220:223], a[156:159], v[84:87]\n"
"v_cvt_pk_bf16_f32 v56, v56, v57\n"
"v_cvt_pk_bf16_f32 v57, v58, v59\n"
"v_mfma_f32_16x16x32_bf16 v[88:91], a[220:223], a[160:163], v[88:91]\n"
"ds_read_b64_tr_b16 a[224:225], v195 offset:45568\n"
"v_mul_f32 v152, s62, v152\n"
"v_mfma_f32_16x16x32_bf16 v[88:91], a[216:219], a[148:151], v[88:91]\n"
"ds_read_b64_tr_b16 a[226:227], v195 offset:47616\n"
"ds_read_b64_tr_b16 a[228:229], v195 offset:29184\n"
"v_mfma_f32_16x16x32_bf16 v[88:91], a[212:215], a[136:139], v[88:91]\n"
"s_waitcnt vmcnt(15)\n"
"ds_read_b64_tr_b16 a[230:231], v195 offset:31232\n"
"ds_read_b64_tr_b16 a[232:233], v195 offset:12800\n"
"v_mfma_f32_16x16x32_bf16 v[88:91], a[208:211], a[124:127], v[88:91]\n"
"s_add_u32 m0, s60, 28288\n"
"ds_read_b64_tr_b16 a[234:235], v195 offset:14848\n"
"ds_read_b64_tr_b16 a[236:237], v193 offset:61952\n"
"v_mfma_f32_16x16x32_bf16 v[92:95], a[208:211], a[128:131], v[92:95]\n"
"s_barrier\n"
"ds_read_b64_tr_b16 a[238:239], v193 offset:64000\n"
"v_mul_f32 v153, s62, v153\n"
"v_mfma_f32_16x16x32_bf16 v[92:95], a[212:215], a[140:143], v[92:95]\n"
"buffer_load_dwordx4 v204, s[32:35], s63 offen offset:384 nt lds\n"
"v_mfma_f32_16x16x32_bf16 v[92:95], a[216:219], a[152:155], v[92:95]\n"
"v_mul_f32 v154, s62, v154\n"
"v_mul_f32 v155, s62, v155\n"
"v_mfma_f32_16x16x32_bf16 v[92:95], a[220:223], a[164:167], v[92:95]\n"
"s_waitcnt lgkmcnt(6)\n"
"v_cvt_pk_bf16_f32 v58, v152, v153\n"
"v_cvt_pk_bf16_f32 v59, v154, v155\n"
"v_mfma_f32_16x16x32_bf16 v[188:191], a[224:227], a[164:167], v[188:191]\n"
"s_waitcnt lgkmcnt(4)\n"
"v_mul_f32 v60, s62, v60\n"
"v_mul_f32 v61, s62, v61\n"
"v_mfma_f32_16x16x32_bf16 v[188:191], a[228:231], a[152:155], v[188:191]\n"
"s_waitcnt lgkmcnt(2)\n"
"v_readfirstlane_b32 s56, v212\n"
"v_readfirstlane_b32 s57, v213\n"
"s_nop 0\n"
"s_and_b32 s56, s56, 0xffff0000\n"
"s_add_u32 s56, s56, 0x9e370000\n"
"s_add_u32 s57, s57, 0x7f4a7c15\n"
"v_mfma_f32_16x16x32_bf16 v[188:191], a[232:235], a[140:143], v[188:191]\n"
"s_waitcnt lgkmcnt(0)\n"
"v_mul_f32 v62, s62, v62\n"
"v_mul_f32 v63, s62, v63\n"
"v_mfma_f32_16x16x32_bf16 v[188:191], a[236:239], a[128:131], v[188:191]\n"
"v_cvt_pk_bf16_f32 v60, v60, v61\n"
"v_cvt_pk_bf16_f32 v61, v62, v63\n"
"v_mfma_f32_16x16x32_bf16 v[184:187], a[236:239], a[124:127], v[184:187]\n"
"ds_read_b64 a[208:209], v200 offset:49152\n"
"v_mul_f32 v156, s62, v156\n"
"v_mfma_f32_16x16x32_bf16 v[184:187], a[232:235], a[136:139], v[184:187]\n"
"ds_read_b64 a[210:211], v201 offset:49152\n"
"v_mul_f32 v157, s62, v157\n"
"v_mfma_f32_16x16x32_bf16 v[184:187], a[228:231], a[148:151], v[184:187]\n"
"v_mul_f32 v158, s62, v158\n"
"v_mul_f32 v159, s62, v159\n"
"v_mfma_f32_16x16x32_bf16 v[184:187], a[224:227], a[160:163], v[184:187]\n"
"s_mov_b32 s63, 49152\n"
"ds_read_b64 a[212:213], v200 offset:49664\n"
"v_cvt_pk_bf16_f32 v62, v156, v157\n"
"v_mfma_f32_16x16x32_bf16 v[180:183], a[224:227], a[156:159], v[180:183]\n"
"s_add_u32 m0, s60, 32768\n"
"ds_read_b64 a[214:215], v201 offset:49664\n"
"v_cvt_pk_bf16_f32 v63, v158, v159\n"
"v_mfma_f32_16x16x32_bf16 v[180:183], a[228:231], a[144:147], v[180:183]\n"
"buffer_load_dwordx4 v205, s[28:31], s63 offen nt lds\n"
"v_mfma_f32_16x16x32_bf16 v[180:183], a[232:235], a[132:135], v[180:183]\n"
"ds_read_b64 a[216:217], v200 offset:53248\n"
"v_mul_f32 v64, s62, v64\n"
"v_mfma_f32_16x16x32_bf16 v[180:183], a[236:239], a[120:123], v[180:183]\n"
"s_waitcnt lgkmcnt(3)\n"
"ds_read_b64 a[218:219], v201 offset:53248\n"
"v_mul_f32 v65, s62, v65\n"
"v_mfma_f32_16x16x32_bf16 v[96:99], a[208:211], v[0:3], 0\n"
"v_mul_f32 v66, s62, v66\n"
"v_mul_f32 v67, s62, v67\n"
"v_mfma_f32_16x16x32_bf16 v[100:103], a[208:211], v[4:7], 0\n"
"ds_read_b64 a[220:221], v200 offset:53760\n"
"v_cvt_pk_bf16_f32 v64, v64, v65\n"
"v_mfma_f32_16x16x32_bf16 v[104:107], a[208:211], v[8:11], 0\n"
"s_waitcnt lgkmcnt(3)\n"
"ds_read_b64 a[222:223], v201 offset:53760\n"
"v_cvt_pk_bf16_f32 v65, v66, v67\n"
"v_mfma_f32_16x16x32_bf16 v[96:99], a[212:215], v[12:15], v[96:99]\n"
"v_mul_f32 v160, s62, v160\n"
"v_mul_f32 v161, s62, v161\n"
"v_mfma_f32_16x16x32_bf16 v[100:103], a[212:215], v[16:19], v[100:103]\n"
"ds_read_b64 a[224:225], v200 offset:57344\n"
"v_mul_f32 v162, s62, v162\n"
"v_mfma_f32_16x16x32_bf16 v[104:107], a[212:215], v[20:23], v[104:107]\n"
"s_waitcnt lgkmcnt(3)\n"
"ds_read_b64 a[226:227], v201 offset:57344\n"
"v_mul_f32 v163, s62, v163\n"
"v_mfma_f32_16x16x32_bf16 v[96:99], a[216:219], v[24:27], v[96:99]\n"
"v_cvt_pk_bf16_f32 v66, v160, v161\n"
"v_cvt_pk_bf16_f32 v67, v162, v163\n"
"v_mfma_f32_16x16x32_bf16 v[100:103], a[216:219], v[28:31], v[100:103]\n"
"ds_read_b64 a[228:229], v200 offset:57856\n"
"v_mul_f32 v68, s62, v68\n"
"v_mfma_f32_16x16x32_bf16 v[104:107], a[216:219], v[32:35], v[104:107]\n"
"s_waitcnt lgkmcnt(3)\n"
"ds_read_b64 a[230:231], v201 offset:57856\n"
"s_add_u32 m0, s60, 36736\n"
"v_mfma_f32_16x16x32_bf16 v[96:99], a[220:223], v[36:39], v[96:99]\n"
"buffer_load_dwordx4 v205, s[28:31], s63 offen offset:128 nt lds\n"
"v_mfma_f32_16x16x32_bf16 v[100:103], a[220:223], v[40:43], v[100:103]\n"
"ds_read_b64 a[232:233], v200 offset:61440\n"
"v_mul_f32 v69, s62, v69\n"
"v_mfma_f32_16x16x32_bf16 v[104:107], a[220:223], v[44:47], v[104:107]\n"
"s_waitcnt lgkmcnt(3)\n"
"ds_read_b64 a[234:235], v201 offset:61440\n"
"v_mul_f32 v70, s62, v70\n"
"v_mfma_f32_16x16x32_bf16 v[96:99], a[224:227], v[48:51], v[96:99]\n"
"v_mul_f32 v71, s62, v71\n"
"v_cvt_pk_bf16_f32 v68, v68, v69\n"
"v_mfma_f32_16x16x32_bf16 v[100:103], a[224:227], v[52:55], v[100:103]\n"
"ds_read_b64 a[236:237], v200 offset:61952\n"
"v_cvt_pk_bf16_f32 v69, v70, v71\n"
"v_mfma_f32_16x16x32_bf16 v[104:107], a[224:227], v[56:59], v[104:107]\n"
"s_waitcnt lgkmcnt(3)\n"
"ds_read_b64 a[238:239], v201 offset:61952\n"
"v_mul_f32 v164, s62, v164\n"
"v_mfma_f32_16x16x32_bf16 v[96:99], a[228:231], v[60:63], v[96:99]\n"
"v_mul_f32 v165, s62, v165\n"
"v_mul_f32 v166, s62, v166\n"
"v_mfma_f32_16x16x32_bf16 v[100:103], a[228:231], v[64:67], v[100:103]\n"
"v_mul_f32 v167, s62, v167\n"
"v_cvt_pk_bf16_f32 v70, v164, v165\n"
"v_cvt_pk_bf16_f32 v71, v166, v167\n"
"ds_read_b64 a[208:209], v200 offset:51200\n"
"s_nop 0\n"
"v_mfma_f32_16x16x32_bf16 v[104:107], a[228:231], v[68:71], v[104:107]\n"
"s_waitcnt lgkmcnt(3)\n"
"v_mul_f32 v72, s62, v72\n"
"v_mul_f32 v73, s62, v73\n"
"v_mul_f32 v74, s62, v74\n"
"v_mul_f32 v75, s62, v75\n"
"v_cvt_pk_bf16_f32 v72, v72, v73\n"
"v_cvt_pk_bf16_f32 v73, v74, v75\n"
"v_mul_f32 v168, s62, v168\n"
"v_mul_f32 v169, s62, v169\n"
"v_mul_f32 v170, s62, v170\n"
"v_mul_f32 v171, s62, v171\n"
"v_cvt_pk_bf16_f32 v74, v168, v169\n"
"v_cvt_pk_bf16_f32 v75, v170, v171\n"
"ds_read_b64 a[210:211], v201 offset:51200\n"
"s_nop 0\n"
"v_mfma_f32_16x16x32_bf16 v[96:99], a[232:235], v[72:75], v[96:99]\n"
"v_mul_f32 v76, s62, v76\n"
"v_mul_f32 v77, s62, v77\n"
"v_mul_f32 v78, s62, v78\n"
"v_mul_f32 v79, s62, v79\n"
"v_cvt_pk_bf16_f32 v76, v76, v77\n"
"v_cvt_pk_bf16_f32 v77, v78, v79\n"
"v_mul_f32 v172, s62, v172\n"
"v_mul_f32 v173, s62, v173\n"
"v_mul_f32 v174, s62, v174\n"
"v_mul_f32 v175, s62, v175\n"
"v_cvt_pk_bf16_f32 v78, v172, v173\n"
"v_cvt_pk_bf16_f32 v79, v174, v175\n"
"s_nop 1\n"
"v_mfma_f32_16x16x32_bf16 v[100:103], a[232:235], v[76:79], v[100:103]\n"
"v_mul_f32 v80, s62, v80\n"
"v_mul_f32 v81, s62, v81\n"
"v_mul_f32 v82, s62, v82\n"
"v_mul_f32 v83, s62, v83\n"
"v_cvt_pk_bf16_f32 v80, v80, v81\n"
"v_cvt_pk_bf16_f32 v81, v82, v83\n"
"v_mul_f32 v176, s62, v176\n"
"v_mul_f32 v177, s62, v177\n"
"v_mul_f32 v178, s62, v178\n"
"v_mul_f32 v179, s62, v179\n"
"v_cvt_pk_bf16_f32 v82, v176, v177\n"
"v_cvt_pk_bf16_f32 v83, v178, v179\n"
"ds_read_b64 a[212:213], v200 offset:51712\n"
"s_nop 0\n"
"v_mfma_f32_16x16x32_bf16 v[104:107], a[232:235], v[80:83], v[104:107]\n"
"s_waitcnt lgkmcnt(3)\n"
"v_mul_f32 v84, s62, v84\n"
"v_mul_f32 v85, s62, v85\n"
"v_mul_f32 v86, s62, v86\n"
"v_mul_f32 v87, s62, v87\n"
"v_cvt_pk_bf16_f32 v84, v84, v85\n"
"v_cvt_pk_bf16_f32 v85, v86, v87\n"
"v_mul_f32 v180, s62, v180\n"
"v_mul_f32 v181, s62, v181\n"
"v_mul_f32 v182, s62, v182\n"
"v_mul_f32 v183, s62, v183\n"
"v_cvt_pk_bf16_f32 v86, v180, v181\n"
"v_cvt_pk_bf16_f32 v87, v182, v183\n"
"ds_read_b64 a[214:215], v201 offset:51712\n"
"s_nop 0\n"
"v_mfma_f32_16x16x32_bf16 v[96:99], a[236:239], v[84:87], v[96:99]\n"
"s_add_u32 m0, s60, 40704\n"
"v_mul_f32 v88, s62, v88\n"
"v_mul_f32 v89, s62, v89\n"
"v_mul_f32 v90, s62, v90\n"
"v_mul_f32 v91, s62, v91\n"
"v_cvt_pk_bf16_f32 v88, v88, v89\n"
"v_cvt_pk_bf16_f32 v89, v90, v91\n"
"v_mul_f32 v184, s62, v184\n"
"v_mul_f32 v185, s62, v185\n"
"v_mul_f32 v186, s62, v186\n"
"v_mul_f32 v187, s62, v187\n"
"v_cvt_pk_bf16_f32 v90, v184, v185\n"
"v_cvt_pk_bf16_f32 v91, v186, v187\n"
"s_nop 1\n"
"v_mfma_f32_16x16x32_bf16 v[100:103], a[236:239], v[88:91], v[100:103]\n"
"v_mul_f32 v92, s62, v92\n"
"v_mul_f32 v93, s62, v93\n"
"v_mul_f32 v94, s62, v94\n"
"v_mul_f32 v95, s62, v95\n"
"v_cvt_pk_bf16_f32 v92, v92, v93\n"
"v_cvt_pk_bf16_f32 v93, v94, v95\n"
"v_mul_f32 v188, s62, v188\n"
"ds_read_b64 a[216:217], v200 offset:55296\n"
"v_mul_f32 v189, s62, v189\n"
"v_mul_f32 v190, s62, v190\n"
"v_mul_f32 v191, s62, v191\n"
"v_cvt_pk_bf16_f32 v94, v188, v189\n"
"v_cvt_pk_bf16_f32 v95, v190, v191\n"
"s_nop 1\n"
"v_mfma_f32_16x16x32_bf16 v[104:107], a[236:239], v[92:95], v[104:107]\n"
"s_waitcnt lgkmcnt(3)\n"
"ds_read_b64 a[218:219], v201 offset:55296\n"
"s_mov_b32 s62, 0x47800000\n"
"v_mfma_f32_16x16x32_bf16 v[108:111], a[208:211], v[0:3], 0\n"
"buffer_load_dwordx4 v205, s[28:31], s63 offen offset:256 nt lds\n"
"v_mfma_f32_16x16x32_bf16 v[112:115], a[208:211], v[4:7], 0\n"
"ds_read_b64 a[220:221], v200 offset:55808\n"
"ds_read_b64 a[222:223], v201 offset:55808\n"
"v_mfma_f32_16x16x32_bf16 v[116:119], a[208:211], v[8:11], 0\n"
"s_waitcnt lgkmcnt(4)\n"
"ds_read_b64 a[224:225], v200 offset:59392\n"
"ds_read_b64 a[226:227], v201 offset:59392\n"
"v_mfma_f32_16x16x32_bf16 v[108:111], a[212:215], v[12:15], v[108:111]\n"
"ds_read_b64 a[228:229], v200 offset:59904\n"
"ds_read_b64 a[230:231], v201 offset:59904\n"
"v_mfma_f32_16x16x32_bf16 v[112:115], a[212:215], v[16:19], v[112:115]\n"
"ds_read_b64 a[232:233], v200 offset:63488\n"
"ds_read_b64 a[234:235], v201 offset:63488\n"
"v_mfma_f32_16x16x32_bf16 v[116:119], a[212:215], v[20:23], v[116:119]\n"
"s_waitcnt lgkmcnt(8)\n"
"ds_read_b64 a[236:237], v200 offset:64000\n"
"ds_read_b64 a[238:239], v201 offset:64000\n"
"v_mfma_f32_16x16x32_bf16 v[108:111], a[216:219], v[24:27], v[108:111]\n"
"s_waitcnt vmcnt(15)\n"
"s_barrier\n"
"ds_read_b64 a[208:209], v202 offset:0\n"
"v_mfma_f32_16x16x32_bf16 v[112:115], a[216:219], v[28:31], v[112:115]\n"
"ds_read_b64 a[210:211], v203 offset:0\n"
"ds_read_b64 a[212:213], v202 offset:512\n"
"v_mfma_f32_16x16x32_bf16 v[116:119], a[216:219], v[32:35], v[116:119]\n"
"s_waitcnt lgkmcnt(11)\n"
"ds_read_b64 a[214:215], v203 offset:512\n"
"ds_read_b64 a[216:217], v202 offset:4096\n"
"v_mfma_f32_16x16x32_bf16 v[108:111], a[220:223], v[36:39], v[108:111]\n"
"ds_read_b64 a[218:219], v203 offset:4096\n"
"v_mfma_f32_16x16x32_bf16 v[112:115], a[220:223], v[40:43], v[112:115]\n"
"s_add_u32 m0, s60, 44672\n"
"v_mfma_f32_16x16x32_bf16 v[116:119], a[220:223], v[44:47], v[116:119]\n"
"s_waitcnt lgkmcnt(12)\n"
"buffer_load_dwordx4 v205, s[28:31], s63 offen offset:384 nt lds\n"
"v_mfma_f32_16x16x32_bf16 v[108:111], a[224:227], v[48:51], v[108:111]\n"
"ds_read_b64 a[220:221], v202 offset:4608\n"
"ds_read_b64 a[222:223], v203 offset:4608\n"
"v_mfma_f32_16x16x32_bf16 v[112:115], a[224:227], v[52:55], v[112:115]\n"
"v_mfma_f32_16x16x32_bf16 v[116:119], a[224:227], v[56:59], v[116:119]\n"
"s_waitcnt lgkmcnt(12)\n"
"ds_read_b64 a[224:225], v202 offset:8192\n"
"ds_read_b64 a[226:227], v203 offset:8192\n"
"v_mfma_f32_16x16x32_bf16 v[108:111], a[228:231], v[60:63], v[108:111]\n"
"v_mfma_f32_16x16x32_bf16 v[112:115], a[228:231], v[64:67], v[112:115]\n"
"v_mfma_f32_16x16x32_bf16 v[116:119], a[228:231], v[68:71], v[116:119]\n"
"s_waitcnt lgkmcnt(12)\n"
"ds_read_b64 a[228:229], v202 offset:8704\n"
"ds_read_b64 a[230:231], v203 offset:8704\n"
"v_mfma_f32_16x16x32_bf16 v[108:111], a[232:235], v[72:75], v[108:111]\n"
"v_mfma_f32_16x16x32_bf16 v[112:115], a[232:235], v[76:79], v[112:115]\n"
"v_mfma_f32_16x16x32_bf16 v[116:119], a[232:235], v[80:83], v[116:119]\n"
"s_waitcnt lgkmcnt(12)\n"
"ds_read_b64 a[232:233], v202 offset:12288\n"
"ds_read_b64 a[234:235], v203 offset:12288\n"
"v_mfma_f32_16x16x32_bf16 v[108:111], a[236:239], v[84:87], v[108:111]\n"
"v_mfma_f32_16x16x32_bf16 v[112:115], a[236:239], v[88:91], v[112:115]\n"
"s_mov_b32 s63, 32768\n"
"v_mfma_f32_16x16x32_bf16 v[116:119], a[236:239], v[92:95], v[116:119]\n"
"s_waitcnt lgkmcnt(12)\n"
"s_add_u32 m0, s60, 49152\n"
"ds_read_b64 a[236:237], v202 offset:12800\n"
"v_mfma_f32_16x16x32_bf16 v[120:123], a[208:211], v[0:3], 0\n"
"s_waitcnt lgkmcnt(11)\n"
"buffer_load_dwordx4 v204, s[32:35], s63 offen nt lds\n"
"v_mfma_f32_16x16x32_bf16 v[120:123], a[212:215], v[12:15], v[120:123]\n"
"s_waitcnt lgkmcnt(9)\n"
"v_max3_f32 v220, v96, v97, v98\n"
"v_max3_f32 v222, v100, v101, v102\n"
"v_mfma_f32_16x16x32_bf16 v[120:123], a[216:219], v[24:27], v[120:123]\n"
"s_waitcnt lgkmcnt(7)\n"
"v_max3_f32 v224, v104, v105, v106\n"
"v_max3_f32 v221, v108, v109, v110\n"
"v_mfma_f32_16x16x32_bf16 v[120:123], a[220:223], v[36:39], v[120:123]\n"
"v_max3_f32 v223, v112, v113, v114\n"
"v_max3_f32 v225, v116, v117, v118\n"
"v_mfma_f32_16x16x32_bf16 v[124:127], a[220:223], v[40:43], 0\n"
"v_max3_f32 v220, v220, v99, v111\n"
"v_max3_f32 v222, v222, v103, v115\n"
"v_mfma_f32_16x16x32_bf16 v[124:127], a[216:219], v[28:31], v[124:127]\n"
"v_max3_f32 v224, v224, v107, v119\n"
"v_max_f32 v220, v220, v221\n"
"v_mfma_f32_16x16x32_bf16 v[124:127], a[212:215], v[16:19], v[124:127]\n"
"v_max_f32 v222, v222, v223\n"
"v_max_f32 v224, v224, v225\n"
"v_mfma_f32_16x16x32_bf16 v[124:127], a[208:211], v[4:7], v[124:127]\n"
"v_mov_b32 v221, v220\n"
"v_mov_b32 v223, v222\n"
"v_mfma_f32_16x16x32_bf16 v[128:131], a[208:211], v[8:11], 0\n"
"ds_read_b64 a[238:239], v203 offset:12800\n"
"v_mov_b32 v225, v224\n"
"v_mfma_f32_16x16x32_bf16 v[128:131], a[212:215], v[20:23], v[128:131]\n"
"v_permlane32_swap_b32 v220, v221\n"
"v_mfma_f32_16x16x32_bf16 v[128:131], a[216:219], v[32:35], v[128:131]\n"
"v_permlane32_swap_b32 v222, v223\n"
"v_mfma_f32_16x16x32_bf16 v[128:131], a[220:223], v[44:47], v[128:131]\n"
"s_waitcnt lgkmcnt(6)\n"
"s_add_u32 m0, s60, 53120\n"
"ds_read_b64 a[208:209], v202 offset:2048\n"
"v_mfma_f32_16x16x32_bf16 v[128:131], a[224:227], v[56:59], v[128:131]\n"
"s_waitcnt lgkmcnt(5)\n"
"buffer_load_dwordx4 v204, s[32:35], s63 offen offset:128 nt lds\n"
"v_mfma_f32_16x16x32_bf16 v[128:131], a[228:231], v[68:71], v[128:131]\n"
"s_waitcnt lgkmcnt(3)\n"
"v_permlane32_swap_b32 v224, v225\n"
"v_mfma_f32_16x16x32_bf16 v[128:131], a[232:235], v[80:83], v[128:131]\n"
"s_waitcnt lgkmcnt(1)\n"
"v_max_f32 v220, v220, v221\n"
"v_max_f32 v222, v222, v223\n"
"v_mfma_f32_16x16x32_bf16 v[128:131], a[236:239], v[92:95], v[128:131]\n"
"v_max_f32 v224, v224, v225\n"
"v_mov_b32 v221, v220\n"
"v_mfma_f32_16x16x32_bf16 v[124:127], a[236:239], v[88:91], v[124:127]\n"
"v_mov_b32 v223, v222\n"
"v_mov_b32 v225, v224\n"
"v_mfma_f32_16x16x32_bf16 v[124:127], a[232:235], v[76:79], v[124:127]\n"
"ds_read_b64 a[210:211], v203 offset:2048\n"
"ds_read_b64 a[212:213], v202 offset:2560\n"
"v_mfma_f32_16x16x32_bf16 v[124:127], a[228:231], v[64:67], v[124:127]\n"
"ds_read_b64 a[214:215], v203 offset:2560\n"
"ds_read_b64 a[216:217], v202 offset:6144\n"
"v_mfma_f32_16x16x32_bf16 v[124:127], a[224:227], v[52:55], v[124:127]\n"
"ds_read_b64 a[218:219], v203 offset:6144\n"
"ds_read_b64 a[220:221], v202 offset:6656\n"
"v_mfma_f32_16x16x32_bf16 v[120:123], a[224:227], v[48:51], v[120:123]\n"
"ds_read_b64 a[222:223], v203 offset:6656\n"
"ds_read_b64 a[224:225], v202 offset:10240\n"
"v_mfma_f32_16x16x32_bf16 v[120:123], a[228:231], v[60:63], v[120:123]\n"
"v_permlane16_swap_b32 v220, v221\n"
"v_mfma_f32_16x16x32_bf16 v[120:123], a[232:235], v[72:75], v[120:123]\n"
"v_permlane16_swap_b32 v222, v223\n"
"v_mfma_f32_16x16x32_bf16 v[120:123], a[236:239], v[84:87], v[120:123]\n"
"s_waitcnt lgkmcnt(7)\n"
"v_permlane16_swap_b32 v224, v225\n"
"v_mfma_f32_16x16x32_bf16 v[132:135], a[208:211], v[0:3], 0\n"
"s_waitcnt lgkmcnt(5)\n"
"s_add_u32 m0, s60, 57088\n"
"v_max_f32 v220, v220, v221\n"
"v_mfma_f32_16x16x32_bf16 v[132:135], a[212:215], v[12:15], v[132:135]\n"
"s_waitcnt lgkmcnt(3)\n"
"buffer_load_dwordx4 v204, s[32:35], s63 offen offset:256 nt lds\n"
"v_mfma_f32_16x16x32_bf16 v[132:135], a[216:219], v[24:27], v[132:135]\n"
"s_waitcnt lgkmcnt(1)\n"
"v_max_f32 v222, v222, v223\n"
"v_max_f32 v224, v224, v225\n"
"v_mfma_f32_16x16x32_bf16 v[132:135], a[220:223], v[36:39], v[132:135]\n"
"v_sub_f32 v208, 0xc1000000, v220\n"
"v_sub_f32 v212, 0xc1000000, v222\n"
"v_mfma_f32_16x16x32_bf16 v[136:139], a[220:223], v[40:43], 0\n"
"v_sub_f32 v216, 0xc1000000, v224\n"
"v_add_f32 v96, v208, v96\n"
"v_mfma_f32_16x16x32_bf16 v[136:139], a[216:219], v[28:31], v[136:139]\n"
"v_add_f32 v97, v208, v97\n"
"ds_read_b64 a[226:227], v203 offset:10240\n"
"v_mfma_f32_16x16x32_bf16 v[136:139], a[212:215], v[16:19], v[136:139]\n"
"s_waitcnt vmcnt(15)\n"
"v_exp_f32 v226, v96\n"
"v_mfma_f32_16x16x32_bf16 v[136:139], a[208:211], v[4:7], v[136:139]\n"
"s_barrier\n"
"v_exp_f32 v227, v97\n"
"v_mfma_f32_16x16x32_bf16 v[140:143], a[208:211], v[8:11], 0\n"
"ds_read_b64 a[228:229], v202 offset:10752\n"
"v_add_f32 v98, v208, v98\n"
"v_mfma_f32_16x16x32_bf16 v[140:143], a[212:215], v[20:23], v[140:143]\n"
"ds_read_b64 a[230:231], v203 offset:10752\n"
"ds_read_b64 a[232:233], v202 offset:14336\n"
"v_mfma_f32_16x16x32_bf16 v[140:143], a[216:219], v[32:35], v[140:143]\n"
"ds_read_b64 a[234:235], v203 offset:14336\n"
"ds_read_b64 a[236:237], v202 offset:14848\n"
"v_mfma_f32_16x16x32_bf16 v[140:143], a[220:223], v[44:47], v[140:143]\n"
"v_add_f32 v99, v208, v99\n"
"v_exp_f32 v220, v98\n"
"v_exp_f32 v221, v99\n"
"v_add_f32 v100, v212, v100\n"
"v_add_f32 v101, v212, v101\n"
"v_exp_f32 v222, v100\n"
"v_exp_f32 v223, v101\n"
"v_cvt_pk_bf16_f32 v144, v226, v227\n"
"v_cvt_pk_bf16_f32 v145, v220, v221\n"
"v_add_f32 v102, v212, v102\n"
"v_add_f32 v103, v212, v103\n"
"v_exp_f32 v224, v102\n"
"v_exp_f32 v225, v103\n"
"v_add_f32 v104, v216, v104\n"
"v_add_f32 v105, v216, v105\n"
"v_exp_f32 v226, v104\n"
"v_exp_f32 v227, v105\n"
"v_cvt_pk_bf16_f32 v148, v222, v223\n"
"v_cvt_pk_bf16_f32 v149, v224, v225\n"
"v_add_f32 v106, v216, v106\n"
"v_add_f32 v107, v216, v107\n"
"v_exp_f32 v220, v106\n"
"v_exp_f32 v221, v107\n"
"v_add_f32 v108, v208, v108\n"
"v_add_f32 v109, v208, v109\n"
"v_exp_f32 v222, v108\n"
"v_exp_f32 v223, v109\n"
"v_cvt_pk_bf16_f32 v152, v226, v227\n"
"v_cvt_pk_bf16_f32 v153, v220, v221\n"
"v_add_f32 v110, v208, v110\n"
"v_add_f32 v111, v208, v111\n"
"v_exp_f32 v224, v110\n"
"v_exp_f32 v225, v111\n"
"v_add_f32 v112, v212, v112\n"
"v_add_f32 v113, v212, v113\n"
"v_exp_f32 v226, v112\n"
"v_exp_f32 v227, v113\n"
"v_cvt_pk_bf16_f32 v146, v222, v223\n"
"v_cvt_pk_bf16_f32 v147, v224, v225\n"
"v_add_f32 v114, v212, v114\n"
"v_add_f32 v115, v212, v115\n"
"v_exp_f32 v220, v114\n"
"v_exp_f32 v221, v115\n"
"v_add_f32 v116, v216, v116\n"
"v_add_f32 v117, v216, v117\n"
"v_exp_f32 v222, v116\n"
"v_exp_f32 v223, v117\n"
"v_cvt_pk_bf16_f32 v150, v226, v227\n"
"v_cvt_pk_bf16_f32 v151, v220, v221\n"
"v_add_f32 v118, v216, v118\n"
"v_add_f32 v119, v216, v119\n"
"ds_read_b64 a[238:239], v203 offset:14848\n"
"v_exp_f32 v224, v118\n"
"v_exp_f32 v225, v119\n"
"v_cvt_pk_bf16_f32 v154, v222, v223\n"
"v_cvt_pk_bf16_f32 v155, v224, v225\n"
"v_mfma_f32_16x16x32_bf16 a[0:3], a[204:207], v[144:147], 0\n"
"ds_read_b64_tr_b16 a[208:209], v196 offset:16384\n"
"ds_read_b64_tr_b16 a[210:211], v196 offset:18432\n"
"v_mfma_f32_16x16x32_bf16 a[4:7], a[204:207], v[148:151], 0\n"
"s_add_u32 m0, s60, 61056\n"
"ds_read_b64_tr_b16 a[212:213], v197 offset:16384\n"
"ds_read_b64_tr_b16 a[214:215], v197 offset:18432\n"
"v_mfma_f32_16x16x32_bf16 a[8:11], a[204:207], v[152:155], 0\n"
"s_waitcnt lgkmcnt(10)\n"
"buffer_load_dwordx4 v204, s[32:35], s63 offen offset:384 nt lds\n"
"v_mfma_f32_16x16x32_bf16 v[140:143], a[224:227], v[56:59], v[140:143]\n"
"s_waitcnt lgkmcnt(8)\n"
"ds_read_b64_tr_b16 a[216:217], v196 offset:16896\n"
"ds_read_b64_tr_b16 a[218:219], v196 offset:18944\n"
"v_mfma_f32_16x16x32_bf16 v[140:143], a[228:231], v[68:71], v[140:143]\n"
"s_waitcnt lgkmcnt(8)\n"
"ds_read_b64_tr_b16 a[220:221], v197 offset:16896\n"
"ds_read_b64_tr_b16 a[222:223], v197 offset:18944\n"
"v_mfma_f32_16x16x32_bf16 v[140:143], a[232:235], v[80:83], v[140:143]\n"
"s_waitcnt lgkmcnt(8)\n"
"v_mov_b32 v209, v208\n"
"v_mov_b32 v210, v208\n"
"v_mfma_f32_16x16x32_bf16 v[140:143], a[236:239], v[92:95], v[140:143]\n"
"v_mov_b32 v211, v208\n"
"v_mov_b32 v213, v212\n"
"v_mfma_f32_16x16x32_bf16 v[136:139], a[236:239], v[88:91], v[136:139]\n"
"v_mov_b32 v214, v212\n"
"v_mov_b32 v215, v212\n"
"v_mfma_f32_16x16x32_bf16 v[136:139], a[232:235], v[76:79], v[136:139]\n"
"v_mov_b32 v217, v216\n"
"v_mov_b32 v218, v216\n"
"v_mfma_f32_16x16x32_bf16 v[136:139], a[228:231], v[64:67], v[136:139]\n"
"v_mov_b32 v219, v216\n"
"v_add_f32 v120, v208, v120\n"
"v_mfma_f32_16x16x32_bf16 v[136:139], a[224:227], v[52:55], v[136:139]\n"
"v_add_f32 v121, v208, v121\n"
"v_mfma_f32_16x16x32_bf16 v[132:135], a[224:227], v[48:51], v[132:135]\n"
"ds_read_b64_tr_b16 a[224:225], v196 offset:20480\n"
"ds_read_b64_tr_b16 a[226:227], v196 offset:22528\n"
"v_mfma_f32_16x16x32_bf16 v[132:135], a[228:231], v[60:63], v[132:135]\n"
"ds_read_b64_tr_b16 a[228:229], v197 offset:20480\n"
"ds_read_b64_tr_b16 a[230:231], v197 offset:22528\n"
"v_mfma_f32_16x16x32_bf16 v[132:135], a[232:235], v[72:75], v[132:135]\n"
"s_mov_b32 s63, 65536\n"
"ds_read_b64_tr_b16 a[232:233], v196 offset:20992\n"
"ds_read_b64_tr_b16 a[234:235], v196 offset:23040\n"
"v_mfma_f32_16x16x32_bf16 v[132:135], a[236:239], v[84:87], v[132:135]\n"
"s_waitcnt lgkmcnt(12)\n"
"s_add_u32 m0, s60, 65536\n"
"ds_read_b64_tr_b16 a[236:237], v197 offset:20992\n"
"s_nop 7\n"
"v_mfma_f32_16x16x32_bf16 v[168:171], a[208:211], v[144:147], 0\n"
"s_waitcnt lgkmcnt(11)\n"
"buffer_load_dwordx4 v205, s[28:31], s63 offen nt lds\n"
"v_mfma_f32_16x16x32_bf16 v[180:183], a[212:215], v[144:147], 0\n"
"s_waitcnt lgkmcnt(9)\n"
"ds_read_b64_tr_b16 a[238:239], v197 offset:23040\n"
"v_mfma_f32_16x16x32_bf16 v[228:231], a[216:219], v[144:147], 0\n"
"s_waitcnt lgkmcnt(8)\n"
"v_exp_f32 v226, v120\n"
"v_mfma_f32_16x16x32_bf16 v[240:243], a[220:223], v[144:147], 0\n"
"v_exp_f32 v227, v121\n"
"v_mfma_f32_16x16x32_bf16 v[244:247], a[220:223], v[148:151], 0\n"
"v_add_f32 v122, v208, v122\n"
"v_add_f32 v123, v208, v123\n"
"v_mfma_f32_16x16x32_bf16 v[232:235], a[216:219], v[148:151], 0\n"
"v_exp_f32 v220, v122\n"
"v_mfma_f32_16x16x32_bf16 v[184:187], a[212:215], v[148:151], 0\n"
"v_exp_f32 v221, v123\n"
"v_mfma_f32_16x16x32_bf16 v[172:175], a[208:211], v[148:151], 0\n"
"v_add_f32 v124, v212, v124\n"
"v_add_f32 v125, v212, v125\n"
"v_mfma_f32_16x16x32_bf16 v[176:179], a[208:211], v[152:155], 0\n"
"ds_read_b64_tr_b16 a[208:209], v196 offset:24576\n"
"ds_read_b64_tr_b16 a[210:211], v196 offset:26624\n"
"v_mfma_f32_16x16x32_bf16 v[188:191], a[212:215], v[152:155], 0\n"
"ds_read_b64_tr_b16 a[212:213], v197 offset:24576\n"
"ds_read_b64_tr_b16 a[214:215], v197 offset:26624\n"
"v_mfma_f32_16x16x32_bf16 v[236:239], a[216:219], v[152:155], 0\n"
"ds_read_b64_tr_b16 a[216:217], v196 offset:25088\n"
"ds_read_b64_tr_b16 a[218:219], v196 offset:27136\n"
"v_mfma_f32_16x16x32_bf16 v[248:251], a[220:223], v[152:155], 0\n"
"s_waitcnt lgkmcnt(12)\n"
"s_add_u32 m0, s60, 69504\n"
"ds_read_b64_tr_b16 a[220:221], v197 offset:25088\n"
"v_mfma_f32_16x16x32_bf16 a[56:59], a[224:227], v[152:155], 0\n"
"s_waitcnt lgkmcnt(11)\n"
"buffer_load_dwordx4 v205, s[28:31], s63 offen offset:128 nt lds\n"
"v_mfma_f32_16x16x32_bf16 a[68:71], a[228:231], v[152:155], 0\n"
"s_waitcnt lgkmcnt(9)\n"
"ds_read_b64_tr_b16 a[222:223], v197 offset:27136\n"
"v_mfma_f32_16x16x32_bf16 a[80:83], a[232:235], v[152:155], 0\n"
"s_waitcnt lgkmcnt(8)\n"
"v_exp_f32 v222, v124\n"
"v_mfma_f32_16x16x32_bf16 a[92:95], a[236:239], v[152:155], 0\n"
"v_exp_f32 v223, v125\n"
"v_mfma_f32_16x16x32_bf16 a[88:91], a[236:239], v[148:151], 0\n"
"v_cvt_pk_bf16_f32 v156, v226, v227\n"
"v_cvt_pk_bf16_f32 v157, v220, v221\n"
"v_mfma_f32_16x16x32_bf16 a[76:79], a[232:235], v[148:151], 0\n"
"v_add_f32 v126, v212, v126\n"
"v_add_f32 v127, v212, v127\n"
"v_mfma_f32_16x16x32_bf16 a[64:67], a[228:231], v[148:151], 0\n"
"v_exp_f32 v224, v126\n"
"v_mfma_f32_16x16x32_bf16 a[52:55], a[224:227], v[148:151], 0\n"
"v_exp_f32 v225, v127\n"
"s_nop 7\n"
"v_mfma_f32_16x16x32_bf16 a[48:51], a[224:227], v[144:147], 0\n"
"ds_read_b64_tr_b16 a[224:225], v196 offset:28672\n"
"ds_read_b64_tr_b16 a[226:227], v196 offset:30720\n"
"v_mfma_f32_16x16x32_bf16 a[60:63], a[228:231], v[144:147], 0\n"
"ds_read_b64_tr_b16 a[228:229], v197 offset:28672\n"
"ds_read_b64_tr_b16 a[230:231], v197 offset:30720\n"
"v_mfma_f32_16x16x32_bf16 a[72:75], a[232:235], v[144:147], 0\n"
"ds_read_b64_tr_b16 a[232:233], v196 offset:29184\n"
"ds_read_b64_tr_b16 a[234:235], v196 offset:31232\n"
"v_mfma_f32_16x16x32_bf16 a[84:87], a[236:239], v[144:147], 0\n"
"s_waitcnt lgkmcnt(12)\n"
"ds_read_b64_tr_b16 a[236:237], v197 offset:29184\n"
"ds_read_b64_tr_b16 a[238:239], v197 offset:31232\n"
"v_mfma_f32_16x16x32_bf16 a[96:99], a[208:211], v[144:147], 0\n"
"s_waitcnt lgkmcnt(12)\n"
"s_add_u32 m0, s60, 73472\n"
"v_add_f32 v128, v216, v128\n"
"v_mfma_f32_16x16x32_bf16 a[108:111], a[212:215], v[144:147], 0\n"
"s_waitcnt lgkmcnt(10)\n"
"buffer_load_dwordx4 v205, s[28:31], s63 offen offset:256 nt lds\n"
"v_mfma_f32_16x16x32_bf16 a[120:123], a[216:219], v[144:147], 0\n"
"s_waitcnt lgkmcnt(8)\n"
"v_add_f32 v129, v216, v129\n"
"v_mfma_f32_16x16x32_bf16 a[132:135], a[220:223], v[144:147], 0\n"
"v_exp_f32 v226, v128\n"
"v_mfma_f32_16x16x32_bf16 a[136:139], a[220:223], v[148:151], 0\n"
"v_exp_f32 v227, v129\n"
"v_mfma_f32_16x16x32_bf16 a[124:127], a[216:219], v[148:151], 0\n"
"v_cvt_pk_bf16_f32 v160, v222, v223\n"
"v_cvt_pk_bf16_f32 v161, v224, v225\n"
"v_mfma_f32_16x16x32_bf16 a[112:115], a[212:215], v[148:151], 0\n"
"s_waitcnt vmcnt(15)\n"
"s_barrier\n"
"v_add_f32 v130, v216, v130\n"
"v_mfma_f32_16x16x32_bf16 a[100:103], a[208:211], v[148:151], 0\n"
"v_add_f32 v131, v216, v131\n"
"v_mfma_f32_16x16x32_bf16 a[104:107], a[208:211], v[152:155], 0\n"
"ds_read_b64 a[208:209], v198 offset:0\n"
"ds_read_b64 a[210:211], v199 offset:0\n"
"v_mfma_f32_16x16x32_bf16 a[116:119], a[212:215], v[152:155], 0\n"
"ds_read_b64 a[212:213], v198 offset:512\n"
"ds_read_b64 a[214:215], v199 offset:512\n"
"v_mfma_f32_16x16x32_bf16 a[128:131], a[216:219], v[152:155], 0\n"
"ds_read_b64 a[216:217], v198 offset:4096\n"
"ds_read_b64 a[218:219], v199 offset:4096\n"
"v_mfma_f32_16x16x32_bf16 a[140:143], a[220:223], v[152:155], 0\n"
"s_waitcnt lgkmcnt(12)\n"
"ds_read_b64 a[220:221], v198 offset:4608\n"
"ds_read_b64 a[222:223], v199 offset:4608\n"
"v_mfma_f32_16x16x32_bf16 a[152:155], a[224:227], v[152:155], 0\n"
"s_waitcnt lgkmcnt(12)\n"
"v_exp_f32 v220, v130\n"
"v_mfma_f32_16x16x32_bf16 a[164:167], a[228:231], v[152:155], 0\n"
"s_waitcnt lgkmcnt(10)\n"
"s_add_u32 m0, s60, 77440\n"
"v_mfma_f32_16x16x32_bf16 a[176:179], a[232:235], v[152:155], 0\n"
"s_waitcnt lgkmcnt(8)\n"
"buffer_load_dwordx4 v205, s[28:31], s63 offen offset:384 nt lds\n"
"v_mfma_f32_16x16x32_bf16 a[188:191], a[236:239], v[152:155], 0\n"
"v_exp_f32 v221, v131\n"
"s_nop 7\n"
"v_mfma_f32_16x16x32_bf16 a[184:187], a[236:239], v[148:151], 0\n"
"v_add_f32 v132, v208, v132\n"
"v_add_f32 v133, v208, v133\n"
"v_mfma_f32_16x16x32_bf16 a[172:175], a[232:235], v[148:151], 0\n"
"v_exp_f32 v222, v132\n"
"v_mfma_f32_16x16x32_bf16 a[160:163], a[228:231], v[148:151], 0\n"
"v_exp_f32 v223, v133\n"
"v_mfma_f32_16x16x32_bf16 a[148:151], a[224:227], v[148:151], 0\n"
"v_cvt_pk_bf16_f32 v164, v226, v227\n"
"v_cvt_pk_bf16_f32 v165, v220, v221\n"
"v_mfma_f32_16x16x32_bf16 a[144:147], a[224:227], v[144:147], 0\n"
"ds_read_b64 a[224:225], v198 offset:8192\n"
"ds_read_b64 a[226:227], v199 offset:8192\n"
"v_mfma_f32_16x16x32_bf16 a[156:159], a[228:231], v[144:147], 0\n"
"ds_read_b64 a[228:229], v198 offset:8704\n"
"ds_read_b64 a[230:231], v199 offset:8704\n"
"v_mfma_f32_16x16x32_bf16 a[168:171], a[232:235], v[144:147], 0\n"
"ds_read_b64 a[232:233], v198 offset:12288\n"
"ds_read_b64 a[234:235], v199 offset:12288\n"
"v_mfma_f32_16x16x32_bf16 a[180:183], a[236:239], v[144:147], 0\n"
"s_waitcnt lgkmcnt(12)\n"
"ds_read_b64 a[236:237], v198 offset:12800\n"
"ds_read_b64 a[238:239], v199 offset:12800\n"
"v_mfma_f32_16x16x32_bf16 v[96:99], a[208:211], v[0:3], v[208:211]\n"
"s_waitcnt lgkmcnt(12)\n"
"v_add_f32 v134, v208, v134\n"
"v_add_f32 v135, v208, v135\n"
"v_mfma_f32_16x16x32_bf16 v[96:99], a[212:215], v[12:15], v[96:99]\n"
"s_waitcnt lgkmcnt(10)\n"
"s_mov_b32 s63, 49152\n"
"s_cmp_eq_u32 %2, 3\n"
"s_cselect_b32 s20, 0x40000000, 0\n"
"v_mfma_f32_16x16x32_bf16 v[96:99], a[216:219], v[24:27], v[96:99]\n"
"s_waitcnt lgkmcnt(8)\n"
"s_add_u32 m0, s60, 81920\n"
"s_mov_b32 s21, s20\n"
"v_mfma_f32_16x16x32_bf16 v[96:99], a[220:223], v[36:39], v[96:99]\n"
"s_mul_i32 s70, s101, 3\n"
"buffer_load_dwordx4 v204, s[32:35], s63 offen nt lds\n"
"v_mfma_f32_16x16x32_bf16 v[100:103], a[220:223], v[40:43], v[212:215]\n"
"s_add_u32 s70, s70, %2\n"
"v_exp_f32 v224, v134\n"
"v_mfma_f32_16x16x32_bf16 v[100:103], a[216:219], v[28:31], v[100:103]\n"
"s_mul_i32 s72, s70, 3584\n"
"v_exp_f32 v225, v135\n"
"v_mfma_f32_16x16x32_bf16 v[100:103], a[212:215], v[16:19], v[100:103]\n"
"s_add_u32 s21, s21, s72\n"
"v_add_f32 v136, v212, v136\n"
"v_add_f32 v137, v212, v137\n"
"v_mfma_f32_16x16x32_bf16 v[100:103], a[208:211], v[4:7], v[100:103]\n"
"s_lshl_b32 s72, s70, 8\n"
"v_exp_f32 v226, v136\n"
"v_mfma_f32_16x16x32_bf16 v[104:107], a[208:211], v[8:11], v[216:219]\n"
"s_add_u32 s20, s20, s72\n"
"ds_read_b64 a[208:209], v198 offset:2048\n"
"ds_read_b64 a[210:211], v199 offset:2048\n"
"v_mfma_f32_16x16x32_bf16 v[104:107], a[212:215], v[20:23], v[104:107]\n"
"s_lshr_b32 s71, s101, 2\n"
"ds_read_b64 a[212:213], v198 offset:2560\n"
"ds_read_b64 a[214:215], v199 offset:2560\n"
"v_mfma_f32_16x16x32_bf16 v[104:107], a[216:219], v[32:35], v[104:107]\n"
"s_mul_i32 s72, s71, 111616\n"
"ds_read_b64 a[216:217], v198 offset:6144\n"
"ds_read_b64 a[218:219], v199 offset:6144\n"
"v_mfma_f32_16x16x32_bf16 v[104:107], a[220:223], v[44:47], v[104:107]\n"
"s_waitcnt lgkmcnt(12)\n"
"ds_read_b64 a[220:221], v198 offset:6656\n"
"ds_read_b64 a[222:223], v199 offset:6656\n"
"s_nop 7\n"
"v_mfma_f32_16x16x32_bf16 v[104:107], a[224:227], v[56:59], v[104:107]\n"
"s_waitcnt lgkmcnt(12)\n"
"v_exp_f32 v227, v137\n"
"v_mfma_f32_16x16x32_bf16 v[104:107], a[228:231], v[68:71], v[104:107]\n"
"s_waitcnt lgkmcnt(10)\n"
"v_cvt_pk_bf16_f32 v158, v222, v223\n"
"v_cvt_pk_bf16_f32 v159, v224, v225\n"
"v_mfma_f32_16x16x32_bf16 v[104:107], a[232:235], v[80:83], v[104:107]\n"
"s_waitcnt lgkmcnt(8)\n"
"s_add_u32 m0, s60, 85888\n"
"v_add_f32 v138, v212, v138\n"
"v_mfma_f32_16x16x32_bf16 v[104:107], a[236:239], v[92:95], v[104:107]\n"
"s_add_u32 s20, s20, s72\n"
"buffer_load_dwordx4 v204, s[32:35], s63 offen offset:128 nt lds\n"
"v_mfma_f32_16x16x32_bf16 v[100:103], a[236:239], v[88:91], v[100:103]\n"
"s_lshl_b32 s22, s101, 7\n"
"v_add_f32 v139, v212, v139\n"
"s_cmp_eq_u32 %2, 3\n"
"s_cselect_b32 s22, s22, 0x40000000\n"
"v_mfma_f32_16x16x32_bf16 v[100:103], a[232:235], v[76:79], v[100:103]\n"
"s_cmp_eq_u32 s22, 0\n"
"s_cselect_b32 s23, 2048, 0x40000000\n"
"v_exp_f32 v220, v138\n"
"v_mfma_f32_16x16x32_bf16 v[100:103], a[228:231], v[64:67], v[100:103]\n"
"s_lshl_b32 s71, s68, 13\n"
"s_add_u32 s40, s40, s71\n"
"s_addc_u32 s41, s41, 0\n"
"v_exp_f32 v221, v139\n"
"v_mfma_f32_16x16x32_bf16 v[100:103], a[224:227], v[52:55], v[100:103]\n"
"s_lshl_b32 s68, s68, 3\n"
"s_add_u32 s68, s68, s56\n"
"v_add_f32 v140, v216, v140\n"
"v_add_f32 v141, v216, v141\n"
"v_mfma_f32_16x16x32_bf16 v[96:99], a[224:227], v[48:51], v[96:99]\n"
"ds_read_b64 a[224:225], v198 offset:10240\n"
"ds_read_b64 a[226:227], v199 offset:10240\n"
"v_mfma_f32_16x16x32_bf16 v[96:99], a[228:231], v[60:63], v[96:99]\n"
"v_exp_f32 v222, v140\n"
"v_mfma_f32_16x16x32_bf16 v[96:99], a[232:235], v[72:75], v[96:99]\n"
"ds_read_b64 a[228:229], v198 offset:10752\n"
"ds_read_b64 a[230:231], v199 offset:10752\n"
"v_mfma_f32_16x16x32_bf16 v[96:99], a[236:239], v[84:87], v[96:99]\n"
"s_waitcnt lgkmcnt(10)\n"
"v_exp_f32 v223, v141\n"
"v_mfma_f32_16x16x32_bf16 v[108:111], a[208:211], v[0:3], v[208:211]\n"
"s_waitcnt lgkmcnt(8)\n"
"ds_read_b64 a[232:233], v198 offset:14336\n"
"v_cvt_pk_bf16_f32 v162, v226, v227\n"
"v_mfma_f32_16x16x32_bf16 v[108:111], a[212:215], v[12:15], v[108:111]\n"
"s_waitcnt lgkmcnt(7)\n"
"ds_read_b64 a[234:235], v199 offset:14336\n"
"v_cvt_pk_bf16_f32 v163, v220, v221\n"
"v_mfma_f32_16x16x32_bf16 v[108:111], a[216:219], v[24:27], v[108:111]\n"
"s_waitcnt lgkmcnt(6)\n"
"ds_read_b64 a[236:237], v198 offset:14848\n"
"v_add_f32 v142, v216, v142\n"
"v_mfma_f32_16x16x32_bf16 v[108:111], a[220:223], v[36:39], v[108:111]\n"
"s_add_u32 m0, s60, 89856\n"
"v_add_f32 v143, v216, v143\n"
"ds_read_b64 a[238:239], v199 offset:14848\n"
"v_mfma_f32_16x16x32_bf16 v[112:115], a[220:223], v[40:43], v[212:215]\n"
"buffer_load_dwordx4 v204, s[32:35], s63 offen offset:256 nt lds\n"
"v_mfma_f32_16x16x32_bf16 v[112:115], a[216:219], v[28:31], v[112:115]\n"
"v_exp_f32 v224, v142\n"
"v_mfma_f32_16x16x32_bf16 v[112:115], a[212:215], v[16:19], v[112:115]\n"
"s_waitcnt vmcnt(15)\n"
"v_exp_f32 v225, v143\n"
"v_mfma_f32_16x16x32_bf16 v[112:115], a[208:211], v[4:7], v[112:115]\n"
"s_barrier\n"
"v_cvt_pk_bf16_f32 v166, v222, v223\n"
"v_cvt_pk_bf16_f32 v167, v224, v225\n"
"s_nop 7\n"
"v_mfma_f32_16x16x32_bf16 v[116:119], a[208:211], v[8:11], v[216:219]\n"
"ds_read_b64_tr_b16 a[208:209], v192 offset:16384\n"
"ds_read_b64_tr_b16 a[210:211], v192 offset:18432\n"
"v_mfma_f32_16x16x32_bf16 v[116:119], a[212:215], v[20:23], v[116:119]\n"
"ds_read_b64_tr_b16 a[212:213], v193 offset:16384\n"
"ds_read_b64_tr_b16 a[214:215], v193 offset:18432\n"
"v_mfma_f32_16x16x32_bf16 v[116:119], a[216:219], v[32:35], v[116:119]\n"
"ds_read_b64_tr_b16 a[216:217], v192 offset:16896\n"
"ds_read_b64_tr_b16 a[218:219], v192 offset:18944\n"
"v_mfma_f32_16x16x32_bf16 v[116:119], a[220:223], v[44:47], v[116:119]\n"
"ds_read_b64_tr_b16 a[220:221], v193 offset:16896\n"
"s_waitcnt lgkmcnt(14)\n"
"ds_read_b64_tr_b16 a[222:223], v193 offset:18944\n"
"v_mfma_f32_16x16x32_bf16 a[192:195], a[204:207], v[156:159], a[0:3]\n"
"v_mfma_f32_16x16x32_bf16 a[196:199], a[204:207], v[160:163], a[4:7]\n"
"v_mfma_f32_16x16x32_bf16 a[200:203], a[204:207], v[164:167], a[8:11]\n"
"s_waitcnt lgkmcnt(14)\n"
"v_mfma_f32_16x16x32_bf16 v[116:119], a[224:227], v[56:59], v[116:119]\n"
"s_waitcnt lgkmcnt(12)\n"
"v_mfma_f32_16x16x32_bf16 v[116:119], a[228:231], v[68:71], v[116:119]\n"
"s_waitcnt lgkmcnt(10)\n"
"s_add_u32 m0, s60, 93824\n"
"v_mfma_f32_16x16x32_bf16 v[116:119], a[232:235], v[80:83], v[116:119]\n"
"s_waitcnt lgkmcnt(8)\n"
"buffer_load_dwordx4 v204, s[32:35], s63 offen offset:384 nt lds\n"
"v_mfma_f32_16x16x32_bf16 v[116:119], a[236:239], v[92:95], v[116:119]\n"
"v_accvgpr_read_b32 v226, a192\n"
"v_accvgpr_read_b32 v227, a196\n"
"v_mfma_f32_16x16x32_bf16 v[112:115], a[236:239], v[88:91], v[112:115]\n"
"v_accvgpr_read_b32 v220, a200\n"
"v_add_f32 v226, v226, v227\n"
"v_mfma_f32_16x16x32_bf16 v[112:115], a[232:235], v[76:79], v[112:115]\n"
"v_add_f32 v226, v226, v220\n"
"v_cmp_ngt_f32 vcc, 0x47800000, v226\n"
"v_mfma_f32_16x16x32_bf16 v[112:115], a[228:231], v[64:67], v[112:115]\n"
"v_mfma_f32_16x16x32_bf16 v[112:115], a[224:227], v[52:55], v[112:115]\n"
"v_mfma_f32_16x16x32_bf16 v[108:111], a[224:227], v[48:51], v[108:111]\n"
"ds_read_b64_tr_b16 a[224:225], v192 offset:20480\n"
"ds_read_b64_tr_b16 a[226:227], v192 offset:22528\n"
"v_mfma_f32_16x16x32_bf16 v[108:111], a[228:231], v[60:63], v[108:111]\n"
"ds_read_b64_tr_b16 a[228:229], v193 offset:20480\n"
"ds_read_b64_tr_b16 a[230:231], v193 offset:22528\n"
"v_mfma_f32_16x16x32_bf16 v[108:111], a[232:235], v[72:75], v[108:111]\n"
"ds_read_b64_tr_b16 a[232:233], v192 offset:20992\n"
"ds_read_b64_tr_b16 a[234:235], v192 offset:23040\n"
"v_mfma_f32_16x16x32_bf16 v[108:111], a[236:239], v[84:87], v[108:111]\n"
"s_waitcnt lgkmcnt(12)\n"
"s_cbranch_vccnz Lstub1\n"
"Lback1:\n"
"ds_read_b64_tr_b16 a[236:237], v193 offset:20992\n"
"v_mfma_f32_16x16x32_bf16 v[168:171], a[208:211], v[156:159], v[168:171]\n"
"s_waitcnt lgkmcnt(11)\n"
"ds_read_b64_tr_b16 a[238:239], v193 offset:23040\n"
"s_nop 7\n"
"v_mfma_f32_16x16x32_bf16 v[180:183], a[212:215], v[156:159], v[180:183]\n"
"s_waitcnt lgkmcnt(10)\n"
"s_mov_b32 s63, 81920\n"
"v_mfma_f32_16x16x32_bf16 v[228:231], a[216:219], v[156:159], v[228:231]\n"
"s_waitcnt lgkmcnt(8)\n"
"s_add_u32 m0, s60, 98304\n"
"v_mfma_f32_16x16x32_bf16 v[240:243], a[220:223], v[156:159], v[240:243]\n"
"buffer_load_dwordx4 v205, s[28:31], s63 offen nt lds\n"
"v_mfma_f32_16x16x32_bf16 v[244:247], a[220:223], v[160:163], v[244:247]\n"
"v_exp_f32 v221, v96\n"
"v_mfma_f32_16x16x32_bf16 v[232:235], a[216:219], v[160:163], v[232:235]\n"
"v_exp_f32 v222, v97\n"
"v_mfma_f32_16x16x32_bf16 v[184:187], a[212:215], v[160:163], v[184:187]\n"
"v_exp_f32 v223, v98\n"
"v_mfma_f32_16x16x32_bf16 v[172:175], a[208:211], v[160:163], v[172:175]\n"
"v_exp_f32 v224, v99\n"
"v_mfma_f32_16x16x32_bf16 v[176:179], a[208:211], v[164:167], v[176:179]\n"
"ds_read_b64_tr_b16 a[208:209], v192 offset:24576\n"
"ds_read_b64_tr_b16 a[210:211], v192 offset:26624\n"
"v_mfma_f32_16x16x32_bf16 v[188:191], a[212:215], v[164:167], v[188:191]\n"
"ds_read_b64_tr_b16 a[212:213], v193 offset:24576\n"
"ds_read_b64_tr_b16 a[214:215], v193 offset:26624\n"
"v_mfma_f32_16x16x32_bf16 v[236:239], a[216:219], v[164:167], v[236:239]\n"
"ds_read_b64_tr_b16 a[216:217], v192 offset:25088\n"
"ds_read_b64_tr_b16 a[218:219], v192 offset:27136\n"
"v_mfma_f32_16x16x32_bf16 v[248:251], a[220:223], v[164:167], v[248:251]\n"
"s_waitcnt lgkmcnt(12)\n"
"ds_read_b64_tr_b16 a[220:221], v193 offset:25088\n"
"ds_read_b64_tr_b16 a[222:223], v193 offset:27136\n"
"v_mfma_f32_16x16x32_bf16 a[56:59], a[224:227], v[164:167], a[56:59]\n"
"s_waitcnt lgkmcnt(12)\n"
"v_exp_f32 v225, v100\n"
"v_mfma_f32_16x16x32_bf16 a[68:71], a[228:231], v[164:167], a[68:71]\n"
"s_waitcnt lgkmcnt(10)\n"
"v_exp_f32 v226, v101\n"
"v_mfma_f32_16x16x32_bf16 a[80:83], a[232:235], v[164:167], a[80:83]\n"
"s_waitcnt lgkmcnt(8)\n"
"s_add_u32 m0, s60, 102272\n"
"v_cvt_pk_bf16_f32 v144, v221, v222\n"
"v_mfma_f32_16x16x32_bf16 a[92:95], a[236:239], v[164:167], a[92:95]\n"
"buffer_load_dwordx4 v205, s[28:31], s63 offen offset:128 nt lds\n"
"v_mfma_f32_16x16x32_bf16 a[88:91], a[236:239], v[160:163], a[88:91]\n"
"v_cvt_pk_bf16_f32 v145, v223, v224\n"
"v_mfma_f32_16x16x32_bf16 a[76:79], a[232:235], v[160:163], a[76:79]\n"
"v_exp_f32 v227, v102\n"
"v_mfma_f32_16x16x32_bf16 a[64:67], a[228:231], v[160:163], a[64:67]\n"
"v_exp_f32 v220, v103\n"
"v_mfma_f32_16x16x32_bf16 a[52:55], a[224:227], v[160:163], a[52:55]\n"
"v_exp_f32 v221, v104\n"
"v_mfma_f32_16x16x32_bf16 a[48:51], a[224:227], v[156:159], a[48:51]\n"
"ds_read_b64_tr_b16 a[224:225], v192 offset:28672\n"
"ds_read_b64_tr_b16 a[226:227], v192 offset:30720\n"
"s_nop 7\n"
"v_mfma_f32_16x16x32_bf16 a[60:63], a[228:231], v[156:159], a[60:63]\n"
"ds_read_b64_tr_b16 a[228:229], v193 offset:28672\n"
"ds_read_b64_tr_b16 a[230:231], v193 offset:30720\n"
"v_mfma_f32_16x16x32_bf16 a[72:75], a[232:235], v[156:159], a[72:75]\n"
"ds_read_b64_tr_b16 a[232:233], v192 offset:29184\n"
"ds_read_b64_tr_b16 a[234:235], v192 offset:31232\n"
"v_mfma_f32_16x16x32_bf16 a[84:87], a[236:239], v[156:159], a[84:87]\n"
"s_waitcnt lgkmcnt(12)\n"
"ds_read_b64_tr_b16 a[236:237], v193 offset:29184\n"
"ds_read_b64_tr_b16 a[238:239], v193 offset:31232\n"
"v_mfma_f32_16x16x32_bf16 a[96:99], a[208:211], v[156:159], a[96:99]\n"
"s_waitcnt lgkmcnt(12)\n"
"v_exp_f32 v222, v105\n"
"v_mfma_f32_16x16x32_bf16 a[108:111], a[212:215], v[156:159], a[108:111]\n"
"s_waitcnt lgkmcnt(10)\n"
"v_cvt_pk_bf16_f32 v148, v225, v226\n"
"v_cvt_pk_bf16_f32 v149, v227, v220\n"
"v_mfma_f32_16x16x32_bf16 a[120:123], a[216:219], v[156:159], a[120:123]\n"
"s_waitcnt lgkmcnt(8)\n"
"v_exp_f32 v223, v106\n"
"v_mfma_f32_16x16x32_bf16 a[132:135], a[220:223], v[156:159], a[132:135]\n"
"s_add_u32 m0, s60, 106240\n"
"v_exp_f32 v224, v107\n"
"v_mfma_f32_16x16x32_bf16 a[136:139], a[220:223], v[160:163], a[136:139]\n"
"buffer_load_dwordx4 v205, s[28:31], s63 offen offset:256 nt lds\n"
"v_mfma_f32_16x16x32_bf16 a[124:127], a[216:219], v[160:163], a[124:127]\n"
"v_exp_f32 v225, v108\n"
"v_mfma_f32_16x16x32_bf16 a[112:115], a[212:215], v[160:163], a[112:115]\n"
"s_waitcnt vmcnt(15)\n"
"s_barrier\n"
"v_mfma_f32_16x16x32_bf16 a[100:103], a[208:211], v[160:163], a[100:103]\n"
"v_exp_f32 v226, v109\n"
"v_mfma_f32_16x16x32_bf16 a[104:107], a[208:211], v[164:167], a[104:107]\n"
"ds_read_b64 a[208:209], v198 offset:32768\n"
"ds_read_b64 a[210:211], v199 offset:32768\n"
"v_mfma_f32_16x16x32_bf16 a[116:119], a[212:215], v[164:167], a[116:119]\n"
"ds_read_b64 a[212:213], v198 offset:33280\n"
"ds_read_b64 a[214:215], v199 offset:33280\n"
"v_mfma_f32_16x16x32_bf16 a[128:131], a[216:219], v[164:167], a[128:131]\n"
"ds_read_b64 a[216:217], v198 offset:36864\n"
"ds_read_b64 a[218:219], v199 offset:36864\n"
"v_mfma_f32_16x16x32_bf16 a[140:143], a[220:223], v[164:167], a[140:143]\n"
"s_waitcnt lgkmcnt(12)\n"
"ds_read_b64 a[220:221], v198 offset:37376\n"
"ds_read_b64 a[222:223], v199 offset:37376\n"
"v_mfma_f32_16x16x32_bf16 a[152:155], a[224:227], v[164:167], a[152:155]\n"
"s_waitcnt lgkmcnt(12)\n"
"v_cvt_pk_bf16_f32 v152, v221, v222\n"
"v_cvt_pk_bf16_f32 v153, v223, v224\n"
"v_mfma_f32_16x16x32_bf16 a[164:167], a[228:231], v[164:167], a[164:167]\n"
"s_waitcnt lgkmcnt(10)\n"
"v_exp_f32 v227, v110\n"
"v_mfma_f32_16x16x32_bf16 a[176:179], a[232:235], v[164:167], a[176:179]\n"
"s_waitcnt lgkmcnt(8)\n"
"v_exp_f32 v220, v111\n"
"v_mfma_f32_16x16x32_bf16 a[188:191], a[236:239], v[164:167], a[188:191]\n"
"v_exp_f32 v221, v112\n"
"v_mfma_f32_16x16x32_bf16 a[184:187], a[236:239], v[160:163], a[184:187]\n"
"s_add_u32 m0, s60, 110208\n"
"v_exp_f32 v222, v113\n"
"s_nop 7\n"
"v_mfma_f32_16x16x32_bf16 a[172:175], a[232:235], v[160:163], a[172:175]\n"
"buffer_load_dwordx4 v205, s[28:31], s63 offen offset:384 nt lds\n"
"v_mfma_f32_16x16x32_bf16 a[160:163], a[228:231], v[160:163], a[160:163]\n"
"v_cvt_pk_bf16_f32 v146, v225, v226\n"
"v_cvt_pk_bf16_f32 v147, v227, v220\n"
"v_mfma_f32_16x16x32_bf16 a[148:151], a[224:227], v[160:163], a[148:151]\n"
"v_exp_f32 v223, v114\n"
"v_mfma_f32_16x16x32_bf16 a[144:147], a[224:227], v[156:159], a[144:147]\n"
"ds_read_b64 a[224:225], v198 offset:40960\n"
"ds_read_b64 a[226:227], v199 offset:40960\n"
"v_mfma_f32_16x16x32_bf16 a[156:159], a[228:231], v[156:159], a[156:159]\n"
"ds_read_b64 a[228:229], v198 offset:41472\n"
"ds_read_b64 a[230:231], v199 offset:41472\n"
"v_mfma_f32_16x16x32_bf16 a[168:171], a[232:235], v[156:159], a[168:171]\n"
"ds_read_b64 a[232:233], v198 offset:45056\n"
"ds_read_b64 a[234:235], v199 offset:45056\n"
"v_mfma_f32_16x16x32_bf16 a[180:183], a[236:239], v[156:159], a[180:183]\n"
"s_waitcnt lgkmcnt(12)\n"
"ds_read_b64 a[236:237], v198 offset:45568\n"
"ds_read_b64 a[238:239], v199 offset:45568\n"
"v_mfma_f32_16x16x32_bf16 v[120:123], a[208:211], v[0:3], v[208:211]\n"
"s_waitcnt lgkmcnt(12)\n"
"v_exp_f32 v224, v115\n"
"v_mfma_f32_16x16x32_bf16 v[120:123], a[212:215], v[12:15], v[120:123]\n"
"s_waitcnt lgkmcnt(10)\n"
"v_exp_f32 v225, v116\n"
"v_mfma_f32_16x16x32_bf16 v[120:123], a[216:219], v[24:27], v[120:123]\n"
"s_waitcnt lgkmcnt(8)\n"
"v_exp_f32 v226, v117\n"
"v_mfma_f32_16x16x32_bf16 v[120:123], a[220:223], v[36:39], v[120:123]\n"
"v_cvt_pk_bf16_f32 v150, v221, v222\n"
"v_cvt_pk_bf16_f32 v151, v223, v224\n"
"v_mfma_f32_16x16x32_bf16 v[124:127], a[220:223], v[40:43], v[212:215]\n"
"s_mov_b32 s63, 65536\n"
"v_exp_f32 v227, v118\n"
"v_mfma_f32_16x16x32_bf16 v[124:127], a[216:219], v[28:31], v[124:127]\n"
"s_add_u32 m0, s60, 114688\n"
"v_exp_f32 v220, v119\n"
"v_mfma_f32_16x16x32_bf16 v[124:127], a[212:215], v[16:19], v[124:127]\n"
"buffer_load_dwordx4 v204, s[32:35], s63 offen nt lds\n"
"v_mfma_f32_16x16x32_bf16 v[124:127], a[208:211], v[4:7], v[124:127]\n"
"v_cvt_pk_bf16_f32 v154, v225, v226\n"
"v_cvt_pk_bf16_f32 v155, v227, v220\n"
"v_mfma_f32_16x16x32_bf16 v[128:131], a[208:211], v[8:11], v[216:219]\n"
"ds_read_b64 a[208:209], v198 offset:34816\n"
"ds_read_b64 a[210:211], v199 offset:34816\n"
"v_mfma_f32_16x16x32_bf16 v[128:131], a[212:215], v[20:23], v[128:131]\n"
"ds_read_b64 a[212:213], v198 offset:35328\n"
"ds_read_b64 a[214:215], v199 offset:35328\n"
"v_mfma_f32_16x16x32_bf16 v[128:131], a[216:219], v[32:35], v[128:131]\n"
"ds_read_b64 a[216:217], v198 offset:38912\n"
"ds_read_b64 a[218:219], v199 offset:38912\n"
"v_mfma_f32_16x16x32_bf16 v[128:131], a[220:223], v[44:47], v[128:131]\n"
"s_waitcnt lgkmcnt(12)\n"
"ds_read_b64 a[220:221], v198 offset:39424\n"
"ds_read_b64 a[222:223], v199 offset:39424\n"
"v_mfma_f32_16x16x32_bf16 v[128:131], a[224:227], v[56:59], v[128:131]\n"
"s_waitcnt lgkmcnt(12)\n"
"s_nop 7\n"
"v_mfma_f32_16x16x32_bf16 v[128:131], a[228:231], v[68:71], v[128:131]\n"
"s_waitcnt lgkmcnt(10)\n"
"v_mfma_f32_16x16x32_bf16 v[128:131], a[232:235], v[80:83], v[128:131]\n"
"s_waitcnt lgkmcnt(8)\n"
"v_mfma_f32_16x16x32_bf16 v[128:131], a[236:239], v[92:95], v[128:131]\n"
"v_mfma_f32_16x16x32_bf16 v[124:127], a[236:239], v[88:91], v[124:127]\n"
"v_mfma_f32_16x16x32_bf16 v[124:127], a[232:235], v[76:79], v[124:127]\n"
"s_add_u32 m0, s60, 118656\n"
"v_mfma_f32_16x16x32_bf16 v[124:127], a[228:231], v[64:67], v[124:127]\n"
"buffer_load_dwordx4 v204, s[32:35], s63 offen offset:128 nt lds\n"
"v_mfma_f32_16x16x32_bf16 v[124:127], a[224:227], v[52:55], v[124:127]\n"
"v_mfma_f32_16x16x32_bf16 v[120:123], a[224:227], v[48:51], v[120:123]\n"
"ds_read_b64 a[224:225], v198 offset:43008\n"
"ds_read_b64 a[226:227], v199 offset:43008\n"
"v_mfma_f32_16x16x32_bf16 v[120:123], a[228:231], v[60:63], v[120:123]\n"
"ds_read_b64 a[228:229], v198 offset:43520\n"
"ds_read_b64 a[230:231], v199 offset:43520\n"
"v_mfma_f32_16x16x32_bf16 v[120:123], a[232:235], v[72:75], v[120:123]\n"
"ds_read_b64 a[232:233], v198 offset:47104\n"
"ds_read_b64 a[234:235], v199 offset:47104\n"
"v_mfma_f32_16x16x32_bf16 v[120:123], a[236:239], v[84:87], v[120:123]\n"
"s_waitcnt lgkmcnt(12)\n"
"ds_read_b64 a[236:237], v198 offset:47616\n"
"ds_read_b64 a[238:239], v199 offset:47616\n"
"v_mfma_f32_16x16x32_bf16 v[132:135], a[208:211], v[0:3], v[208:211]\n"
"s_waitcnt lgkmcnt(12)\n"
"v_mfma_f32_16x16x32_bf16 v[132:135], a[212:215], v[12:15], v[132:135]\n"
"s_waitcnt lgkmcnt(10)\n"
"v_mfma_f32_16x16x32_bf16 v[132:135], a[216:219], v[24:27], v[132:135]\n"
"s_waitcnt lgkmcnt(8)\n"
"v_mfma_f32_16x16x32_bf16 v[132:135], a[220:223], v[36:39], v[132:135]\n"
"v_mfma_f32_16x16x32_bf16 v[136:139], a[220:223], v[40:43], v[212:215]\n"
"v_mfma_f32_16x16x32_bf16 v[136:139], a[216:219], v[28:31], v[136:139]\n"
"v_mfma_f32_16x16x32_bf16 v[136:139], a[212:215], v[16:19], v[136:139]\n"
"s_add_u32 m0, s60, 122624\n"
"s_waitcnt vmcnt(14)\n"
"s_barrier\n"
"v_mfma_f32_16x16x32_bf16 v[136:139], a[208:211], v[4:7], v[136:139]\n"
"buffer_load_dwordx4 v204, s[32:35], s63 offen offset:256 nt lds\n"
"v_mfma_f32_16x16x32_bf16 v[140:143], a[208:211], v[8:11], v[216:219]\n"
"ds_read_b64_tr_b16 a[208:209], v192 offset:49152\n"
"ds_read_b64_tr_b16 a[210:211], v192 offset:51200\n"
"s_nop 7\n"
"v_mfma_f32_16x16x32_bf16 v[140:143], a[212:215], v[20:23], v[140:143]\n"
"ds_read_b64_tr_b16 a[212:213], v193 offset:49152\n"
"ds_read_b64_tr_b16 a[214:215], v193 offset:51200\n"
"v_mfma_f32_16x16x32_bf16 v[140:143], a[216:219], v[32:35], v[140:143]\n"
"ds_read_b64_tr_b16 a[216:217], v192 offset:49664\n"
"ds_read_b64_tr_b16 a[218:219], v192 offset:51712\n"
"v_mfma_f32_16x16x32_bf16 v[140:143], a[220:223], v[44:47], v[140:143]\n"
"ds_read_b64_tr_b16 a[220:221], v193 offset:49664\n"
"s_waitcnt lgkmcnt(14)\n"
"ds_read_b64_tr_b16 a[222:223], v193 offset:51712\n"
"v_mfma_f32_16x16x32_bf16 a[0:3], a[204:207], v[144:147], a[192:195]\n"
"v_mfma_f32_16x16x32_bf16 a[4:7], a[204:207], v[148:151], a[196:199]\n"
"v_mfma_f32_16x16x32_bf16 a[8:11], a[204:207], v[152:155], a[200:203]\n"
"s_waitcnt lgkmcnt(14)\n"
"v_mfma_f32_16x16x32_bf16 v[140:143], a[224:227], v[56:59], v[140:143]\n"
"s_waitcnt lgkmcnt(12)\n"
"v_mfma_f32_16x16x32_bf16 v[140:143], a[228:231], v[68:71], v[140:143]\n"
"s_waitcnt lgkmcnt(10)\n"
"v_mfma_f32_16x16x32_bf16 v[140:143], a[232:235], v[80:83], v[140:143]\n"
"s_waitcnt lgkmcnt(8)\n"
"v_accvgpr_read_b32 v221, a0\n"
"v_accvgpr_read_b32 v222, a4\n"
"v_mfma_f32_16x16x32_bf16 v[140:143], a[236:239], v[92:95], v[140:143]\n"
"v_accvgpr_read_b32 v223, a8\n"
"v_add_f32 v221, v221, v222\n"
"v_mfma_f32_16x16x32_bf16 v[136:139], a[236:239], v[88:91], v[136:139]\n"
"s_add_u32 m0, s60, 126592\n"
"v_add_f32 v221, v221, v223\n"
"v_cmp_ngt_f32 vcc, 0x47800000, v221\n"
"v_mfma_f32_16x16x32_bf16 v[136:139], a[232:235], v[76:79], v[136:139]\n"
"buffer_load_dwordx4 v204, s[32:35], s63 offen offset:384 nt lds\n"
"v_mfma_f32_16x16x32_bf16 v[136:139], a[228:231], v[64:67], v[136:139]\n"
"v_mfma_f32_16x16x32_bf16 v[136:139], a[224:227], v[52:55], v[136:139]\n"
"v_mfma_f32_16x16x32_bf16 v[132:135], a[224:227], v[48:51], v[132:135]\n"
"ds_read_b64_tr_b16 a[224:225], v192 offset:53248\n"
"ds_read_b64_tr_b16 a[226:227], v192 offset:55296\n"
"v_mfma_f32_16x16x32_bf16 v[132:135], a[228:231], v[60:63], v[132:135]\n"
"ds_read_b64_tr_b16 a[228:229], v193 offset:53248\n"
"ds_read_b64_tr_b16 a[230:231], v193 offset:55296\n"
"v_mfma_f32_16x16x32_bf16 v[132:135], a[232:235], v[72:75], v[132:135]\n"
"ds_read_b64_tr_b16 a[232:233], v192 offset:53760\n"
"ds_read_b64_tr_b16 a[234:235], v192 offset:55808\n"
"v_mfma_f32_16x16x32_bf16 v[132:135], a[236:239], v[84:87], v[132:135]\n"
"s_waitcnt lgkmcnt(12)\n"
"s_cbranch_vccnz Lstub2\n"
"Lback2:\n"
"ds_read_b64_tr_b16 a[236:237], v193 offset:53760\n"
"v_mfma_f32_16x16x32_bf16 v[168:171], a[208:211], v[144:147], v[168:171]\n"
"s_waitcnt lgkmcnt(11)\n"
"ds_read_b64_tr_b16 a[238:239], v193 offset:55808\n"
"v_mfma_f32_16x16x32_bf16 v[180:183], a[212:215], v[144:147], v[180:183]\n"
"s_waitcnt lgkmcnt(10)\n"
"v_exp_f32 v224, v120\n"
"s_nop 7\n"
"v_mfma_f32_16x16x32_bf16 v[228:231], a[216:219], v[144:147], v[228:231]\n"
"s_waitcnt lgkmcnt(8)\n"
"v_exp_f32 v225, v121\n"
"v_mfma_f32_16x16x32_bf16 v[240:243], a[220:223], v[144:147], v[240:243]\n"
"v_exp_f32 v226, v122\n"
"v_mfma_f32_16x16x32_bf16 v[244:247], a[220:223], v[148:151], v[244:247]\n"
"s_mov_b32 s63, 98304\n"
"v_exp_f32 v227, v123\n"
"v_mfma_f32_16x16x32_bf16 v[232:235], a[216:219], v[148:151], v[232:235]\n"
"s_add_u32 m0, s60, 131072\n"
"v_exp_f32 v220, v124\n"
"v_mfma_f32_16x16x32_bf16 v[184:187], a[212:215], v[148:151], v[184:187]\n"
"buffer_load_dwordx4 v205, s[28:31], s63 offen nt lds\n"
"v_mfma_f32_16x16x32_bf16 v[172:175], a[208:211], v[148:151], v[172:175]\n"
"v_exp_f32 v221, v125\n"
"v_mfma_f32_16x16x32_bf16 v[176:179], a[208:211], v[152:155], v[176:179]\n"
"ds_read_b64_tr_b16 a[208:209], v192 offset:57344\n"
"ds_read_b64_tr_b16 a[210:211], v192 offset:59392\n"
"v_mfma_f32_16x16x32_bf16 v[188:191], a[212:215], v[152:155], v[188:191]\n"
"ds_read_b64_tr_b16 a[212:213], v193 offset:57344\n"
"ds_read_b64_tr_b16 a[214:215], v193 offset:59392\n"
"v_mfma_f32_16x16x32_bf16 v[236:239], a[216:219], v[152:155], v[236:239]\n"
"ds_read_b64_tr_b16 a[216:217], v192 offset:57856\n"
"ds_read_b64_tr_b16 a[218:219], v192 offset:59904\n"
"v_mfma_f32_16x16x32_bf16 v[248:251], a[220:223], v[152:155], v[248:251]\n"
"s_waitcnt lgkmcnt(12)\n"
"ds_read_b64_tr_b16 a[220:221], v193 offset:57856\n"
"ds_read_b64_tr_b16 a[222:223], v193 offset:59904\n"
"v_mfma_f32_16x16x32_bf16 a[56:59], a[224:227], v[152:155], a[56:59]\n"
"s_waitcnt lgkmcnt(12)\n"
"v_cvt_pk_bf16_f32 v156, v224, v225\n"
"v_cvt_pk_bf16_f32 v157, v226, v227\n"
"v_mfma_f32_16x16x32_bf16 a[68:71], a[228:231], v[152:155], a[68:71]\n"
"s_waitcnt lgkmcnt(10)\n"
"v_exp_f32 v222, v126\n"
"v_mfma_f32_16x16x32_bf16 a[80:83], a[232:235], v[152:155], a[80:83]\n"
"s_waitcnt lgkmcnt(8)\n"
"v_exp_f32 v223, v127\n"
"v_mfma_f32_16x16x32_bf16 a[92:95], a[236:239], v[152:155], a[92:95]\n"
"v_exp_f32 v224, v128\n"
"v_mfma_f32_16x16x32_bf16 a[88:91], a[236:239], v[148:151], a[88:91]\n"
"v_exp_f32 v225, v129\n"
"v_mfma_f32_16x16x32_bf16 a[76:79], a[232:235], v[148:151], a[76:79]\n"
"s_add_u32 m0, s60, 135040\n"
"v_cvt_pk_bf16_f32 v160, v220, v221\n"
"v_cvt_pk_bf16_f32 v161, v222, v223\n"
"v_mfma_f32_16x16x32_bf16 a[64:67], a[228:231], v[148:151], a[64:67]\n"
"buffer_load_dwordx4 v205, s[28:31], s63 offen offset:128 nt lds\n"
"v_mfma_f32_16x16x32_bf16 a[52:55], a[224:227], v[148:151], a[52:55]\n"
"v_exp_f32 v226, v130\n"
"v_mfma_f32_16x16x32_bf16 a[48:51], a[224:227], v[144:147], a[48:51]\n"
"ds_read_b64_tr_b16 a[224:225], v192 offset:61440\n"
"ds_read_b64_tr_b16 a[226:227], v192 offset:63488\n"
"v_mfma_f32_16x16x32_bf16 a[60:63], a[228:231], v[144:147], a[60:63]\n"
"ds_read_b64_tr_b16 a[228:229], v193 offset:61440\n"
"ds_read_b64_tr_b16 a[230:231], v193 offset:63488\n"
"s_nop 7\n"
"v_mfma_f32_16x16x32_bf16 a[72:75], a[232:235], v[144:147], a[72:75]\n"
"ds_read_b64_tr_b16 a[232:233], v192 offset:61952\n"
"ds_read_b64_tr_b16 a[234:235], v192 offset:64000\n"
"v_mfma_f32_16x16x32_bf16 a[84:87], a[236:239], v[144:147], a[84:87]\n"
"s_waitcnt lgkmcnt(12)\n"
"ds_read_b64_tr_b16 a[236:237], v193 offset:61952\n"
"ds_read_b64_tr_b16 a[238:239], v193 offset:64000\n"
"v_mfma_f32_16x16x32_bf16 a[96:99], a[208:211], v[144:147], a[96:99]\n"
"s_waitcnt lgkmcnt(12)\n"
"v_exp_f32 v227, v131\n"
"v_mfma_f32_16x16x32_bf16 a[108:111], a[212:215], v[144:147], a[108:111]\n"
"s_waitcnt lgkmcnt(10)\n"
"v_exp_f32 v220, v132\n"
"v_mfma_f32_16x16x32_bf16 a[120:123], a[216:219], v[144:147], a[120:123]\n"
"s_waitcnt lgkmcnt(8)\n"
"v_exp_f32 v221, v133\n"
"v_mfma_f32_16x16x32_bf16 a[132:135], a[220:223], v[144:147], a[132:135]\n"
"v_cvt_pk_bf16_f32 v164, v224, v225\n"
"v_cvt_pk_bf16_f32 v165, v226, v227\n"
"v_mfma_f32_16x16x32_bf16 a[136:139], a[220:223], v[148:151], a[136:139]\n"
"v_exp_f32 v222, v134\n"
"v_mfma_f32_16x16x32_bf16 a[124:127], a[216:219], v[148:151], a[124:127]\n"
"v_exp_f32 v223, v135\n"
"v_mfma_f32_16x16x32_bf16 a[112:115], a[212:215], v[148:151], a[112:115]\n"
"s_add_u32 m0, s60, 139008\n"
"s_waitcnt vmcnt(14)\n"
"s_barrier\n"
"v_mfma_f32_16x16x32_bf16 a[100:103], a[208:211], v[148:151], a[100:103]\n"
"buffer_load_dwordx4 v205, s[28:31], s63 offen offset:256 nt lds\n"
"v_mfma_f32_16x16x32_bf16 a[104:107], a[208:211], v[152:155], a[104:107]\n"
"ds_read_b64 a[208:209], v200 offset:0\n"
"ds_read_b64 a[210:211], v201 offset:0\n"
"v_mfma_f32_16x16x32_bf16 a[116:119], a[212:215], v[152:155], a[116:119]\n"
"ds_read_b64 a[212:213], v200 offset:512\n"
"ds_read_b64 a[214:215], v201 offset:512\n"
"v_mfma_f32_16x16x32_bf16 a[128:131], a[216:219], v[152:155], a[128:131]\n"
"ds_read_b64 a[216:217], v200 offset:4096\n"
"ds_read_b64 a[218:219], v201 offset:4096\n"
"v_mfma_f32_16x16x32_bf16 a[140:143], a[220:223], v[152:155], a[140:143]\n"
"s_waitcnt lgkmcnt(12)\n"
"ds_read_b64 a[220:221], v200 offset:4608\n"
"ds_read_b64 a[222:223], v201 offset:4608\n"
"v_mfma_f32_16x16x32_bf16 a[152:155], a[224:227], v[152:155], a[152:155]\n"
"s_waitcnt lgkmcnt(12)\n"
"v_exp_f32 v224, v136\n"
"v_mfma_f32_16x16x32_bf16 a[164:167], a[228:231], v[152:155], a[164:167]\n"
"s_waitcnt lgkmcnt(10)\n"
"v_exp_f32 v225, v137\n"
"v_mfma_f32_16x16x32_bf16 a[176:179], a[232:235], v[152:155], a[176:179]\n"
"s_waitcnt lgkmcnt(8)\n"
"v_cvt_pk_bf16_f32 v158, v220, v221\n"
"v_cvt_pk_bf16_f32 v159, v222, v223\n"
"v_mfma_f32_16x16x32_bf16 a[188:191], a[236:239], v[152:155], a[188:191]\n"
"v_exp_f32 v226, v138\n"
"v_mfma_f32_16x16x32_bf16 a[184:187], a[236:239], v[148:151], a[184:187]\n"
"v_exp_f32 v227, v139\n"
"v_mfma_f32_16x16x32_bf16 a[172:175], a[232:235], v[148:151], a[172:175]\n"
"v_exp_f32 v220, v140\n"
"s_nop 7\n"
"v_mfma_f32_16x16x32_bf16 a[160:163], a[228:231], v[148:151], a[160:163]\n"
"v_exp_f32 v221, v141\n"
"v_mfma_f32_16x16x32_bf16 a[148:151], a[224:227], v[148:151], a[148:151]\n"
"s_add_u32 m0, s60, 142976\n"
"v_cvt_pk_bf16_f32 v162, v224, v225\n"
"v_cvt_pk_bf16_f32 v163, v226, v227\n"
"v_mfma_f32_16x16x32_bf16 a[144:147], a[224:227], v[144:147], a[144:147]\n"
"buffer_load_dwordx4 v205, s[28:31], s63 offen offset:384 nt lds\n"
"v_mfma_f32_16x16x32_bf16 a[156:159], a[228:231], v[144:147], a[156:159]\n"
"ds_read_b64 a[224:225], v200 offset:8192\n"
"ds_read_b64 a[226:227], v201 offset:8192\n"
"v_mfma_f32_16x16x32_bf16 a[168:171], a[232:235], v[144:147], a[168:171]\n"
"ds_read_b64 a[228:229], v200 offset:8704\n"
"ds_read_b64 a[230:231], v201 offset:8704\n"
"v_mfma_f32_16x16x32_bf16 a[180:183], a[236:239], v[144:147], a[180:183]\n"
"s_waitcnt lgkmcnt(10)\n"
"ds_read_b64 a[232:233], v200 offset:12288\n"
"ds_read_b64 a[234:235], v201 offset:12288\n"
"v_mfma_f32_16x16x32_bf16 v[96:99], a[208:211], v[0:3], v[208:211]\n"
"s_waitcnt lgkmcnt(10)\n"
"ds_read_b64 a[236:237], v200 offset:12800\n"
"ds_read_b64 a[238:239], v201 offset:12800\n"
"v_mfma_f32_16x16x32_bf16 v[96:99], a[212:215], v[12:15], v[96:99]\n"
"s_waitcnt lgkmcnt(10)\n"
"v_exp_f32 v222, v142\n"
"v_mfma_f32_16x16x32_bf16 v[96:99], a[216:219], v[24:27], v[96:99]\n"
"s_waitcnt lgkmcnt(8)\n"
"v_exp_f32 v223, v143\n"
"v_mfma_f32_16x16x32_bf16 v[96:99], a[220:223], v[36:39], v[96:99]\n"
"v_cvt_pk_bf16_f32 v166, v220, v221\n"
"v_cvt_pk_bf16_f32 v167, v222, v223\n"
"v_mfma_f32_16x16x32_bf16 v[100:103], a[220:223], v[40:43], v[212:215]\n"
"v_mfma_f32_16x16x32_bf16 v[100:103], a[216:219], v[28:31], v[100:103]\n"
"v_mfma_f32_16x16x32_bf16 v[100:103], a[212:215], v[16:19], v[100:103]\n"
"v_mfma_f32_16x16x32_bf16 v[100:103], a[208:211], v[4:7], v[100:103]\n"
"s_mov_b32 s63, 81920\n"
"v_mfma_f32_16x16x32_bf16 v[104:107], a[208:211], v[8:11], v[216:219]\n"
"s_add_u32 m0, s60, 147456\n"
"ds_read_b64 a[208:209], v200 offset:2048\n"
"ds_read_b64 a[210:211], v201 offset:2048\n"
"v_mfma_f32_16x16x32_bf16 v[104:107], a[212:215], v[20:23], v[104:107]\n"
"buffer_load_dwordx4 v204, s[32:35], s63 offen nt lds\n"
"v_mfma_f32_16x16x32_bf16 v[104:107], a[216:219], v[32:35], v[104:107]\n"
"ds_read_b64 a[212:213], v200 offset:2560\n"
"ds_read_b64 a[214:215], v201 offset:2560\n"
"v_mfma_f32_16x16x32_bf16 v[104:107], a[220:223], v[44:47], v[104:107]\n"
"s_waitcnt lgkmcnt(10)\n"
"ds_read_b64 a[216:217], v200 offset:6144\n"
"ds_read_b64 a[218:219], v201 offset:6144\n"
"v_mfma_f32_16x16x32_bf16 v[104:107], a[224:227], v[56:59], v[104:107]\n"
"s_waitcnt lgkmcnt(10)\n"
"ds_read_b64 a[220:221], v200 offset:6656\n"
"ds_read_b64 a[222:223], v201 offset:6656\n"
"v_mfma_f32_16x16x32_bf16 v[104:107], a[228:231], v[68:71], v[104:107]\n"
"s_waitcnt lgkmcnt(10)\n"
"s_nop 7\n"
"v_mfma_f32_16x16x32_bf16 v[104:107], a[232:235], v[80:83], v[104:107]\n"
"s_waitcnt lgkmcnt(8)\n"
"v_mfma_f32_16x16x32_bf16 v[104:107], a[236:239], v[92:95], v[104:107]\n"
"v_mfma_f32_16x16x32_bf16 v[100:103], a[236:239], v[88:91], v[100:103]\n"
"v_mfma_f32_16x16x32_bf16 v[100:103], a[232:235], v[76:79], v[100:103]\n"
"v_mfma_f32_16x16x32_bf16 v[100:103], a[228:231], v[64:67], v[100:103]\n"
"v_mfma_f32_16x16x32_bf16 v[100:103], a[224:227], v[52:55], v[100:103]\n"
"v_mfma_f32_16x16x32_bf16 v[96:99], a[224:227], v[48:51], v[96:99]\n"
"s_add_u32 m0, s60, 151424\n"
"ds_read_b64 a[224:225], v200 offset:10240\n"
"ds_read_b64 a[226:227], v201 offset:10240\n"
"v_mfma_f32_16x16x32_bf16 v[96:99], a[228:231], v[60:63], v[96:99]\n"
"buffer_load_dwordx4 v204, s[32:35], s63 offen offset:128 nt lds\n"
"v_mfma_f32_16x16x32_bf16 v[96:99], a[232:235], v[72:75], v[96:99]\n"
"ds_read_b64 a[228:229], v200 offset:10752\n"
"ds_read_b64 a[230:231], v201 offset:10752\n"
"v_mfma_f32_16x16x32_bf16 v[96:99], a[236:239], v[84:87], v[96:99]\n"
"s_waitcnt lgkmcnt(10)\n"
"ds_read_b64 a[232:233], v200 offset:14336\n"
"ds_read_b64 a[234:235], v201 offset:14336\n"
"v_mfma_f32_16x16x32_bf16 v[108:111], a[208:211], v[0:3], v[208:211]\n"
"s_waitcnt lgkmcnt(10)\n"
"ds_read_b64 a[236:237], v200 offset:14848\n"
"ds_read_b64 a[238:239], v201 offset:14848\n"
"v_mfma_f32_16x16x32_bf16 v[108:111], a[212:215], v[12:15], v[108:111]\n"
"s_waitcnt lgkmcnt(10)\n"
"v_mfma_f32_16x16x32_bf16 v[108:111], a[216:219], v[24:27], v[108:111]\n"
"s_waitcnt lgkmcnt(8)\n"
"v_mfma_f32_16x16x32_bf16 v[108:111], a[220:223], v[36:39], v[108:111]\n"
"v_mfma_f32_16x16x32_bf16 v[112:115], a[220:223], v[40:43], v[212:215]\n"
"v_mfma_f32_16x16x32_bf16 v[112:115], a[216:219], v[28:31], v[112:115]\n"
"v_mfma_f32_16x16x32_bf16 v[112:115], a[212:215], v[16:19], v[112:115]\n"
"s_waitcnt vmcnt(14)\n"
"s_barrier\n"
"v_mfma_f32_16x16x32_bf16 v[112:115], a[208:211], v[4:7], v[112:115]\n"
"v_mfma_f32_16x16x32_bf16 v[116:119], a[208:211], v[8:11], v[216:219]\n"
"ds_read_b64_tr_b16 a[208:209], v194 offset:16384\n"
"ds_read_b64_tr_b16 a[210:211], v194 offset:18432\n"
"v_mfma_f32_16x16x32_bf16 v[116:119], a[212:215], v[20:23], v[116:119]\n"
"s_add_u32 m0, s60, 155392\n"
"ds_read_b64_tr_b16 a[212:213], v195 offset:16384\n"
"ds_read_b64_tr_b16 a[214:215], v195 offset:18432\n"
"s_nop 7\n"
"v_mfma_f32_16x16x32_bf16 v[116:119], a[216:219], v[32:35], v[116:119]\n"
"buffer_load_dwordx4 v204, s[32:35], s63 offen offset:256 nt lds\n"
"v_mfma_f32_16x16x32_bf16 v[116:119], a[220:223], v[44:47], v[116:119]\n"
"ds_read_b64_tr_b16 a[216:217], v194 offset:16896\n"
"ds_read_b64_tr_b16 a[218:219], v194 offset:18944\n"
"v_mfma_f32_16x16x32_bf16 a[192:195], a[204:207], v[156:159], a[0:3]\n"
"ds_read_b64_tr_b16 a[220:221], v195 offset:16896\n"
"s_waitcnt lgkmcnt(14)\n"
"ds_read_b64_tr_b16 a[222:223], v195 offset:18944\n"
"v_mfma_f32_16x16x32_bf16 a[196:199], a[204:207], v[160:163], a[4:7]\n"
"v_mfma_f32_16x16x32_bf16 a[200:203], a[204:207], v[164:167], a[8:11]\n"
"s_waitcnt lgkmcnt(14)\n"
"v_mfma_f32_16x16x32_bf16 v[116:119], a[224:227], v[56:59], v[116:119]\n"
"s_waitcnt lgkmcnt(12)\n"
"v_mfma_f32_16x16x32_bf16 v[116:119], a[228:231], v[68:71], v[116:119]\n"
"s_waitcnt lgkmcnt(10)\n"
"v_mfma_f32_16x16x32_bf16 v[116:119], a[232:235], v[80:83], v[116:119]\n"
"s_waitcnt lgkmcnt(8)\n"
"v_accvgpr_read_b32 v224, a192\n"
"v_accvgpr_read_b32 v225, a196\n"
"v_mfma_f32_16x16x32_bf16 v[116:119], a[236:239], v[92:95], v[116:119]\n"
"v_accvgpr_read_b32 v226, a200\n"
"v_add_f32 v224, v224, v225\n"
"v_mfma_f32_16x16x32_bf16 v[112:115], a[236:239], v[88:91], v[112:115]\n"
"v_add_f32 v224, v224, v226\n"
"v_cmp_ngt_f32 vcc, 0x47800000, v224\n"
"v_mfma_f32_16x16x32_bf16 v[112:115], a[232:235], v[76:79], v[112:115]\n"
"v_mfma_f32_16x16x32_bf16 v[112:115], a[228:231], v[64:67], v[112:115]\n"
"v_mfma_f32_16x16x32_bf16 v[112:115], a[224:227], v[52:55], v[112:115]\n"
"s_add_u32 m0, s60, 159360\n"
"v_mfma_f32_16x16x32_bf16 v[108:111], a[224:227], v[48:51], v[108:111]\n"
"buffer_load_dwordx4 v204, s[32:35], s63 offen offset:384 nt lds\n"
"v_mfma_f32_16x16x32_bf16 v[108:111], a[228:231], v[60:63], v[108:111]\n"
"ds_read_b64_tr_b16 a[224:225], v194 offset:20480\n"
"ds_read_b64_tr_b16 a[226:227], v194 offset:22528\n"
"v_mfma_f32_16x16x32_bf16 v[108:111], a[232:235], v[72:75], v[108:111]\n"
"ds_read_b64_tr_b16 a[228:229], v195 offset:20480\n"
"ds_read_b64_tr_b16 a[230:231], v195 offset:22528\n"
"v_mfma_f32_16x16x32_bf16 v[108:111], a[236:239], v[84:87], v[108:111]\n"
"s_waitcnt lgkmcnt(10)\n"
"s_cbranch_vccnz Lstub3\n"
"Lback3:\n"
"ds_read_b64_tr_b16 a[232:233], v194 offset:20992\n"
"v_mfma_f32_16x16x32_bf16 v[168:171], a[208:211], v[156:159], v[168:171]\n"
"s_waitcnt lgkmcnt(9)\n"
"ds_read_b64_tr_b16 a[234:235], v194 offset:23040\n"
"ds_read_b64_tr_b16 a[236:237], v195 offset:20992\n"
"v_mfma_f32_16x16x32_bf16 v[180:183], a[212:215], v[156:159], v[180:183]\n"
"s_waitcnt lgkmcnt(9)\n"
"ds_read_b64_tr_b16 a[238:239], v195 offset:23040\n"
"v_mfma_f32_16x16x32_bf16 v[228:231], a[216:219], v[156:159], v[228:231]\n"
"s_waitcnt lgkmcnt(8)\n"
"v_exp_f32 v227, v96\n"
"s_nop 7\n"
"v_mfma_f32_16x16x32_bf16 v[240:243], a[220:223], v[156:159], v[240:243]\n"
"v_exp_f32 v220, v97\n"
"v_mfma_f32_16x16x32_bf16 v[244:247], a[220:223], v[160:163], v[244:247]\n"
"v_exp_f32 v221, v98\n"
"v_mfma_f32_16x16x32_bf16 v[232:235], a[216:219], v[160:163], v[232:235]\n"
"v_exp_f32 v222, v99\n"
"v_mfma_f32_16x16x32_bf16 v[184:187], a[212:215], v[160:163], v[184:187]\n"
"v_exp_f32 v223, v100\n"
"v_mfma_f32_16x16x32_bf16 v[172:175], a[208:211], v[160:163], v[172:175]\n"
"s_mov_b32 s63, 114688\n"
"v_exp_f32 v224, v101\n"
"v_mfma_f32_16x16x32_bf16 v[176:179], a[208:211], v[164:167], v[176:179]\n"
"s_add_u32 m0, s60, 0\n"
"ds_read_b64_tr_b16 a[208:209], v194 offset:24576\n"
"ds_read_b64_tr_b16 a[210:211], v194 offset:26624\n"
"v_mfma_f32_16x16x32_bf16 v[188:191], a[212:215], v[164:167], v[188:191]\n"
"buffer_load_dwordx4 v205, s[28:31], s63 offen nt lds\n"
"v_mfma_f32_16x16x32_bf16 v[236:239], a[216:219], v[164:167], v[236:239]\n"
"ds_read_b64_tr_b16 a[212:213], v195 offset:24576\n"
"ds_read_b64_tr_b16 a[214:215], v195 offset:26624\n"
"v_mfma_f32_16x16x32_bf16 v[248:251], a[220:223], v[164:167], v[248:251]\n"
"s_waitcnt lgkmcnt(10)\n"
"ds_read_b64_tr_b16 a[216:217], v194 offset:25088\n"
"ds_read_b64_tr_b16 a[218:219], v194 offset:27136\n"
"v_mfma_f32_16x16x32_bf16 a[56:59], a[224:227], v[164:167], a[56:59]\n"
"s_waitcnt lgkmcnt(10)\n"
"ds_read_b64_tr_b16 a[220:221], v195 offset:25088\n"
"ds_read_b64_tr_b16 a[222:223], v195 offset:27136\n"
"v_mfma_f32_16x16x32_bf16 a[68:71], a[228:231], v[164:167], a[68:71]\n"
"s_waitcnt lgkmcnt(10)\n"
"v_cvt_pk_bf16_f32 v144, v227, v220\n"
"v_cvt_pk_bf16_f32 v145, v221, v222\n"
"v_mfma_f32_16x16x32_bf16 a[80:83], a[232:235], v[164:167], a[80:83]\n"
"s_waitcnt lgkmcnt(8)\n"
"v_exp_f32 v225, v102\n"
"v_mfma_f32_16x16x32_bf16 a[92:95], a[236:239], v[164:167], a[92:95]\n"
"v_exp_f32 v226, v103\n"
"v_mfma_f32_16x16x32_bf16 a[88:91], a[236:239], v[160:163], a[88:91]\n"
"v_exp_f32 v227, v104\n"
"v_mfma_f32_16x16x32_bf16 a[76:79], a[232:235], v[160:163], a[76:79]\n"
"v_exp_f32 v220, v105\n"
"v_mfma_f32_16x16x32_bf16 a[64:67], a[228:231], v[160:163], a[64:67]\n"
"v_cvt_pk_bf16_f32 v148, v223, v224\n"
"v_cvt_pk_bf16_f32 v149, v225, v226\n"
"v_mfma_f32_16x16x32_bf16 a[52:55], a[224:227], v[160:163], a[52:55]\n"
"v_exp_f32 v221, v106\n"
"v_mfma_f32_16x16x32_bf16 a[48:51], a[224:227], v[156:159], a[48:51]\n"
"s_add_u32 m0, s60, 3968\n"
"ds_read_b64_tr_b16 a[224:225], v194 offset:28672\n"
"ds_read_b64_tr_b16 a[226:227], v194 offset:30720\n"
"v_mfma_f32_16x16x32_bf16 a[60:63], a[228:231], v[156:159], a[60:63]\n"
"buffer_load_dwordx4 v205, s[28:31], s63 offen offset:128 nt lds\n"
"v_mfma_f32_16x16x32_bf16 a[72:75], a[232:235], v[156:159], a[72:75]\n"
"ds_read_b64_tr_b16 a[228:229], v195 offset:28672\n"
"ds_read_b64_tr_b16 a[230:231], v195 offset:30720\n"
"s_nop 7\n"
"v_mfma_f32_16x16x32_bf16 a[84:87], a[236:239], v[156:159], a[84:87]\n"
"s_waitcnt lgkmcnt(10)\n"
"ds_read_b64_tr_b16 a[232:233], v194 offset:29184\n"
"ds_read_b64_tr_b16 a[234:235], v194 offset:31232\n"
"v_mfma_f32_16x16x32_bf16 a[96:99], a[208:211], v[156:159], a[96:99]\n"
"s_waitcnt lgkmcnt(10)\n"
"ds_read_b64_tr_b16 a[236:237], v195 offset:29184\n"
"ds_read_b64_tr_b16 a[238:239], v195 offset:31232\n"
"v_mfma_f32_16x16x32_bf16 a[108:111], a[212:215], v[156:159], a[108:111]\n"
"s_waitcnt lgkmcnt(10)\n"
"v_exp_f32 v222, v107\n"
"v_mfma_f32_16x16x32_bf16 a[120:123], a[216:219], v[156:159], a[120:123]\n"
"s_waitcnt lgkmcnt(8)\n"
"v_exp_f32 v223, v108\n"
"v_mfma_f32_16x16x32_bf16 a[132:135], a[220:223], v[156:159], a[132:135]\n"
"v_exp_f32 v224, v109\n"
"v_mfma_f32_16x16x32_bf16 a[136:139], a[220:223], v[160:163], a[136:139]\n"
"v_cvt_pk_bf16_f32 v152, v227, v220\n"
"v_cvt_pk_bf16_f32 v153, v221, v222\n"
"v_mfma_f32_16x16x32_bf16 a[124:127], a[216:219], v[160:163], a[124:127]\n"
"v_exp_f32 v225, v110\n"
"v_mfma_f32_16x16x32_bf16 a[112:115], a[212:215], v[160:163], a[112:115]\n"
"s_waitcnt vmcnt(14)\n"
"s_barrier\n"
"v_mfma_f32_16x16x32_bf16 a[100:103], a[208:211], v[160:163], a[100:103]\n"
"v_exp_f32 v226, v111\n"
"v_mfma_f32_16x16x32_bf16 a[104:107], a[208:211], v[164:167], a[104:107]\n"
"ds_read_b64 a[208:209], v200 offset:32768\n"
"ds_read_b64 a[210:211], v201 offset:32768\n"
"v_mfma_f32_16x16x32_bf16 a[116:119], a[212:215], v[164:167], a[116:119]\n"
"s_add_u32 m0, s60, 7936\n"
"ds_read_b64 a[212:213], v200 offset:33280\n"
"ds_read_b64 a[214:215], v201 offset:33280\n"
"v_mfma_f32_16x16x32_bf16 a[128:131], a[216:219], v[164:167], a[128:131]\n"
"buffer_load_dwordx4 v205, s[28:31], s63 offen offset:256 nt lds\n"
"v_mfma_f32_16x16x32_bf16 a[140:143], a[220:223], v[164:167], a[140:143]\n"
"s_waitcnt lgkmcnt(10)\n"
"ds_read_b64 a[216:217], v200 offset:36864\n"
"ds_read_b64 a[218:219], v201 offset:36864\n"
"v_mfma_f32_16x16x32_bf16 a[152:155], a[224:227], v[164:167], a[152:155]\n"
"s_waitcnt lgkmcnt(10)\n"
"ds_read_b64 a[220:221], v200 offset:37376\n"
"ds_read_b64 a[222:223], v201 offset:37376\n"
"v_mfma_f32_16x16x32_bf16 a[164:167], a[228:231], v[164:167], a[164:167]\n"
"s_waitcnt lgkmcnt(10)\n"
"v_exp_f32 v227, v112\n"
"v_mfma_f32_16x16x32_bf16 a[176:179], a[232:235], v[164:167], a[176:179]\n"
"s_waitcnt lgkmcnt(8)\n"
"v_exp_f32 v220, v113\n"
"v_mfma_f32_16x16x32_bf16 a[188:191], a[236:239], v[164:167], a[188:191]\n"
"v_cvt_pk_bf16_f32 v146, v223, v224\n"
"v_cvt_pk_bf16_f32 v147, v225, v226\n"
"v_mfma_f32_16x16x32_bf16 a[184:187], a[236:239], v[160:163], a[184:187]\n"
"v_exp_f32 v221, v114\n"
"v_mfma_f32_16x16x32_bf16 a[172:175], a[232:235], v[160:163], a[172:175]\n"
"v_exp_f32 v222, v115\n"
"v_mfma_f32_16x16x32_bf16 a[160:163], a[228:231], v[160:163], a[160:163]\n"
"v_exp_f32 v223, v116\n"
"s_nop 7\n"
"v_mfma_f32_16x16x32_bf16 a[148:151], a[224:227], v[160:163], a[148:151]\n"
"v_exp_f32 v224, v117\n"
"v_mfma_f32_16x16x32_bf16 a[144:147], a[224:227], v[156:159], a[144:147]\n"
"ds_read_b64 a[224:225], v200 offset:40960\n"
"ds_read_b64 a[226:227], v201 offset:40960\n"
"v_mfma_f32_16x16x32_bf16 a[156:159], a[228:231], v[156:159], a[156:159]\n"
"ds_read_b64 a[228:229], v200 offset:41472\n"
"ds_read_b64 a[230:231], v201 offset:41472\n"
"v_mfma_f32_16x16x32_bf16 a[168:171], a[232:235], v[156:159], a[168:171]\n"
"s_add_u32 m0, s60, 11904\n"
"ds_read_b64 a[232:233], v200 offset:45056\n"
"ds_read_b64 a[234:235], v201 offset:45056\n"
"v_mfma_f32_16x16x32_bf16 a[180:183], a[236:239], v[156:159], a[180:183]\n"
"s_waitcnt lgkmcnt(12)\n"
"buffer_load_dwordx4 v205, s[28:31], s63 offen offset:384 nt lds\n"
"v_mfma_f32_16x16x32_bf16 v[120:123], a[208:211], v[0:3], v[208:211]\n"
"s_waitcnt lgkmcnt(10)\n"
"ds_read_b64 a[236:237], v200 offset:45568\n"
"ds_read_b64 a[238:239], v201 offset:45568\n"
"v_mfma_f32_16x16x32_bf16 v[120:123], a[212:215], v[12:15], v[120:123]\n"
"s_waitcnt lgkmcnt(10)\n"
"v_cvt_pk_bf16_f32 v150, v227, v220\n"
"v_cvt_pk_bf16_f32 v151, v221, v222\n"
"v_mfma_f32_16x16x32_bf16 v[120:123], a[216:219], v[24:27], v[120:123]\n"
"s_waitcnt lgkmcnt(8)\n"
"v_exp_f32 v225, v118\n"
"v_mfma_f32_16x16x32_bf16 v[120:123], a[220:223], v[36:39], v[120:123]\n"
"v_exp_f32 v226, v119\n"
"v_mfma_f32_16x16x32_bf16 v[124:127], a[220:223], v[40:43], v[212:215]\n"
"v_cvt_pk_bf16_f32 v154, v223, v224\n"
"v_cvt_pk_bf16_f32 v155, v225, v226\n"
"v_mfma_f32_16x16x32_bf16 v[124:127], a[216:219], v[28:31], v[124:127]\n"
"v_mfma_f32_16x16x32_bf16 v[124:127], a[212:215], v[16:19], v[124:127]\n"
"v_mfma_f32_16x16x32_bf16 v[124:127], a[208:211], v[4:7], v[124:127]\n"
"v_mfma_f32_16x16x32_bf16 v[128:131], a[208:211], v[8:11], v[216:219]\n"
"ds_read_b64 a[208:209], v200 offset:34816\n"
"ds_read_b64 a[210:211], v201 offset:34816\n"
"v_mfma_f32_16x16x32_bf16 v[128:131], a[212:215], v[20:23], v[128:131]\n"
"ds_read_b64 a[212:213], v200 offset:35328\n"
"ds_read_b64 a[214:215], v201 offset:35328\n"
"v_mfma_f32_16x16x32_bf16 v[128:131], a[216:219], v[32:35], v[128:131]\n"
"s_mov_b32 s63, 98304\n"
"ds_read_b64 a[216:217], v200 offset:38912\n"
"ds_read_b64 a[218:219], v201 offset:38912\n"
"v_mfma_f32_16x16x32_bf16 v[128:131], a[220:223], v[44:47], v[128:131]\n"
"s_waitcnt lgkmcnt(12)\n"
"s_add_u32 m0, s60, 16384\n"
"ds_read_b64 a[220:221], v200 offset:39424\n"
"v_mfma_f32_16x16x32_bf16 v[128:131], a[224:227], v[56:59], v[128:131]\n"
"s_waitcnt lgkmcnt(11)\n"
"buffer_load_dwordx4 v204, s[32:35], s63 offen nt lds\n"
"v_mfma_f32_16x16x32_bf16 v[128:131], a[228:231], v[68:71], v[128:131]\n"
"s_waitcnt lgkmcnt(9)\n"
"ds_read_b64 a[222:223], v201 offset:39424\n"
"v_mfma_f32_16x16x32_bf16 v[128:131], a[232:235], v[80:83], v[128:131]\n"
"s_waitcnt lgkmcnt(8)\n"
"s_nop 7\n"
"v_mfma_f32_16x16x32_bf16 v[128:131], a[236:239], v[92:95], v[128:131]\n"
"v_mfma_f32_16x16x32_bf16 v[124:127], a[236:239], v[88:91], v[124:127]\n"
"v_mfma_f32_16x16x32_bf16 v[124:127], a[232:235], v[76:79], v[124:127]\n"
"v_mfma_f32_16x16x32_bf16 v[124:127], a[228:231], v[64:67], v[124:127]\n"
"v_mfma_f32_16x16x32_bf16 v[124:127], a[224:227], v[52:55], v[124:127]\n"
"v_mfma_f32_16x16x32_bf16 v[120:123], a[224:227], v[48:51], v[120:123]\n"
"ds_read_b64 a[224:225], v200 offset:43008\n"
"ds_read_b64 a[226:227], v201 offset:43008\n"
"v_mfma_f32_16x16x32_bf16 v[120:123], a[228:231], v[60:63], v[120:123]\n"
"ds_read_b64 a[228:229], v200 offset:43520\n"
"ds_read_b64 a[230:231], v201 offset:43520\n"
"v_mfma_f32_16x16x32_bf16 v[120:123], a[232:235], v[72:75], v[120:123]\n"
"ds_read_b64 a[232:233], v200 offset:47104\n"
"ds_read_b64 a[234:235], v201 offset:47104\n"
"v_mfma_f32_16x16x32_bf16 v[120:123], a[236:239], v[84:87], v[120:123]\n"
"s_waitcnt lgkmcnt(12)\n"
"s_add_u32 m0, s60, 20352\n"
"ds_read_b64 a[236:237], v200 offset:47616\n"
"v_mfma_f32_16x16x32_bf16 v[132:135], a[208:211], v[0:3], v[208:211]\n"
"s_waitcnt lgkmcnt(11)\n"
"buffer_load_dwordx4 v204, s[32:35], s63 offen offset:128 nt lds\n"
"v_mfma_f32_16x16x32_bf16 v[132:135], a[212:215], v[12:15], v[132:135]\n"
"s_waitcnt lgkmcnt(9)\n"
"ds_read_b64 a[238:239], v201 offset:47616\n"
"v_mfma_f32_16x16x32_bf16 v[132:135], a[216:219], v[24:27], v[132:135]\n"
"s_waitcnt lgkmcnt(8)\n"
"v_mfma_f32_16x16x32_bf16 v[132:135], a[220:223], v[36:39], v[132:135]\n"
"v_mfma_f32_16x16x32_bf16 v[136:139], a[220:223], v[40:43], v[212:215]\n"
"v_mfma_f32_16x16x32_bf16 v[136:139], a[216:219], v[28:31], v[136:139]\n"
"v_mfma_f32_16x16x32_bf16 v[136:139], a[212:215], v[16:19], v[136:139]\n"
"s_waitcnt vmcnt(14)\n"
"s_barrier\n"
"v_mfma_f32_16x16x32_bf16 v[136:139], a[208:211], v[4:7], v[136:139]\n"
"v_mfma_f32_16x16x32_bf16 v[140:143], a[208:211], v[8:11], v[216:219]\n"
"ds_read_b64_tr_b16 a[208:209], v194 offset:49152\n"
"ds_read_b64_tr_b16 a[210:211], v194 offset:51200\n"
"v_mfma_f32_16x16x32_bf16 v[140:143], a[212:215], v[20:23], v[140:143]\n"
"ds_read_b64_tr_b16 a[212:213], v195 offset:49152\n"
"ds_read_b64_tr_b16 a[214:215], v195 offset:51200\n"
"v_mfma_f32_16x16x32_bf16 v[140:143], a[216:219], v[32:35], v[140:143]\n"
"ds_read_b64_tr_b16 a[216:217], v194 offset:49664\n"
"ds_read_b64_tr_b16 a[218:219], v194 offset:51712\n"
"s_nop 7\n"
"v_mfma_f32_16x16x32_bf16 v[140:143], a[220:223], v[44:47], v[140:143]\n"
"ds_read_b64_tr_b16 a[220:221], v195 offset:49664\n"
"s_waitcnt lgkmcnt(14)\n"
"ds_read_b64_tr_b16 a[222:223], v195 offset:51712\n"
"v_mfma_f32_16x16x32_bf16 a[0:3], a[204:207], v[144:147], a[192:195]\n"
"s_add_u32 m0, s60, 24320\n"
"v_mfma_f32_16x16x32_bf16 a[4:7], a[204:207], v[148:151], a[196:199]\n"
"buffer_load_dwordx4 v204, s[32:35], s63 offen offset:256 nt lds\n"
"v_mfma_f32_16x16x32_bf16 a[8:11], a[204:207], v[152:155], a[200:203]\n"
"s_waitcnt lgkmcnt(14)\n"
"v_mfma_f32_16x16x32_bf16 v[140:143], a[224:227], v[56:59], v[140:143]\n"
"s_waitcnt lgkmcnt(12)\n"
"v_mfma_f32_16x16x32_bf16 v[140:143], a[228:231], v[68:71], v[140:143]\n"
"s_waitcnt lgkmcnt(10)\n"
"v_mfma_f32_16x16x32_bf16 v[140:143], a[232:235], v[80:83], v[140:143]\n"
"s_waitcnt lgkmcnt(8)\n"
"v_accvgpr_read_b32 v227, a0\n"
"v_accvgpr_read_b32 v220, a4\n"
"v_mfma_f32_16x16x32_bf16 v[140:143], a[236:239], v[92:95], v[140:143]\n"
"v_accvgpr_read_b32 v221, a8\n"
"v_add_f32 v227, v227, v220\n"
"v_mfma_f32_16x16x32_bf16 v[136:139], a[236:239], v[88:91], v[136:139]\n"
"v_add_f32 v227, v227, v221\n"
"v_cmp_ngt_f32 vcc, 0x47800000, v227\n"
"v_mfma_f32_16x16x32_bf16 v[136:139], a[232:235], v[76:79], v[136:139]\n"
"v_mfma_f32_16x16x32_bf16 v[136:139], a[228:231], v[64:67], v[136:139]\n"
"v_mfma_f32_16x16x32_bf16 v[136:139], a[224:227], v[52:55], v[136:139]\n"
"v_mfma_f32_16x16x32_bf16 v[132:135], a[224:227], v[48:51], v[132:135]\n"
"ds_read_b64_tr_b16 a[224:225], v194 offset:53248\n"
"ds_read_b64_tr_b16 a[226:227], v194 offset:55296\n"
"v_mfma_f32_16x16x32_bf16 v[132:135], a[228:231], v[60:63], v[132:135]\n"
"ds_read_b64_tr_b16 a[228:229], v195 offset:53248\n"
"ds_read_b64_tr_b16 a[230:231], v195 offset:55296\n"
"v_mfma_f32_16x16x32_bf16 v[132:135], a[232:235], v[72:75], v[132:135]\n"
"s_add_u32 m0, s60, 28288\n"
"ds_read_b64_tr_b16 a[232:233], v194 offset:53760\n"
"ds_read_b64_tr_b16 a[234:235], v194 offset:55808\n"
"v_mfma_f32_16x16x32_bf16 v[132:135], a[236:239], v[84:87], v[132:135]\n"
"s_waitcnt lgkmcnt(12)\n"
"s_cbranch_vccnz Lstub4\n"
"Lback4:\n"
"ds_read_b64_tr_b16 a[236:237], v195 offset:53760\n"
"v_mfma_f32_16x16x32_bf16 v[168:171], a[208:211], v[144:147], v[168:171]\n"
"s_waitcnt lgkmcnt(11)\n"
"buffer_load_dwordx4 v204, s[32:35], s63 offen offset:384 nt lds\n"
"v_mfma_f32_16x16x32_bf16 v[180:183], a[212:215], v[144:147], v[180:183]\n"
"s_waitcnt lgkmcnt(9)\n"
"ds_read_b64_tr_b16 a[238:239], v195 offset:55808\n"
"v_mfma_f32_16x16x32_bf16 v[228:231], a[216:219], v[144:147], v[228:231]\n"
"s_waitcnt lgkmcnt(8)\n"
"v_exp_f32 v222, v120\n"
"v_mfma_f32_16x16x32_bf16 v[240:243], a[220:223], v[144:147], v[240:243]\n"
"v_exp_f32 v223, v121\n"
"s_nop 7\n"
"v_mfma_f32_16x16x32_bf16 v[244:247], a[220:223], v[148:151], v[244:247]\n"
"v_exp_f32 v224, v122\n"
"v_mfma_f32_16x16x32_bf16 v[232:235], a[216:219], v[148:151], v[232:235]\n"
"v_exp_f32 v225, v123\n"
"v_mfma_f32_16x16x32_bf16 v[184:187], a[212:215], v[148:151], v[184:187]\n"
"v_exp_f32 v226, v124\n"
"v_mfma_f32_16x16x32_bf16 v[172:175], a[208:211], v[148:151], v[172:175]\n"
"v_exp_f32 v227, v125\n"
"v_mfma_f32_16x16x32_bf16 v[176:179], a[208:211], v[152:155], v[176:179]\n"
"ds_read_b64_tr_b16 a[208:209], v194 offset:57344\n"
"ds_read_b64_tr_b16 a[210:211], v194 offset:59392\n"
"v_mfma_f32_16x16x32_bf16 v[188:191], a[212:215], v[152:155], v[188:191]\n"
"ds_read_b64_tr_b16 a[212:213], v195 offset:57344\n"
"ds_read_b64_tr_b16 a[214:215], v195 offset:59392\n"
"v_mfma_f32_16x16x32_bf16 v[236:239], a[216:219], v[152:155], v[236:239]\n"
"s_mov_b32 s63, 131072\n"
"ds_read_b64_tr_b16 a[216:217], v194 offset:57856\n"
"ds_read_b64_tr_b16 a[218:219], v194 offset:59904\n"
"v_mfma_f32_16x16x32_bf16 v[248:251], a[220:223], v[152:155], v[248:251]\n"
"s_waitcnt lgkmcnt(12)\n"
"s_add_u32 m0, s60, 32768\n"
"ds_read_b64_tr_b16 a[220:221], v195 offset:57856\n"
"v_mfma_f32_16x16x32_bf16 a[56:59], a[224:227], v[152:155], a[56:59]\n"
"s_waitcnt lgkmcnt(11)\n"
"buffer_load_dwordx4 v205, s[28:31], s63 offen nt lds\n"
"v_mfma_f32_16x16x32_bf16 a[68:71], a[228:231], v[152:155], a[68:71]\n"
"s_waitcnt lgkmcnt(9)\n"
"ds_read_b64_tr_b16 a[222:223], v195 offset:59904\n"
"v_cvt_pk_bf16_f32 v156, v222, v223\n"
"v_mfma_f32_16x16x32_bf16 a[80:83], a[232:235], v[152:155], a[80:83]\n"
"s_waitcnt lgkmcnt(8)\n"
"v_cvt_pk_bf16_f32 v157, v224, v225\n"
"v_mfma_f32_16x16x32_bf16 a[92:95], a[236:239], v[152:155], a[92:95]\n"
"v_exp_f32 v220, v126\n"
"v_mfma_f32_16x16x32_bf16 a[88:91], a[236:239], v[148:151], a[88:91]\n"
"v_exp_f32 v221, v127\n"
"v_mfma_f32_16x16x32_bf16 a[76:79], a[232:235], v[148:151], a[76:79]\n"
"v_exp_f32 v222, v128\n"
"v_mfma_f32_16x16x32_bf16 a[64:67], a[228:231], v[148:151], a[64:67]\n"
"v_exp_f32 v223, v129\n"
"v_mfma_f32_16x16x32_bf16 a[52:55], a[224:227], v[148:151], a[52:55]\n"
"v_cvt_pk_bf16_f32 v160, v226, v227\n"
"v_cvt_pk_bf16_f32 v161, v220, v221\n"
"v_mfma_f32_16x16x32_bf16 a[48:51], a[224:227], v[144:147], a[48:51]\n"
"ds_read_b64_tr_b16 a[224:225], v194 offset:61440\n"
"ds_read_b64_tr_b16 a[226:227], v194 offset:63488\n"
"v_mfma_f32_16x16x32_bf16 a[60:63], a[228:231], v[144:147], a[60:63]\n"
"ds_read_b64_tr_b16 a[228:229], v195 offset:61440\n"
"ds_read_b64_tr_b16 a[230:231], v195 offset:63488\n"
"v_mfma_f32_16x16x32_bf16 a[72:75], a[232:235], v[144:147], a[72:75]\n"
"ds_read_b64_tr_b16 a[232:233], v194 offset:61952\n"
"ds_read_b64_tr_b16 a[234:235], v194 offset:64000\n"
"v_mfma_f32_16x16x32_bf16 a[84:87], a[236:239], v[144:147], a[84:87]\n"
"s_waitcnt lgkmcnt(12)\n"
"s_add_u32 m0, s60, 36736\n"
"ds_read_b64_tr_b16 a[236:237], v195 offset:61952\n"
"s_nop 7\n"
"v_mfma_f32_16x16x32_bf16 a[96:99], a[208:211], v[144:147], a[96:99]\n"
"s_waitcnt lgkmcnt(11)\n"
"buffer_load_dwordx4 v205, s[28:31], s63 offen offset:128 nt lds\n"
"v_mfma_f32_16x16x32_bf16 a[108:111], a[212:215], v[144:147], a[108:111]\n"
"s_waitcnt lgkmcnt(9)\n"
"ds_read_b64_tr_b16 a[238:239], v195 offset:64000\n"
"v_mfma_f32_16x16x32_bf16 a[120:123], a[216:219], v[144:147], a[120:123]\n"
"s_waitcnt lgkmcnt(8)\n"
"v_exp_f32 v224, v130\n"
"v_mfma_f32_16x16x32_bf16 a[132:135], a[220:223], v[144:147], a[132:135]\n"
"v_exp_f32 v225, v131\n"
"v_mfma_f32_16x16x32_bf16 a[136:139], a[220:223], v[148:151], a[136:139]\n"
"v_exp_f32 v226, v132\n"
"v_mfma_f32_16x16x32_bf16 a[124:127], a[216:219], v[148:151], a[124:127]\n"
"v_exp_f32 v227, v133\n"
"v_mfma_f32_16x16x32_bf16 a[112:115], a[212:215], v[148:151], a[112:115]\n"
"s_waitcnt vmcnt(14)\n"
"s_barrier\n"
"v_cvt_pk_bf16_f32 v164, v222, v223\n"
"v_mfma_f32_16x16x32_bf16 a[100:103], a[208:211], v[148:151], a[100:103]\n"
"v_cvt_pk_bf16_f32 v165, v224, v225\n"
"v_mfma_f32_16x16x32_bf16 a[104:107], a[208:211], v[152:155], a[104:107]\n"
"ds_read_b64 a[208:209], v202 offset:0\n"
"ds_read_b64 a[210:211], v203 offset:0\n"
"v_mfma_f32_16x16x32_bf16 a[116:119], a[212:215], v[152:155], a[116:119]\n"
"ds_read_b64 a[212:213], v202 offset:512\n"
"ds_read_b64 a[214:215], v203 offset:512\n"
"v_mfma_f32_16x16x32_bf16 a[128:131], a[216:219], v[152:155], a[128:131]\n"
"ds_read_b64 a[216:217], v202 offset:4096\n"
"ds_read_b64 a[218:219], v203 offset:4096\n"
"v_mfma_f32_16x16x32_bf16 a[140:143], a[220:223], v[152:155], a[140:143]\n"
"s_waitcnt lgkmcnt(12)\n"
"ds_read_b64 a[220:221], v202 offset:4608\n"
"ds_read_b64 a[222:223], v203 offset:4608\n"
"v_mfma_f32_16x16x32_bf16 a[152:155], a[224:227], v[152:155], a[152:155]\n"
"s_waitcnt lgkmcnt(12)\n"
"s_add_u32 m0, s60, 40704\n"
"v_mfma_f32_16x16x32_bf16 a[164:167], a[228:231], v[152:155], a[164:167]\n"
"s_waitcnt lgkmcnt(10)\n"
"buffer_load_dwordx4 v205, s[28:31], s63 offen offset:256 nt lds\n"
"v_mfma_f32_16x16x32_bf16 a[176:179], a[232:235], v[152:155], a[176:179]\n"
"s_waitcnt lgkmcnt(8)\n"
"v_exp_f32 v220, v134\n"
"v_mfma_f32_16x16x32_bf16 a[188:191], a[236:239], v[152:155], a[188:191]\n"
"v_exp_f32 v221, v135\n"
"v_mfma_f32_16x16x32_bf16 a[184:187], a[236:239], v[148:151], a[184:187]\n"
"v_exp_f32 v222, v136\n"
"v_mfma_f32_16x16x32_bf16 a[172:175], a[232:235], v[148:151], a[172:175]\n"
"v_exp_f32 v223, v137\n"
"v_mfma_f32_16x16x32_bf16 a[160:163], a[228:231], v[148:151], a[160:163]\n"
"v_cvt_pk_bf16_f32 v158, v226, v227\n"
"v_cvt_pk_bf16_f32 v159, v220, v221\n"
"v_mfma_f32_16x16x32_bf16 a[148:151], a[224:227], v[148:151], a[148:151]\n"
"v_exp_f32 v224, v138\n"
"s_nop 7\n"
"v_mfma_f32_16x16x32_bf16 a[144:147], a[224:227], v[144:147], a[144:147]\n"
"ds_read_b64 a[224:225], v202 offset:8192\n"
"ds_read_b64 a[226:227], v203 offset:8192\n"
"v_mfma_f32_16x16x32_bf16 a[156:159], a[228:231], v[144:147], a[156:159]\n"
"ds_read_b64 a[228:229], v202 offset:8704\n"
"ds_read_b64 a[230:231], v203 offset:8704\n"
"v_mfma_f32_16x16x32_bf16 a[168:171], a[232:235], v[144:147], a[168:171]\n"
"ds_read_b64 a[232:233], v202 offset:12288\n"
"ds_read_b64 a[234:235], v203 offset:12288\n"
"v_mfma_f32_16x16x32_bf16 a[180:183], a[236:239], v[144:147], a[180:183]\n"
"s_waitcnt lgkmcnt(12)\n"
"ds_read_b64 a[236:237], v202 offset:12800\n"
"ds_read_b64 a[238:239], v203 offset:12800\n"
"v_mfma_f32_16x16x32_bf16 v[96:99], a[208:211], v[0:3], v[208:211]\n"
"s_waitcnt lgkmcnt(12)\n"
"v_exp_f32 v225, v139\n"
"v_mfma_f32_16x16x32_bf16 v[96:99], a[212:215], v[12:15], v[96:99]\n"
"s_waitcnt lgkmcnt(10)\n"
"s_add_u32 m0, s60, 44672\n"
"v_mfma_f32_16x16x32_bf16 v[96:99], a[216:219], v[24:27], v[96:99]\n"
"s_waitcnt lgkmcnt(8)\n"
"buffer_load_dwordx4 v205, s[28:31], s63 offen offset:384 nt lds\n"
"v_mfma_f32_16x16x32_bf16 v[96:99], a[220:223], v[36:39], v[96:99]\n"
"v_exp_f32 v226, v140\n"
"v_mfma_f32_16x16x32_bf16 v[100:103], a[220:223], v[40:43], v[212:215]\n"
"v_exp_f32 v227, v141\n"
"v_mfma_f32_16x16x32_bf16 v[100:103], a[216:219], v[28:31], v[100:103]\n"
"v_cvt_pk_bf16_f32 v162, v222, v223\n"
"v_cvt_pk_bf16_f32 v163, v224, v225\n"
"v_mfma_f32_16x16x32_bf16 v[100:103], a[212:215], v[16:19], v[100:103]\n"
"v_exp_f32 v220, v142\n"
"v_mfma_f32_16x16x32_bf16 v[100:103], a[208:211], v[4:7], v[100:103]\n"
"v_exp_f32 v221, v143\n"
"v_mfma_f32_16x16x32_bf16 v[104:107], a[208:211], v[8:11], v[216:219]\n"
"ds_read_b64 a[208:209], v202 offset:2048\n"
"ds_read_b64 a[210:211], v203 offset:2048\n"
"v_mfma_f32_16x16x32_bf16 v[104:107], a[212:215], v[20:23], v[104:107]\n"
"ds_read_b64 a[212:213], v202 offset:2560\n"
"ds_read_b64 a[214:215], v203 offset:2560\n"
"v_mfma_f32_16x16x32_bf16 v[104:107], a[216:219], v[32:35], v[104:107]\n"
"ds_read_b64 a[216:217], v202 offset:6144\n"
"ds_read_b64 a[218:219], v203 offset:6144\n"
"v_mfma_f32_16x16x32_bf16 v[104:107], a[220:223], v[44:47], v[104:107]\n"
"s_waitcnt lgkmcnt(12)\n"
"ds_read_b64 a[220:221], v202 offset:6656\n"
"ds_read_b64 a[222:223], v203 offset:6656\n"
"v_mfma_f32_16x16x32_bf16 v[104:107], a[224:227], v[56:59], v[104:107]\n"
"s_waitcnt lgkmcnt(12)\n"
"v_cvt_pk_bf16_f32 v166, v226, v227\n"
"v_cvt_pk_bf16_f32 v167, v220, v221\n"
"v_mfma_f32_16x16x32_bf16 v[104:107], a[228:231], v[68:71], v[104:107]\n"
"s_waitcnt lgkmcnt(10)\n"
"s_mov_b32 s63, 114688\n"
"v_mfma_f32_16x16x32_bf16 v[104:107], a[232:235], v[80:83], v[104:107]\n"
"s_waitcnt lgkmcnt(8)\n"
"s_add_u32 m0, s60, 49152\n"
"v_mfma_f32_16x16x32_bf16 v[104:107], a[236:239], v[92:95], v[104:107]\n"
"buffer_load_dwordx4 v204, s[32:35], s63 offen nt lds\n"
"s_nop 7\n"
"v_mfma_f32_16x16x32_bf16 v[100:103], a[236:239], v[88:91], v[100:103]\n"
"v_mfma_f32_16x16x32_bf16 v[100:103], a[232:235], v[76:79], v[100:103]\n"
"v_mfma_f32_16x16x32_bf16 v[100:103], a[228:231], v[64:67], v[100:103]\n"
"v_mfma_f32_16x16x32_bf16 v[100:103], a[224:227], v[52:55], v[100:103]\n"
"v_mfma_f32_16x16x32_bf16 v[96:99], a[224:227], v[48:51], v[96:99]\n"
"ds_read_b64 a[224:225], v202 offset:10240\n"
"ds_read_b64 a[226:227], v203 offset:10240\n"
"v_mfma_f32_16x16x32_bf16 v[96:99], a[228:231], v[60:63], v[96:99]\n"
"ds_read_b64 a[228:229], v202 offset:10752\n"
"ds_read_b64 a[230:231], v203 offset:10752\n"
"v_mfma_f32_16x16x32_bf16 v[96:99], a[232:235], v[72:75], v[96:99]\n"
"ds_read_b64 a[232:233], v202 offset:14336\n"
"ds_read_b64 a[234:235], v203 offset:14336\n"
"v_mfma_f32_16x16x32_bf16 v[96:99], a[236:239], v[84:87], v[96:99]\n"
"s_waitcnt lgkmcnt(12)\n"
"ds_read_b64 a[236:237], v202 offset:14848\n"
"ds_read_b64 a[238:239], v203 offset:14848\n"
"v_mfma_f32_16x16x32_bf16 v[108:111], a[208:211], v[0:3], v[208:211]\n"
"s_waitcnt lgkmcnt(12)\n"
"v_mfma_f32_16x16x32_bf16 v[108:111], a[212:215], v[12:15], v[108:111]\n"
"s_waitcnt lgkmcnt(10)\n"
"v_mfma_f32_16x16x32_bf16 v[108:111], a[216:219], v[24:27], v[108:111]\n"
"s_waitcnt lgkmcnt(8)\n"
"s_add_u32 m0, s60, 53120\n"
"v_mfma_f32_16x16x32_bf16 v[108:111], a[220:223], v[36:39], v[108:111]\n"
"buffer_load_dwordx4 v204, s[32:35], s63 offen offset:128 nt lds\n"
"v_mfma_f32_16x16x32_bf16 v[112:115], a[220:223], v[40:43], v[212:215]\n"
"v_mfma_f32_16x16x32_bf16 v[112:115], a[216:219], v[28:31], v[112:115]\n"
"v_mfma_f32_16x16x32_bf16 v[112:115], a[212:215], v[16:19], v[112:115]\n"
"s_waitcnt vmcnt(14)\n"
"s_barrier\n"
"v_mfma_f32_16x16x32_bf16 v[112:115], a[208:211], v[4:7], v[112:115]\n"
"v_mfma_f32_16x16x32_bf16 v[116:119], a[208:211], v[8:11], v[216:219]\n"
"ds_read_b64_tr_b16 a[208:209], v196 offset:16384\n"
"ds_read_b64_tr_b16 a[210:211], v196 offset:18432\n"
"v_mfma_f32_16x16x32_bf16 v[116:119], a[212:215], v[20:23], v[116:119]\n"
"ds_read_b64_tr_b16 a[212:213], v197 offset:16384\n"
"ds_read_b64_tr_b16 a[214:215], v197 offset:18432\n"
"v_mfma_f32_16x16x32_bf16 v[116:119], a[216:219], v[32:35], v[116:119]\n"
"ds_read_b64_tr_b16 a[216:217], v196 offset:16896\n"
"ds_read_b64_tr_b16 a[218:219], v196 offset:18944\n"
"v_mfma_f32_16x16x32_bf16 v[116:119], a[220:223], v[44:47], v[116:119]\n"
"ds_read_b64_tr_b16 a[220:221], v197 offset:16896\n"
"s_waitcnt lgkmcnt(14)\n"
"ds_read_b64_tr_b16 a[222:223], v197 offset:18944\n"
"s_nop 7\n"
"v_mfma_f32_16x16x32_bf16 a[192:195], a[204:207], v[156:159], a[0:3]\n"
"v_mfma_f32_16x16x32_bf16 a[196:199], a[204:207], v[160:163], a[4:7]\n"
"v_mfma_f32_16x16x32_bf16 a[200:203], a[204:207], v[164:167], a[8:11]\n"
"s_waitcnt lgkmcnt(14)\n"
"v_mfma_f32_16x16x32_bf16 v[116:119], a[224:227], v[56:59], v[116:119]\n"
"s_waitcnt lgkmcnt(12)\n"
"s_add_u32 m0, s60, 57088\n"
"v_mfma_f32_16x16x32_bf16 v[116:119], a[228:231], v[68:71], v[116:119]\n"
"s_waitcnt lgkmcnt(10)\n"
"buffer_load_dwordx4 v204, s[32:35], s63 offen offset:256 nt lds\n"
"v_mfma_f32_16x16x32_bf16 v[116:119], a[232:235], v[80:83], v[116:119]\n"
"s_waitcnt lgkmcnt(8)\n"
"v_accvgpr_read_b32 v222, a192\n"
"v_accvgpr_read_b32 v223, a196\n"
"v_mfma_f32_16x16x32_bf16 v[116:119], a[236:239], v[92:95], v[116:119]\n"
"v_accvgpr_read_b32 v224, a200\n"
"v_add_f32 v222, v222, v223\n"
"v_mfma_f32_16x16x32_bf16 v[112:115], a[236:239], v[88:91], v[112:115]\n"
"v_add_f32 v222, v222, v224\n"
"v_cmp_ngt_f32 vcc, 0x47800000, v222\n"
"v_mfma_f32_16x16x32_bf16 v[112:115], a[232:235], v[76:79], v[112:115]\n"
"v_mfma_f32_16x16x32_bf16 v[112:115], a[228:231], v[64:67], v[112:115]\n"
"v_mfma_f32_16x16x32_bf16 v[112:115], a[224:227], v[52:55], v[112:115]\n"
"v_mfma_f32_16x16x32_bf16 v[108:111], a[224:227], v[48:51], v[108:111]\n"
"ds_read_b64_tr_b16 a[224:225], v196 offset:20480\n"
"ds_read_b64_tr_b16 a[226:227], v196 offset:22528\n"
"v_mfma_f32_16x16x32_bf16 v[108:111], a[228:231], v[60:63], v[108:111]\n"
"ds_read_b64_tr_b16 a[228:229], v197 offset:20480\n"
"ds_read_b64_tr_b16 a[230:231], v197 offset:22528\n"
"v_mfma_f32_16x16x32_bf16 v[108:111], a[232:235], v[72:75], v[108:111]\n"
"ds_read_b64_tr_b16 a[232:233], v196 offset:20992\n"
"ds_read_b64_tr_b16 a[234:235], v196 offset:23040\n"
"v_mfma_f32_16x16x32_bf16 v[108:111], a[236:239], v[84:87], v[108:111]\n"
"s_waitcnt lgkmcnt(12)\n"
"s_cbranch_vccnz Lstub5\n"
"Lback5:\n"
"ds_read_b64_tr_b16 a[236:237], v197 offset:20992\n"
"s_nop 7\n"
"v_mfma_f32_16x16x32_bf16 v[168:171], a[208:211], v[156:159], v[168:171]\n"
"s_waitcnt lgkmcnt(11)\n"
"ds_read_b64_tr_b16 a[238:239], v197 offset:23040\n"
"v_mfma_f32_16x16x32_bf16 v[180:183], a[212:215], v[156:159], v[180:183]\n"
"s_waitcnt lgkmcnt(10)\n"
"s_add_u32 m0, s60, 61056\n"
"v_mfma_f32_16x16x32_bf16 v[228:231], a[216:219], v[156:159], v[228:231]\n"
"s_waitcnt lgkmcnt(8)\n"
"buffer_load_dwordx4 v204, s[32:35], s63 offen offset:384 nt lds\n"
"v_mfma_f32_16x16x32_bf16 v[240:243], a[220:223], v[156:159], v[240:243]\n"
"v_exp_f32 v225, v96\n"
"v_mfma_f32_16x16x32_bf16 v[244:247], a[220:223], v[160:163], v[244:247]\n"
"v_exp_f32 v226, v97\n"
"v_mfma_f32_16x16x32_bf16 v[232:235], a[216:219], v[160:163], v[232:235]\n"
"v_exp_f32 v227, v98\n"
"v_mfma_f32_16x16x32_bf16 v[184:187], a[212:215], v[160:163], v[184:187]\n"
"v_exp_f32 v220, v99\n"
"v_mfma_f32_16x16x32_bf16 v[172:175], a[208:211], v[160:163], v[172:175]\n"
"v_exp_f32 v221, v100\n"
"v_mfma_f32_16x16x32_bf16 v[176:179], a[208:211], v[164:167], v[176:179]\n"
"ds_read_b64_tr_b16 a[208:209], v196 offset:24576\n"
"ds_read_b64_tr_b16 a[210:211], v196 offset:26624\n"
"v_mfma_f32_16x16x32_bf16 v[188:191], a[212:215], v[164:167], v[188:191]\n"
"ds_read_b64_tr_b16 a[212:213], v197 offset:24576\n"
"ds_read_b64_tr_b16 a[214:215], v197 offset:26624\n"
"v_mfma_f32_16x16x32_bf16 v[236:239], a[216:219], v[164:167], v[236:239]\n"
"ds_read_b64_tr_b16 a[216:217], v196 offset:25088\n"
"ds_read_b64_tr_b16 a[218:219], v196 offset:27136\n"
"v_mfma_f32_16x16x32_bf16 v[248:251], a[220:223], v[164:167], v[248:251]\n"
"s_waitcnt lgkmcnt(12)\n"
"ds_read_b64_tr_b16 a[220:221], v197 offset:25088\n"
"ds_read_b64_tr_b16 a[222:223], v197 offset:27136\n"
"v_mfma_f32_16x16x32_bf16 a[56:59], a[224:227], v[164:167], a[56:59]\n"
"s_waitcnt lgkmcnt(12)\n"
"v_exp_f32 v222, v101\n"
"v_mfma_f32_16x16x32_bf16 a[68:71], a[228:231], v[164:167], a[68:71]\n"
"s_waitcnt lgkmcnt(10)\n"
"s_mov_b32 s63, 147456\n"
"v_cvt_pk_bf16_f32 v144, v225, v226\n"
"s_nop 7\n"
"v_mfma_f32_16x16x32_bf16 a[80:83], a[232:235], v[164:167], a[80:83]\n"
"s_waitcnt lgkmcnt(8)\n"
"s_add_u32 m0, s60, 65536\n"
"v_cvt_pk_bf16_f32 v145, v227, v220\n"
"v_mfma_f32_16x16x32_bf16 a[92:95], a[236:239], v[164:167], a[92:95]\n"
"buffer_load_dwordx4 v205, s[28:31], s63 offen nt lds\n"
"v_mfma_f32_16x16x32_bf16 a[88:91], a[236:239], v[160:163], a[88:91]\n"
"v_exp_f32 v223, v102\n"
"v_mfma_f32_16x16x32_bf16 a[76:79], a[232:235], v[160:163], a[76:79]\n"
"v_exp_f32 v224, v103\n"
"v_mfma_f32_16x16x32_bf16 a[64:67], a[228:231], v[160:163], a[64:67]\n"
"v_exp_f32 v225, v104\n"
"v_mfma_f32_16x16x32_bf16 a[52:55], a[224:227], v[160:163], a[52:55]\n"
"v_exp_f32 v226, v105\n"
"v_mfma_f32_16x16x32_bf16 a[48:51], a[224:227], v[156:159], a[48:51]\n"
"ds_read_b64_tr_b16 a[224:225], v196 offset:28672\n"
"ds_read_b64_tr_b16 a[226:227], v196 offset:30720\n"
"v_mfma_f32_16x16x32_bf16 a[60:63], a[228:231], v[156:159], a[60:63]\n"
"ds_read_b64_tr_b16 a[228:229], v197 offset:28672\n"
"ds_read_b64_tr_b16 a[230:231], v197 offset:30720\n"
"v_mfma_f32_16x16x32_bf16 a[72:75], a[232:235], v[156:159], a[72:75]\n"
"ds_read_b64_tr_b16 a[232:233], v196 offset:29184\n"
"ds_read_b64_tr_b16 a[234:235], v196 offset:31232\n"
"v_mfma_f32_16x16x32_bf16 a[84:87], a[236:239], v[156:159], a[84:87]\n"
"s_waitcnt lgkmcnt(12)\n"
"ds_read_b64_tr_b16 a[236:237], v197 offset:29184\n"
"ds_read_b64_tr_b16 a[238:239], v197 offset:31232\n"
"v_mfma_f32_16x16x32_bf16 a[96:99], a[208:211], v[156:159], a[96:99]\n"
"s_waitcnt lgkmcnt(12)\n"
"v_cvt_pk_bf16_f32 v148, v221, v222\n"
"v_cvt_pk_bf16_f32 v149, v223, v224\n"
"v_mfma_f32_16x16x32_bf16 a[108:111], a[212:215], v[156:159], a[108:111]\n"
"s_waitcnt lgkmcnt(10)\n"
"v_exp_f32 v227, v106\n"
"v_mfma_f32_16x16x32_bf16 a[120:123], a[216:219], v[156:159], a[120:123]\n"
"s_waitcnt lgkmcnt(8)\n"
"s_add_u32 m0, s60, 69504\n"
"v_mfma_f32_16x16x32_bf16 a[132:135], a[220:223], v[156:159], a[132:135]\n"
"buffer_load_dwordx4 v205, s[28:31], s63 offen offset:128 nt lds\n"
"s_nop 7\n"
"v_mfma_f32_16x16x32_bf16 a[136:139], a[220:223], v[160:163], a[136:139]\n"
"v_exp_f32 v220, v107\n"
"v_mfma_f32_16x16x32_bf16 a[124:127], a[216:219], v[160:163], a[124:127]\n"
"v_exp_f32 v221, v108\n"
"v_mfma_f32_16x16x32_bf16 a[112:115], a[212:215], v[160:163], a[112:115]\n"
"s_waitcnt vmcnt(14)\n"
"s_barrier\n"
"v_mfma_f32_16x16x32_bf16 a[100:103], a[208:211], v[160:163], a[100:103]\n"
"v_exp_f32 v222, v109\n"
"v_mfma_f32_16x16x32_bf16 a[104:107], a[208:211], v[164:167], a[104:107]\n"
"ds_read_b64 a[208:209], v198 offset:0\n"
"ds_read_b64 a[210:211], v199 offset:0\n"
"v_mfma_f32_16x16x32_bf16 a[116:119], a[212:215], v[164:167], a[116:119]\n"
"ds_read_b64 a[212:213], v198 offset:512\n"
"ds_read_b64 a[214:215], v199 offset:512\n"
"v_mfma_f32_16x16x32_bf16 a[128:131], a[216:219], v[164:167], a[128:131]\n"
"ds_read_b64 a[216:217], v198 offset:4096\n"
"ds_read_b64 a[218:219], v199 offset:4096\n"
"v_mfma_f32_16x16x32_bf16 a[140:143], a[220:223], v[164:167], a[140:143]\n"
"s_waitcnt lgkmcnt(12)\n"
"ds_read_b64 a[220:221], v198 offset:4608\n"
"ds_read_b64 a[222:223], v199 offset:4608\n"
"v_mfma_f32_16x16x32_bf16 a[152:155], a[224:227], v[164:167], a[152:155]\n"
"s_waitcnt lgkmcnt(12)\n"
"v_cvt_pk_bf16_f32 v152, v225, v226\n"
"v_cvt_pk_bf16_f32 v153, v227, v220\n"
"v_mfma_f32_16x16x32_bf16 a[164:167], a[228:231], v[164:167], a[164:167]\n"
"s_waitcnt lgkmcnt(10)\n"
"v_exp_f32 v223, v110\n"
"v_mfma_f32_16x16x32_bf16 a[176:179], a[232:235], v[164:167], a[176:179]\n"
"s_waitcnt lgkmcnt(8)\n"
"v_exp_f32 v224, v111\n"
"v_mfma_f32_16x16x32_bf16 a[188:191], a[236:239], v[164:167], a[188:191]\n"
"s_add_u32 m0, s60, 73472\n"
"v_exp_f32 v225, v112\n"
"v_mfma_f32_16x16x32_bf16 a[184:187], a[236:239], v[160:163], a[184:187]\n"
"buffer_load_dwordx4 v205, s[28:31], s63 offen offset:256 nt lds\n"
"v_mfma_f32_16x16x32_bf16 a[172:175], a[232:235], v[160:163], a[172:175]\n"
"v_exp_f32 v226, v113\n"
"s_nop 7\n"
"v_mfma_f32_16x16x32_bf16 a[160:163], a[228:231], v[160:163], a[160:163]\n"
"v_cvt_pk_bf16_f32 v146, v221, v222\n"
"v_cvt_pk_bf16_f32 v147, v223, v224\n"
"v_mfma_f32_16x16x32_bf16 a[148:151], a[224:227], v[160:163], a[148:151]\n"
"v_exp_f32 v227, v114\n"
"v_mfma_f32_16x16x32_bf16 a[144:147], a[224:227], v[156:159], a[144:147]\n"
"ds_read_b64 a[224:225], v198 offset:8192\n"
"ds_read_b64 a[226:227], v199 offset:8192\n"
"v_mfma_f32_16x16x32_bf16 a[156:159], a[228:231], v[156:159], a[156:159]\n"
"ds_read_b64 a[228:229], v198 offset:8704\n"
"ds_read_b64 a[230:231], v199 offset:8704\n"
"v_mfma_f32_16x16x32_bf16 a[168:171], a[232:235], v[156:159], a[168:171]\n"
"ds_read_b64 a[232:233], v198 offset:12288\n"
"ds_read_b64 a[234:235], v199 offset:12288\n"
"v_mfma_f32_16x16x32_bf16 a[180:183], a[236:239], v[156:159], a[180:183]\n"
"s_waitcnt lgkmcnt(12)\n"
"ds_read_b64 a[236:237], v198 offset:12800\n"
"ds_read_b64 a[238:239], v199 offset:12800\n"
"v_mfma_f32_16x16x32_bf16 v[120:123], a[208:211], v[0:3], v[208:211]\n"
"s_waitcnt lgkmcnt(12)\n"
"v_exp_f32 v220, v115\n"
"v_mfma_f32_16x16x32_bf16 v[120:123], a[212:215], v[12:15], v[120:123]\n"
"s_waitcnt lgkmcnt(10)\n"
"v_exp_f32 v221, v116\n"
"v_mfma_f32_16x16x32_bf16 v[120:123], a[216:219], v[24:27], v[120:123]\n"
"s_waitcnt lgkmcnt(8)\n"
"v_exp_f32 v222, v117\n"
"v_mfma_f32_16x16x32_bf16 v[120:123], a[220:223], v[36:39], v[120:123]\n"
"v_cvt_pk_bf16_f32 v150, v225, v226\n"
"v_cvt_pk_bf16_f32 v151, v227, v220\n"
"v_mfma_f32_16x16x32_bf16 v[124:127], a[220:223], v[40:43], v[212:215]\n"
"s_add_u32 m0, s60, 77440\n"
"v_exp_f32 v223, v118\n"
"v_mfma_f32_16x16x32_bf16 v[124:127], a[216:219], v[28:31], v[124:127]\n"
"buffer_load_dwordx4 v205, s[28:31], s63 offen offset:384 nt lds\n"
"v_mfma_f32_16x16x32_bf16 v[124:127], a[212:215], v[16:19], v[124:127]\n"
"v_exp_f32 v224, v119\n"
"v_mfma_f32_16x16x32_bf16 v[124:127], a[208:211], v[4:7], v[124:127]\n"
"v_cvt_pk_bf16_f32 v154, v221, v222\n"
"v_cvt_pk_bf16_f32 v155, v223, v224\n"
"s_nop 7\n"
"v_mfma_f32_16x16x32_bf16 v[128:131], a[208:211], v[8:11], v[216:219]\n"
"ds_read_b64 a[208:209], v198 offset:2048\n"
"ds_read_b64 a[210:211], v199 offset:2048\n"
"v_mfma_f32_16x16x32_bf16 v[128:131], a[212:215], v[20:23], v[128:131]\n"
"ds_read_b64 a[212:213], v198 offset:2560\n"
"ds_read_b64 a[214:215], v199 offset:2560\n"
"v_mfma_f32_16x16x32_bf16 v[128:131], a[216:219], v[32:35], v[128:131]\n"
"ds_read_b64 a[216:217], v198 offset:6144\n"
"ds_read_b64 a[218:219], v199 offset:6144\n"
"v_mfma_f32_16x16x32_bf16 v[128:131], a[220:223], v[44:47], v[128:131]\n"
"s_waitcnt lgkmcnt(12)\n"
"ds_read_b64 a[220:221], v198 offset:6656\n"
"ds_read_b64 a[222:223], v199 offset:6656\n"
"v_mfma_f32_16x16x32_bf16 v[128:131], a[224:227], v[56:59], v[128:131]\n"
"s_waitcnt lgkmcnt(12)\n"
"v_mfma_f32_16x16x32_bf16 v[128:131], a[228:231], v[68:71], v[128:131]\n"
"s_waitcnt lgkmcnt(10)\n"
"v_mfma_f32_16x16x32_bf16 v[128:131], a[232:235], v[80:83], v[128:131]\n"
"s_waitcnt lgkmcnt(8)\n"
"v_mfma_f32_16x16x32_bf16 v[128:131], a[236:239], v[92:95], v[128:131]\n"
"v_mfma_f32_16x16x32_bf16 v[124:127], a[236:239], v[88:91], v[124:127]\n"
"s_mov_b32 s63, 131072\n"
"v_mfma_f32_16x16x32_bf16 v[124:127], a[232:235], v[76:79], v[124:127]\n"
"s_add_u32 m0, s60, 81920\n"
"v_mfma_f32_16x16x32_bf16 v[124:127], a[228:231], v[64:67], v[124:127]\n"
"buffer_load_dwordx4 v204, s[32:35], s63 offen nt lds\n"
"v_mfma_f32_16x16x32_bf16 v[124:127], a[224:227], v[52:55], v[124:127]\n"
"v_mfma_f32_16x16x32_bf16 v[120:123], a[224:227], v[48:51], v[120:123]\n"
"ds_read_b64 a[224:225], v198 offset:10240\n"
"ds_read_b64 a[226:227], v199 offset:10240\n"
"v_mfma_f32_16x16x32_bf16 v[120:123], a[228:231], v[60:63], v[120:123]\n"
"ds_read_b64 a[228:229], v198 offset:10752\n"
"ds_read_b64 a[230:231], v199 offset:10752\n"
"s_nop 7\n"
"v_mfma_f32_16x16x32_bf16 v[120:123], a[232:235], v[72:75], v[120:123]\n"
"ds_read_b64 a[232:233], v198 offset:14336\n"
"ds_read_b64 a[234:235], v199 offset:14336\n"
"v_mfma_f32_16x16x32_bf16 v[120:123], a[236:239], v[84:87], v[120:123]\n"
"s_waitcnt lgkmcnt(12)\n"
"ds_read_b64 a[236:237], v198 offset:14848\n"
"ds_read_b64 a[238:239], v199 offset:14848\n"
"v_mfma_f32_16x16x32_bf16 v[132:135], a[208:211], v[0:3], v[208:211]\n"
"s_waitcnt lgkmcnt(12)\n"
"v_mfma_f32_16x16x32_bf16 v[132:135], a[212:215], v[12:15], v[132:135]\n"
"s_waitcnt lgkmcnt(10)\n"
"v_mfma_f32_16x16x32_bf16 v[132:135], a[216:219], v[24:27], v[132:135]\n"
"s_waitcnt lgkmcnt(8)\n"
"v_mfma_f32_16x16x32_bf16 v[132:135], a[220:223], v[36:39], v[132:135]\n"
"v_mfma_f32_16x16x32_bf16 v[136:139], a[220:223], v[40:43], v[212:215]\n"
"v_mfma_f32_16x16x32_bf16 v[136:139], a[216:219], v[28:31], v[136:139]\n"
"s_add_u32 m0, s60, 85888\n"
"v_mfma_f32_16x16x32_bf16 v[136:139], a[212:215], v[16:19], v[136:139]\n"
"s_waitcnt vmcnt(13)\n"
"buffer_load_dwordx4 v204, s[32:35], s63 offen offset:128 nt lds\n"
"v_mfma_f32_16x16x32_bf16 v[136:139], a[208:211], v[4:7], v[136:139]\n"
"s_barrier\n"
"v_mfma_f32_16x16x32_bf16 v[140:143], a[208:211], v[8:11], v[216:219]\n"
"ds_read_b64_tr_b16 a[208:209], v192 offset:16384\n"
"ds_read_b64_tr_b16 a[210:211], v192 offset:18432\n"
"v_mfma_f32_16x16x32_bf16 v[140:143], a[212:215], v[20:23], v[140:143]\n"
"ds_read_b64_tr_b16 a[212:213], v193 offset:16384\n"
"ds_read_b64_tr_b16 a[214:215], v193 offset:18432\n"
"v_mfma_f32_16x16x32_bf16 v[140:143], a[216:219], v[32:35], v[140:143]\n"
"ds_read_b64_tr_b16 a[216:217], v192 offset:16896\n"
"ds_read_b64_tr_b16 a[218:219], v192 offset:18944\n"
"v_mfma_f32_16x16x32_bf16 v[140:143], a[220:223], v[44:47], v[140:143]\n"
"ds_read_b64_tr_b16 a[220:221], v193 offset:16896\n"
"s_waitcnt lgkmcnt(14)\n"
"ds_read_b64_tr_b16 a[222:223], v193 offset:18944\n"
"s_nop 7\n"
"v_mfma_f32_16x16x32_bf16 a[0:3], a[204:207], v[144:147], a[192:195]\n"
"v_mfma_f32_16x16x32_bf16 a[4:7], a[204:207], v[148:151], a[196:199]\n"
"v_mfma_f32_16x16x32_bf16 a[8:11], a[204:207], v[152:155], a[200:203]\n"
"s_waitcnt lgkmcnt(14)\n"
"v_mfma_f32_16x16x32_bf16 v[140:143], a[224:227], v[56:59], v[140:143]\n"
"s_waitcnt lgkmcnt(12)\n"
"v_mfma_f32_16x16x32_bf16 v[140:143], a[228:231], v[68:71], v[140:143]\n"
"s_waitcnt lgkmcnt(10)\n"
"v_mfma_f32_16x16x32_bf16 v[140:143], a[232:235], v[80:83], v[140:143]\n"
"s_waitcnt lgkmcnt(8)\n"
"v_accvgpr_read_b32 v225, a0\n"
"v_accvgpr_read_b32 v226, a4\n"
"v_mfma_f32_16x16x32_bf16 v[140:143], a[236:239], v[92:95], v[140:143]\n"
"s_add_u32 m0, s60, 89856\n"
"v_accvgpr_read_b32 v227, a8\n"
"v_add_f32 v225, v225, v226\n"
"v_mfma_f32_16x16x32_bf16 v[136:139], a[236:239], v[88:91], v[136:139]\n"
"buffer_load_dwordx4 v204, s[32:35], s63 offen offset:256 nt lds\n"
"v_mfma_f32_16x16x32_bf16 v[136:139], a[232:235], v[76:79], v[136:139]\n"
"v_add_f32 v225, v225, v227\n"
"v_cmp_ngt_f32 vcc, 0x47800000, v225\n"
"v_mfma_f32_16x16x32_bf16 v[136:139], a[228:231], v[64:67], v[136:139]\n"
"v_mfma_f32_16x16x32_bf16 v[136:139], a[224:227], v[52:55], v[136:139]\n"
"v_mfma_f32_16x16x32_bf16 v[132:135], a[224:227], v[48:51], v[132:135]\n"
"ds_read_b64_tr_b16 a[224:225], v192 offset:20480\n"
"ds_read_b64_tr_b16 a[226:227], v192 offset:22528\n"
"v_mfma_f32_16x16x32_bf16 v[132:135], a[228:231], v[60:63], v[132:135]\n"
"ds_read_b64_tr_b16 a[228:229], v193 offset:20480\n"
"ds_read_b64_tr_b16 a[230:231], v193 offset:22528\n"
"v_mfma_f32_16x16x32_bf16 v[132:135], a[232:235], v[72:75], v[132:135]\n"
"ds_read_b64_tr_b16 a[232:233], v192 offset:20992\n"
"ds_read_b64_tr_b16 a[234:235], v192 offset:23040\n"
"s_nop 7\n"
"v_mfma_f32_16x16x32_bf16 v[132:135], a[236:239], v[84:87], v[132:135]\n"
"s_waitcnt lgkmcnt(12)\n"
"s_cbranch_vccnz Lstub6\n"
"Lback6:\n"
"ds_read_b64_tr_b16 a[236:237], v193 offset:20992\n"
"v_mfma_f32_16x16x32_bf16 v[168:171], a[208:211], v[144:147], v[168:171]\n"
"s_waitcnt lgkmcnt(11)\n"
"ds_read_b64_tr_b16 a[238:239], v193 offset:23040\n"
"v_mfma_f32_16x16x32_bf16 v[180:183], a[212:215], v[144:147], v[180:183]\n"
"s_waitcnt lgkmcnt(10)\n"
"v_exp_f32 v220, v120\n"
"v_mfma_f32_16x16x32_bf16 v[228:231], a[216:219], v[144:147], v[228:231]\n"
"s_waitcnt lgkmcnt(8)\n"
"v_exp_f32 v221, v121\n"
"v_mfma_f32_16x16x32_bf16 v[240:243], a[220:223], v[144:147], v[240:243]\n"
"v_exp_f32 v222, v122\n"
"v_mfma_f32_16x16x32_bf16 v[244:247], a[220:223], v[148:151], v[244:247]\n"
"s_add_u32 m0, s60, 93824\n"
"v_exp_f32 v223, v123\n"
"v_mfma_f32_16x16x32_bf16 v[232:235], a[216:219], v[148:151], v[232:235]\n"
"buffer_load_dwordx4 v204, s[32:35], s63 offen offset:384 nt lds\n"
"v_mfma_f32_16x16x32_bf16 v[184:187], a[212:215], v[148:151], v[184:187]\n"
"v_exp_f32 v224, v124\n"
"v_mfma_f32_16x16x32_bf16 v[172:175], a[208:211], v[148:151], v[172:175]\n"
"v_exp_f32 v225, v125\n"
"v_mfma_f32_16x16x32_bf16 v[176:179], a[208:211], v[152:155], v[176:179]\n"
"ds_read_b64_tr_b16 a[208:209], v192 offset:24576\n"
"ds_read_b64_tr_b16 a[210:211], v192 offset:26624\n"
"v_mfma_f32_16x16x32_bf16 v[188:191], a[212:215], v[152:155], v[188:191]\n"
"ds_read_b64_tr_b16 a[212:213], v193 offset:24576\n"
"ds_read_b64_tr_b16 a[214:215], v193 offset:26624\n"
"v_mfma_f32_16x16x32_bf16 v[236:239], a[216:219], v[152:155], v[236:239]\n"
"ds_read_b64_tr_b16 a[216:217], v192 offset:25088\n"
"ds_read_b64_tr_b16 a[218:219], v192 offset:27136\n"
"v_mfma_f32_16x16x32_bf16 v[248:251], a[220:223], v[152:155], v[248:251]\n"
"s_waitcnt lgkmcnt(12)\n"
"ds_read_b64_tr_b16 a[220:221], v193 offset:25088\n"
"ds_read_b64_tr_b16 a[222:223], v193 offset:27136\n"
"v_mfma_f32_16x16x32_bf16 a[56:59], a[224:227], v[152:155], a[56:59]\n"
"s_waitcnt lgkmcnt(12)\n"
"v_cvt_pk_bf16_f32 v156, v220, v221\n"
"v_cvt_pk_bf16_f32 v157, v222, v223\n"
"s_nop 7\n"
"v_mfma_f32_16x16x32_bf16 a[68:71], a[228:231], v[152:155], a[68:71]\n"
"s_waitcnt lgkmcnt(10)\n"
"v_exp_f32 v226, v126\n"
"v_mfma_f32_16x16x32_bf16 a[80:83], a[232:235], v[152:155], a[80:83]\n"
"s_waitcnt lgkmcnt(8)\n"
"v_exp_f32 v227, v127\n"
"v_mfma_f32_16x16x32_bf16 a[92:95], a[236:239], v[152:155], a[92:95]\n"
"v_exp_f32 v220, v128\n"
"v_mfma_f32_16x16x32_bf16 a[88:91], a[236:239], v[148:151], a[88:91]\n"
"s_mov_b32 s63, 163840\n"
"v_exp_f32 v221, v129\n"
"v_mfma_f32_16x16x32_bf16 a[76:79], a[232:235], v[148:151], a[76:79]\n"
"s_add_u32 m0, s60, 98304\n"
"v_cvt_pk_bf16_f32 v160, v224, v225\n"
"v_cvt_pk_bf16_f32 v161, v226, v227\n"
"v_mfma_f32_16x16x32_bf16 a[64:67], a[228:231], v[148:151], a[64:67]\n"
"buffer_load_dwordx4 v205, s[28:31], s63 offen nt lds\n"
"v_mfma_f32_16x16x32_bf16 a[52:55], a[224:227], v[148:151], a[52:55]\n"
"v_exp_f32 v222, v130\n"
"v_mfma_f32_16x16x32_bf16 a[48:51], a[224:227], v[144:147], a[48:51]\n"
"ds_read_b64_tr_b16 a[224:225], v192 offset:28672\n"
"ds_read_b64_tr_b16 a[226:227], v192 offset:30720\n"
"v_mfma_f32_16x16x32_bf16 a[60:63], a[228:231], v[144:147], a[60:63]\n"
"ds_read_b64_tr_b16 a[228:229], v193 offset:28672\n"
"ds_read_b64_tr_b16 a[230:231], v193 offset:30720\n"
"v_mfma_f32_16x16x32_bf16 a[72:75], a[232:235], v[144:147], a[72:75]\n"
"ds_read_b64_tr_b16 a[232:233], v192 offset:29184\n"
"ds_read_b64_tr_b16 a[234:235], v192 offset:31232\n"
"v_mfma_f32_16x16x32_bf16 a[84:87], a[236:239], v[144:147], a[84:87]\n"
"s_waitcnt lgkmcnt(12)\n"
"ds_read_b64_tr_b16 a[236:237], v193 offset:29184\n"
"ds_read_b64_tr_b16 a[238:239], v193 offset:31232\n"
"v_mfma_f32_16x16x32_bf16 a[96:99], a[208:211], v[144:147], a[96:99]\n"
"s_waitcnt lgkmcnt(12)\n"
"v_exp_f32 v223, v131\n"
"v_mfma_f32_16x16x32_bf16 a[108:111], a[212:215], v[144:147], a[108:111]\n"
"s_waitcnt lgkmcnt(10)\n"
"v_exp_f32 v224, v132\n"
"v_mfma_f32_16x16x32_bf16 a[120:123], a[216:219], v[144:147], a[120:123]\n"
"s_waitcnt lgkmcnt(8)\n"
"v_exp_f32 v225, v133\n"
"s_nop 7\n"
"v_mfma_f32_16x16x32_bf16 a[132:135], a[220:223], v[144:147], a[132:135]\n"
"v_cvt_pk_bf16_f32 v164, v220, v221\n"
"v_cvt_pk_bf16_f32 v165, v222, v223\n"
"v_mfma_f32_16x16x32_bf16 a[136:139], a[220:223], v[148:151], a[136:139]\n"
"v_exp_f32 v226, v134\n"
"v_mfma_f32_16x16x32_bf16 a[124:127], a[216:219], v[148:151], a[124:127]\n"
"s_add_u32 m0, s60, 102272\n"
"v_exp_f32 v227, v135\n"
"v_mfma_f32_16x16x32_bf16 a[112:115], a[212:215], v[148:151], a[112:115]\n"
"s_waitcnt vmcnt(13)\n"
"buffer_load_dwordx4 v205, s[28:31], s63 offen offset:128 nt lds\n"
"v_mfma_f32_16x16x32_bf16 a[100:103], a[208:211], v[148:151], a[100:103]\n"
"s_barrier\n"
"v_exp_f32 v220, v136\n"
"v_mfma_f32_16x16x32_bf16 a[104:107], a[208:211], v[152:155], a[104:107]\n"
"ds_read_b64 a[208:209], v198 offset:32768\n"
"ds_read_b64 a[210:211], v199 offset:32768\n"
"v_mfma_f32_16x16x32_bf16 a[116:119], a[212:215], v[152:155], a[116:119]\n"
"ds_read_b64 a[212:213], v198 offset:33280\n"
"ds_read_b64 a[214:215], v199 offset:33280\n"
"v_mfma_f32_16x16x32_bf16 a[128:131], a[216:219], v[152:155], a[128:131]\n"
"ds_read_b64 a[216:217], v198 offset:36864\n"
"ds_read_b64 a[218:219], v199 offset:36864\n"
"v_mfma_f32_16x16x32_bf16 a[140:143], a[220:223], v[152:155], a[140:143]\n"
"s_waitcnt lgkmcnt(12)\n"
"ds_read_b64 a[220:221], v198 offset:37376\n"
"ds_read_b64 a[222:223], v199 offset:37376\n"
"v_mfma_f32_16x16x32_bf16 a[152:155], a[224:227], v[152:155], a[152:155]\n"
"s_waitcnt lgkmcnt(12)\n"
"v_exp_f32 v221, v137\n"
"v_mfma_f32_16x16x32_bf16 a[164:167], a[228:231], v[152:155], a[164:167]\n"
"s_waitcnt lgkmcnt(10)\n"
"v_cvt_pk_bf16_f32 v158, v224, v225\n"
"v_cvt_pk_bf16_f32 v159, v226, v227\n"
"v_mfma_f32_16x16x32_bf16 a[176:179], a[232:235], v[152:155], a[176:179]\n"
"s_waitcnt lgkmcnt(8)\n"
"v_exp_f32 v222, v138\n"
"v_mfma_f32_16x16x32_bf16 a[188:191], a[236:239], v[152:155], a[188:191]\n"
"v_exp_f32 v223, v139\n"
"v_mfma_f32_16x16x32_bf16 a[184:187], a[236:239], v[148:151], a[184:187]\n"
"v_exp_f32 v224, v140\n"
"s_nop 7\n"
"v_mfma_f32_16x16x32_bf16 a[172:175], a[232:235], v[148:151], a[172:175]\n"
"v_exp_f32 v225, v141\n"
"v_mfma_f32_16x16x32_bf16 a[160:163], a[228:231], v[148:151], a[160:163]\n"
"s_add_u32 m0, s60, 106240\n"
"v_cvt_pk_bf16_f32 v162, v220, v221\n"
"v_cvt_pk_bf16_f32 v163, v222, v223\n"
"v_mfma_f32_16x16x32_bf16 a[148:151], a[224:227], v[148:151], a[148:151]\n"
"buffer_load_dwordx4 v205, s[28:31], s63 offen offset:256 nt lds\n"
"v_mfma_f32_16x16x32_bf16 a[144:147], a[224:227], v[144:147], a[144:147]\n"
"ds_read_b64 a[224:225], v198 offset:40960\n"
"ds_read_b64 a[226:227], v199 offset:40960\n"
"v_mfma_f32_16x16x32_bf16 a[156:159], a[228:231], v[144:147], a[156:159]\n"
"ds_read_b64 a[228:229], v198 offset:41472\n"
"ds_read_b64 a[230:231], v199 offset:41472\n"
"v_mfma_f32_16x16x32_bf16 a[168:171], a[232:235], v[144:147], a[168:171]\n"
"ds_read_b64 a[232:233], v198 offset:45056\n"
"ds_read_b64 a[234:235], v199 offset:45056\n"
"v_mfma_f32_16x16x32_bf16 a[180:183], a[236:239], v[144:147], a[180:183]\n"
"s_waitcnt lgkmcnt(12)\n"
"ds_read_b64 a[236:237], v198 offset:45568\n"
"ds_read_b64 a[238:239], v199 offset:45568\n"
"v_mfma_f32_16x16x32_bf16 v[96:99], a[208:211], v[0:3], v[208:211]\n"
"s_waitcnt lgkmcnt(12)\n"
"v_exp_f32 v226, v142\n"
"v_mfma_f32_16x16x32_bf16 v[96:99], a[212:215], v[12:15], v[96:99]\n"
"s_waitcnt lgkmcnt(10)\n"
"v_exp_f32 v227, v143\n"
"v_mfma_f32_16x16x32_bf16 v[96:99], a[216:219], v[24:27], v[96:99]\n"
"s_waitcnt lgkmcnt(8)\n"
"v_cvt_pk_bf16_f32 v166, v224, v225\n"
"v_cvt_pk_bf16_f32 v167, v226, v227\n"
"v_mfma_f32_16x16x32_bf16 v[96:99], a[220:223], v[36:39], v[96:99]\n"
"v_mfma_f32_16x16x32_bf16 v[100:103], a[220:223], v[40:43], v[212:215]\n"
"v_mfma_f32_16x16x32_bf16 v[100:103], a[216:219], v[28:31], v[100:103]\n"
"v_mfma_f32_16x16x32_bf16 v[100:103], a[212:215], v[16:19], v[100:103]\n"
"s_nop 7\n"
"v_mfma_f32_16x16x32_bf16 v[100:103], a[208:211], v[4:7], v[100:103]\n"
"s_add_u32 m0, s60, 110208\n"
"v_mfma_f32_16x16x32_bf16 v[104:107], a[208:211], v[8:11], v[216:219]\n"
"buffer_load_dwordx4 v205, s[28:31], s63 offen offset:384 nt lds\n"
"v_mfma_f32_16x16x32_bf16 v[104:107], a[212:215], v[20:23], v[104:107]\n"
"ds_read_b64 a[208:209], v198 offset:34816\n"
"ds_read_b64 a[210:211], v199 offset:34816\n"
"v_mfma_f32_16x16x32_bf16 v[104:107], a[216:219], v[32:35], v[104:107]\n"
"ds_read_b64 a[212:213], v198 offset:35328\n"
"ds_read_b64 a[214:215], v199 offset:35328\n"
"v_mfma_f32_16x16x32_bf16 v[104:107], a[220:223], v[44:47], v[104:107]\n"
"s_waitcnt lgkmcnt(10)\n"
"ds_read_b64 a[216:217], v198 offset:38912\n"
"ds_read_b64 a[218:219], v199 offset:38912\n"
"v_mfma_f32_16x16x32_bf16 v[104:107], a[224:227], v[56:59], v[104:107]\n"
"s_waitcnt lgkmcnt(10)\n"
"ds_read_b64 a[220:221], v198 offset:39424\n"
"ds_read_b64 a[222:223], v199 offset:39424\n"
"v_mfma_f32_16x16x32_bf16 v[104:107], a[228:231], v[68:71], v[104:107]\n"
"s_waitcnt lgkmcnt(10)\n"
"v_mfma_f32_16x16x32_bf16 v[104:107], a[232:235], v[80:83], v[104:107]\n"
"s_waitcnt lgkmcnt(8)\n"
"v_mfma_f32_16x16x32_bf16 v[104:107], a[236:239], v[92:95], v[104:107]\n"
"v_mfma_f32_16x16x32_bf16 v[100:103], a[236:239], v[88:91], v[100:103]\n"
"v_mfma_f32_16x16x32_bf16 v[100:103], a[232:235], v[76:79], v[100:103]\n"
"v_mfma_f32_16x16x32_bf16 v[100:103], a[228:231], v[64:67], v[100:103]\n"
"v_mfma_f32_16x16x32_bf16 v[100:103], a[224:227], v[52:55], v[100:103]\n"
"s_mov_b32 s63, 147456\n"
"v_mfma_f32_16x16x32_bf16 v[96:99], a[224:227], v[48:51], v[96:99]\n"
"s_add_u32 m0, s60, 114688\n"
"ds_read_b64 a[224:225], v198 offset:43008\n"
"ds_read_b64 a[226:227], v199 offset:43008\n"
"s_nop 7\n"
"v_mfma_f32_16x16x32_bf16 v[96:99], a[228:231], v[60:63], v[96:99]\n"
"buffer_load_dwordx4 v204, s[32:35], s63 offen nt lds\n"
"v_mfma_f32_16x16x32_bf16 v[96:99], a[232:235], v[72:75], v[96:99]\n"
"ds_read_b64 a[228:229], v198 offset:43520\n"
"ds_read_b64 a[230:231], v199 offset:43520\n"
"v_mfma_f32_16x16x32_bf16 v[96:99], a[236:239], v[84:87], v[96:99]\n"
"s_waitcnt lgkmcnt(10)\n"
"ds_read_b64 a[232:233], v198 offset:47104\n"
"ds_read_b64 a[234:235], v199 offset:47104\n"
"v_mfma_f32_16x16x32_bf16 v[108:111], a[208:211], v[0:3], v[208:211]\n"
"s_waitcnt lgkmcnt(10)\n"
"ds_read_b64 a[236:237], v198 offset:47616\n"
"ds_read_b64 a[238:239], v199 offset:47616\n"
"v_mfma_f32_16x16x32_bf16 v[108:111], a[212:215], v[12:15], v[108:111]\n"
"s_waitcnt lgkmcnt(10)\n"
"v_mfma_f32_16x16x32_bf16 v[108:111], a[216:219], v[24:27], v[108:111]\n"
"s_waitcnt lgkmcnt(8)\n"
"v_mfma_f32_16x16x32_bf16 v[108:111], a[220:223], v[36:39], v[108:111]\n"
"v_mfma_f32_16x16x32_bf16 v[112:115], a[220:223], v[40:43], v[212:215]\n"
"v_mfma_f32_16x16x32_bf16 v[112:115], a[216:219], v[28:31], v[112:115]\n"
"v_mfma_f32_16x16x32_bf16 v[112:115], a[212:215], v[16:19], v[112:115]\n"
"s_waitcnt vmcnt(13)\n"
"s_barrier\n"
"v_mfma_f32_16x16x32_bf16 v[112:115], a[208:211], v[4:7], v[112:115]\n"
"v_mfma_f32_16x16x32_bf16 v[116:119], a[208:211], v[8:11], v[216:219]\n"
"s_add_u32 m0, s60, 118656\n"
"ds_read_b64_tr_b16 a[208:209], v192 offset:49152\n"
"ds_read_b64_tr_b16 a[210:211], v192 offset:51200\n"
"v_mfma_f32_16x16x32_bf16 v[116:119], a[212:215], v[20:23], v[116:119]\n"
"buffer_load_dwordx4 v204, s[32:35], s63 offen offset:128 nt lds\n"
"v_mfma_f32_16x16x32_bf16 v[116:119], a[216:219], v[32:35], v[116:119]\n"
"ds_read_b64_tr_b16 a[212:213], v193 offset:49152\n"
"ds_read_b64_tr_b16 a[214:215], v193 offset:51200\n"
"s_nop 7\n"
"v_mfma_f32_16x16x32_bf16 v[116:119], a[220:223], v[44:47], v[116:119]\n"
"ds_read_b64_tr_b16 a[216:217], v192 offset:49664\n"
"ds_read_b64_tr_b16 a[218:219], v192 offset:51712\n"
"v_mfma_f32_16x16x32_bf16 a[192:195], a[204:207], v[156:159], a[0:3]\n"
"ds_read_b64_tr_b16 a[220:221], v193 offset:49664\n"
"s_waitcnt lgkmcnt(14)\n"
"ds_read_b64_tr_b16 a[222:223], v193 offset:51712\n"
"v_mfma_f32_16x16x32_bf16 a[196:199], a[204:207], v[160:163], a[4:7]\n"
"v_mfma_f32_16x16x32_bf16 a[200:203], a[204:207], v[164:167], a[8:11]\n"
"s_waitcnt lgkmcnt(14)\n"
"v_mfma_f32_16x16x32_bf16 v[116:119], a[224:227], v[56:59], v[116:119]\n"
"s_waitcnt lgkmcnt(12)\n"
"v_mfma_f32_16x16x32_bf16 v[116:119], a[228:231], v[68:71], v[116:119]\n"
"s_waitcnt lgkmcnt(10)\n"
"v_mfma_f32_16x16x32_bf16 v[116:119], a[232:235], v[80:83], v[116:119]\n"
"s_waitcnt lgkmcnt(8)\n"
"v_accvgpr_read_b32 v220, a192\n"
"v_accvgpr_read_b32 v221, a196\n"
"v_mfma_f32_16x16x32_bf16 v[116:119], a[236:239], v[92:95], v[116:119]\n"
"v_accvgpr_read_b32 v222, a200\n"
"v_add_f32 v220, v220, v221\n"
"v_mfma_f32_16x16x32_bf16 v[112:115], a[236:239], v[88:91], v[112:115]\n"
"v_add_f32 v220, v220, v222\n"
"v_cmp_ngt_f32 vcc, 0x47800000, v220\n"
"v_mfma_f32_16x16x32_bf16 v[112:115], a[232:235], v[76:79], v[112:115]\n"
"v_mfma_f32_16x16x32_bf16 v[112:115], a[228:231], v[64:67], v[112:115]\n"
"s_add_u32 m0, s60, 122624\n"
"v_mfma_f32_16x16x32_bf16 v[112:115], a[224:227], v[52:55], v[112:115]\n"
"buffer_load_dwordx4 v204, s[32:35], s63 offen offset:256 nt lds\n"
"v_mfma_f32_16x16x32_bf16 v[108:111], a[224:227], v[48:51], v[108:111]\n"
"ds_read_b64_tr_b16 a[224:225], v192 offset:53248\n"
"ds_read_b64_tr_b16 a[226:227], v192 offset:55296\n"
"v_mfma_f32_16x16x32_bf16 v[108:111], a[228:231], v[60:63], v[108:111]\n"
"ds_read_b64_tr_b16 a[228:229], v193 offset:53248\n"
"ds_read_b64_tr_b16 a[230:231], v193 offset:55296\n"
"s_nop 7\n"
"v_mfma_f32_16x16x32_bf16 v[108:111], a[232:235], v[72:75], v[108:111]\n"
"ds_read_b64_tr_b16 a[232:233], v192 offset:53760\n"
"ds_read_b64_tr_b16 a[234:235], v192 offset:55808\n"
"v_mfma_f32_16x16x32_bf16 v[108:111], a[236:239], v[84:87], v[108:111]\n"
"s_waitcnt lgkmcnt(12)\n"
"s_cbranch_vccnz Lstub7\n"
"Lback7:\n"
"ds_read_b64_tr_b16 a[236:237], v193 offset:53760\n"
"v_mfma_f32_16x16x32_bf16 v[168:171], a[208:211], v[156:159], v[168:171]\n"
"s_waitcnt lgkmcnt(11)\n"
"ds_read_b64_tr_b16 a[238:239], v193 offset:55808\n"
"v_mfma_f32_16x16x32_bf16 v[180:183], a[212:215], v[156:159], v[180:183]\n"
"s_waitcnt lgkmcnt(10)\n"
"v_exp_f32 v223, v96\n"
"v_mfma_f32_16x16x32_bf16 v[228:231], a[216:219], v[156:159], v[228:231]\n"
"s_waitcnt lgkmcnt(8)\n"
"v_exp_f32 v224, v97\n"
"v_mfma_f32_16x16x32_bf16 v[240:243], a[220:223], v[156:159], v[240:243]\n"
"v_exp_f32 v225, v98\n"
"v_mfma_f32_16x16x32_bf16 v[244:247], a[220:223], v[160:163], v[244:247]\n"
"v_exp_f32 v226, v99\n"
"v_mfma_f32_16x16x32_bf16 v[232:235], a[216:219], v[160:163], v[232:235]\n"
"v_exp_f32 v227, v100\n"
"v_mfma_f32_16x16x32_bf16 v[184:187], a[212:215], v[160:163], v[184:187]\n"
"v_exp_f32 v220, v101\n"
"v_mfma_f32_16x16x32_bf16 v[172:175], a[208:211], v[160:163], v[172:175]\n"
"s_add_u32 m0, s60, 126592\n"
"v_cvt_pk_bf16_f32 v144, v223, v224\n"
"v_cvt_pk_bf16_f32 v145, v225, v226\n"
"v_mfma_f32_16x16x32_bf16 v[176:179], a[208:211], v[164:167], v[176:179]\n"
"buffer_load_dwordx4 v204, s[32:35], s63 offen offset:384 nt lds\n"
"v_mfma_f32_16x16x32_bf16 v[188:191], a[212:215], v[164:167], v[188:191]\n"
"ds_read_b64_tr_b16 a[208:209], v192 offset:57344\n"
"ds_read_b64_tr_b16 a[210:211], v192 offset:59392\n"
"v_mfma_f32_16x16x32_bf16 v[236:239], a[216:219], v[164:167], v[236:239]\n"
"ds_read_b64_tr_b16 a[212:213], v193 offset:57344\n"
"ds_read_b64_tr_b16 a[214:215], v193 offset:59392\n"
"v_mfma_f32_16x16x32_bf16 v[248:251], a[220:223], v[164:167], v[248:251]\n"
"s_waitcnt lgkmcnt(10)\n"
"ds_read_b64_tr_b16 a[216:217], v192 offset:57856\n"
"ds_read_b64_tr_b16 a[218:219], v192 offset:59904\n"
"s_nop 7\n"
"v_mfma_f32_16x16x32_bf16 a[56:59], a[224:227], v[164:167], a[56:59]\n"
"s_waitcnt lgkmcnt(10)\n"
"ds_read_b64_tr_b16 a[220:221], v193 offset:57856\n"
"ds_read_b64_tr_b16 a[222:223], v193 offset:59904\n"
"v_mfma_f32_16x16x32_bf16 a[68:71], a[228:231], v[164:167], a[68:71]\n"
"s_waitcnt lgkmcnt(10)\n"
"v_exp_f32 v221, v102\n"
"v_mfma_f32_16x16x32_bf16 a[80:83], a[232:235], v[164:167], a[80:83]\n"
"s_waitcnt lgkmcnt(8)\n"
"v_exp_f32 v222, v103\n"
"v_mfma_f32_16x16x32_bf16 a[92:95], a[236:239], v[164:167], a[92:95]\n"
"v_exp_f32 v223, v104\n"
"v_mfma_f32_16x16x32_bf16 a[88:91], a[236:239], v[160:163], a[88:91]\n"
"v_exp_f32 v224, v105\n"
"v_mfma_f32_16x16x32_bf16 a[76:79], a[232:235], v[160:163], a[76:79]\n"
"v_cvt_pk_bf16_f32 v148, v227, v220\n"
"v_cvt_pk_bf16_f32 v149, v221, v222\n"
"v_mfma_f32_16x16x32_bf16 a[64:67], a[228:231], v[160:163], a[64:67]\n"
"v_exp_f32 v225, v106\n"
"v_mfma_f32_16x16x32_bf16 a[52:55], a[224:227], v[160:163], a[52:55]\n"
"s_mov_b32 s63, 180224\n"
"v_exp_f32 v226, v107\n"
"v_mfma_f32_16x16x32_bf16 a[48:51], a[224:227], v[156:159], a[48:51]\n"
"s_add_u32 m0, s60, 131072\n"
"ds_read_b64_tr_b16 a[224:225], v192 offset:61440\n"
"ds_read_b64_tr_b16 a[226:227], v192 offset:63488\n"
"v_mfma_f32_16x16x32_bf16 a[60:63], a[228:231], v[156:159], a[60:63]\n"
"buffer_load_dwordx4 v205, s[28:31], s63 offen nt lds\n"
"v_mfma_f32_16x16x32_bf16 a[72:75], a[232:235], v[156:159], a[72:75]\n"
"ds_read_b64_tr_b16 a[228:229], v193 offset:61440\n"
"ds_read_b64_tr_b16 a[230:231], v193 offset:63488\n"
"v_mfma_f32_16x16x32_bf16 a[84:87], a[236:239], v[156:159], a[84:87]\n"
"s_waitcnt lgkmcnt(10)\n"
"ds_read_b64_tr_b16 a[232:233], v192 offset:61952\n"
"ds_read_b64_tr_b16 a[234:235], v192 offset:64000\n"
"v_mfma_f32_16x16x32_bf16 a[96:99], a[208:211], v[156:159], a[96:99]\n"
"s_waitcnt lgkmcnt(10)\n"
"ds_read_b64_tr_b16 a[236:237], v193 offset:61952\n"
"ds_read_b64_tr_b16 a[238:239], v193 offset:64000\n"
"v_mfma_f32_16x16x32_bf16 a[108:111], a[212:215], v[156:159], a[108:111]\n"
"s_waitcnt lgkmcnt(10)\n"
"v_exp_f32 v227, v108\n"
"s_nop 7\n"
"v_mfma_f32_16x16x32_bf16 a[120:123], a[216:219], v[156:159], a[120:123]\n"
"s_waitcnt lgkmcnt(8)\n"
"v_exp_f32 v220, v109\n"
"v_mfma_f32_16x16x32_bf16 a[132:135], a[220:223], v[156:159], a[132:135]\n"
"v_cvt_pk_bf16_f32 v152, v223, v224\n"
"v_cvt_pk_bf16_f32 v153, v225, v226\n"
"v_mfma_f32_16x16x32_bf16 a[136:139], a[220:223], v[160:163], a[136:139]\n"
"v_exp_f32 v221, v110\n"
"v_mfma_f32_16x16x32_bf16 a[124:127], a[216:219], v[160:163], a[124:127]\n"
"v_exp_f32 v222, v111\n"
"v_mfma_f32_16x16x32_bf16 a[112:115], a[212:215], v[160:163], a[112:115]\n"
"s_waitcnt vmcnt(13)\n"
"s_barrier\n"
"v_mfma_f32_16x16x32_bf16 a[100:103], a[208:211], v[160:163], a[100:103]\n"
"v_exp_f32 v223, v112\n"
"v_mfma_f32_16x16x32_bf16 a[104:107], a[208:211], v[164:167], a[104:107]\n"
"s_add_u32 m0, s60, 135040\n"
"ds_read_b64 a[208:209], v200 offset:0\n"
"ds_read_b64 a[210:211], v201 offset:0\n"
"v_mfma_f32_16x16x32_bf16 a[116:119], a[212:215], v[164:167], a[116:119]\n"
"buffer_load_dwordx4 v205, s[28:31], s63 offen offset:128 nt lds\n"
"v_mfma_f32_16x16x32_bf16 a[128:131], a[216:219], v[164:167], a[128:131]\n"
"ds_read_b64 a[212:213], v200 offset:512\n"
"ds_read_b64 a[214:215], v201 offset:512\n"
"v_mfma_f32_16x16x32_bf16 a[140:143], a[220:223], v[164:167], a[140:143]\n"
"s_waitcnt lgkmcnt(10)\n"
"ds_read_b64 a[216:217], v200 offset:4096\n"
"ds_read_b64 a[218:219], v201 offset:4096\n"
"v_mfma_f32_16x16x32_bf16 a[152:155], a[224:227], v[164:167], a[152:155]\n"
"s_waitcnt lgkmcnt(10)\n"
"ds_read_b64 a[220:221], v200 offset:4608\n"
"ds_read_b64 a[222:223], v201 offset:4608\n"
"v_mfma_f32_16x16x32_bf16 a[164:167], a[228:231], v[164:167], a[164:167]\n"
"s_waitcnt lgkmcnt(10)\n"
"v_exp_f32 v224, v113\n"
"v_mfma_f32_16x16x32_bf16 a[176:179], a[232:235], v[164:167], a[176:179]\n"
"s_waitcnt lgkmcnt(8)\n"
"v_cvt_pk_bf16_f32 v146, v227, v220\n"
"v_cvt_pk_bf16_f32 v147, v221, v222\n"
"v_mfma_f32_16x16x32_bf16 a[188:191], a[236:239], v[164:167], a[188:191]\n"
"v_exp_f32 v225, v114\n"
"s_nop 7\n"
"v_mfma_f32_16x16x32_bf16 a[184:187], a[236:239], v[160:163], a[184:187]\n"
"v_exp_f32 v226, v115\n"
"v_mfma_f32_16x16x32_bf16 a[172:175], a[232:235], v[160:163], a[172:175]\n"
"v_exp_f32 v227, v116\n"
"v_mfma_f32_16x16x32_bf16 a[160:163], a[228:231], v[160:163], a[160:163]\n"
"v_exp_f32 v220, v117\n"
"v_mfma_f32_16x16x32_bf16 a[148:151], a[224:227], v[160:163], a[148:151]\n"
"v_cvt_pk_bf16_f32 v150, v223, v224\n"
"v_cvt_pk_bf16_f32 v151, v225, v226\n"
"v_mfma_f32_16x16x32_bf16 a[144:147], a[224:227], v[156:159], a[144:147]\n"
"ds_read_b64 a[224:225], v200 offset:8192\n"
"ds_read_b64 a[226:227], v201 offset:8192\n"
"v_mfma_f32_16x16x32_bf16 a[156:159], a[228:231], v[156:159], a[156:159]\n"
"s_add_u32 m0, s60, 139008\n"
"ds_read_b64 a[228:229], v200 offset:8704\n"
"ds_read_b64 a[230:231], v201 offset:8704\n"
"v_mfma_f32_16x16x32_bf16 a[168:171], a[232:235], v[156:159], a[168:171]\n"
"buffer_load_dwordx4 v205, s[28:31], s63 offen offset:256 nt lds\n"
"v_mfma_f32_16x16x32_bf16 a[180:183], a[236:239], v[156:159], a[180:183]\n"
"s_waitcnt lgkmcnt(10)\n"
"ds_read_b64 a[232:233], v200 offset:12288\n"
"ds_read_b64 a[234:235], v201 offset:12288\n"
"v_mfma_f32_16x16x32_bf16 v[120:123], a[208:211], v[0:3], v[208:211]\n"
"s_waitcnt lgkmcnt(10)\n"
"ds_read_b64 a[236:237], v200 offset:12800\n"
"ds_read_b64 a[238:239], v201 offset:12800\n"
"v_mfma_f32_16x16x32_bf16 v[120:123], a[212:215], v[12:15], v[120:123]\n"
"s_waitcnt lgkmcnt(10)\n"
"v_exp_f32 v221, v118\n"
"v_mfma_f32_16x16x32_bf16 v[120:123], a[216:219], v[24:27], v[120:123]\n"
"s_waitcnt lgkmcnt(8)\n"
"v_exp_f32 v222, v119\n"
"v_mfma_f32_16x16x32_bf16 v[120:123], a[220:223], v[36:39], v[120:123]\n"
"v_cvt_pk_bf16_f32 v154, v227, v220\n"
"v_cvt_pk_bf16_f32 v155, v221, v222\n"
"v_mfma_f32_16x16x32_bf16 v[124:127], a[220:223], v[40:43], v[212:215]\n"
"v_mfma_f32_16x16x32_bf16 v[124:127], a[216:219], v[28:31], v[124:127]\n"
"s_nop 7\n"
"v_mfma_f32_16x16x32_bf16 v[124:127], a[212:215], v[16:19], v[124:127]\n"
"v_mfma_f32_16x16x32_bf16 v[124:127], a[208:211], v[4:7], v[124:127]\n"
"v_mfma_f32_16x16x32_bf16 v[128:131], a[208:211], v[8:11], v[216:219]\n"
"ds_read_b64 a[208:209], v200 offset:2048\n"
"ds_read_b64 a[210:211], v201 offset:2048\n"
"v_mfma_f32_16x16x32_bf16 v[128:131], a[212:215], v[20:23], v[128:131]\n"
"ds_read_b64 a[212:213], v200 offset:2560\n"
"ds_read_b64 a[214:215], v201 offset:2560\n"
"v_mfma_f32_16x16x32_bf16 v[128:131], a[216:219], v[32:35], v[128:131]\n"
"s_add_u32 m0, s60, 142976\n"
"ds_read_b64 a[216:217], v200 offset:6144\n"
"ds_read_b64 a[218:219], v201 offset:6144\n"
"v_mfma_f32_16x16x32_bf16 v[128:131], a[220:223], v[44:47], v[128:131]\n"
"s_waitcnt lgkmcnt(12)\n"
"buffer_load_dwordx4 v205, s[28:31], s63 offen offset:384 nt lds\n"
"v_mfma_f32_16x16x32_bf16 v[128:131], a[224:227], v[56:59], v[128:131]\n"
"s_waitcnt lgkmcnt(10)\n"
"ds_read_b64 a[220:221], v200 offset:6656\n"
"ds_read_b64 a[222:223], v201 offset:6656\n"
"v_mfma_f32_16x16x32_bf16 v[128:131], a[228:231], v[68:71], v[128:131]\n"
"s_waitcnt lgkmcnt(10)\n"
"v_mfma_f32_16x16x32_bf16 v[128:131], a[232:235], v[80:83], v[128:131]\n"
"s_waitcnt lgkmcnt(8)\n"
"v_mfma_f32_16x16x32_bf16 v[128:131], a[236:239], v[92:95], v[128:131]\n"
"v_mfma_f32_16x16x32_bf16 v[124:127], a[236:239], v[88:91], v[124:127]\n"
"v_mfma_f32_16x16x32_bf16 v[124:127], a[232:235], v[76:79], v[124:127]\n"
"v_mfma_f32_16x16x32_bf16 v[124:127], a[228:231], v[64:67], v[124:127]\n"
"v_mfma_f32_16x16x32_bf16 v[124:127], a[224:227], v[52:55], v[124:127]\n"
"s_nop 7\n"
"v_mfma_f32_16x16x32_bf16 v[120:123], a[224:227], v[48:51], v[120:123]\n"
"ds_read_b64 a[224:225], v200 offset:10240\n"
"ds_read_b64 a[226:227], v201 offset:10240\n"
"v_mfma_f32_16x16x32_bf16 v[120:123], a[228:231], v[60:63], v[120:123]\n"
"ds_read_b64 a[228:229], v200 offset:10752\n"
"ds_read_b64 a[230:231], v201 offset:10752\n"
"v_mfma_f32_16x16x32_bf16 v[120:123], a[232:235], v[72:75], v[120:123]\n"
"s_mov_b32 s63, 163840\n"
"ds_read_b64 a[232:233], v200 offset:14336\n"
"ds_read_b64 a[234:235], v201 offset:14336\n"
"v_mfma_f32_16x16x32_bf16 v[120:123], a[236:239], v[84:87], v[120:123]\n"
"s_waitcnt lgkmcnt(12)\n"
"s_add_u32 m0, s60, 147456\n"
"ds_read_b64 a[236:237], v200 offset:14848\n"
"v_mfma_f32_16x16x32_bf16 v[132:135], a[208:211], v[0:3], v[208:211]\n"
"s_waitcnt lgkmcnt(11)\n"
"buffer_load_dwordx4 v204, s[32:35], s63 offen nt lds\n"
"v_mfma_f32_16x16x32_bf16 v[132:135], a[212:215], v[12:15], v[132:135]\n"
"s_waitcnt lgkmcnt(9)\n"
"ds_read_b64 a[238:239], v201 offset:14848\n"
"v_mfma_f32_16x16x32_bf16 v[132:135], a[216:219], v[24:27], v[132:135]\n"
"s_waitcnt lgkmcnt(8)\n"
"v_mfma_f32_16x16x32_bf16 v[132:135], a[220:223], v[36:39], v[132:135]\n"
"v_mfma_f32_16x16x32_bf16 v[136:139], a[220:223], v[40:43], v[212:215]\n"
"v_mfma_f32_16x16x32_bf16 v[136:139], a[216:219], v[28:31], v[136:139]\n"
"v_mfma_f32_16x16x32_bf16 v[136:139], a[212:215], v[16:19], v[136:139]\n"
"s_waitcnt vmcnt(13)\n"
"s_barrier\n"
"v_mfma_f32_16x16x32_bf16 v[136:139], a[208:211], v[4:7], v[136:139]\n"
"v_mfma_f32_16x16x32_bf16 v[140:143], a[208:211], v[8:11], v[216:219]\n"
"ds_read_b64_tr_b16 a[208:209], v194 offset:16384\n"
"ds_read_b64_tr_b16 a[210:211], v194 offset:18432\n"
"v_mfma_f32_16x16x32_bf16 v[140:143], a[212:215], v[20:23], v[140:143]\n"
"ds_read_b64_tr_b16 a[212:213], v195 offset:16384\n"
"ds_read_b64_tr_b16 a[214:215], v195 offset:18432\n"
"s_nop 7\n"
"v_mfma_f32_16x16x32_bf16 v[140:143], a[216:219], v[32:35], v[140:143]\n"
"ds_read_b64_tr_b16 a[216:217], v194 offset:16896\n"
"ds_read_b64_tr_b16 a[218:219], v194 offset:18944\n"
"v_mfma_f32_16x16x32_bf16 v[140:143], a[220:223], v[44:47], v[140:143]\n"
"s_add_u32 m0, s60, 151424\n"
"ds_read_b64_tr_b16 a[220:221], v195 offset:16896\n"
"s_waitcnt lgkmcnt(14)\n"
"ds_read_b64_tr_b16 a[222:223], v195 offset:18944\n"
"v_mfma_f32_16x16x32_bf16 a[0:3], a[204:207], v[144:147], a[192:195]\n"
"buffer_load_dwordx4 v204, s[32:35], s63 offen offset:128 nt lds\n"
"v_mfma_f32_16x16x32_bf16 a[4:7], a[204:207], v[148:151], a[196:199]\n"
"v_mfma_f32_16x16x32_bf16 a[8:11], a[204:207], v[152:155], a[200:203]\n"
"s_waitcnt lgkmcnt(14)\n"
"v_mfma_f32_16x16x32_bf16 v[140:143], a[224:227], v[56:59], v[140:143]\n"
"s_waitcnt lgkmcnt(12)\n"
"v_mfma_f32_16x16x32_bf16 v[140:143], a[228:231], v[68:71], v[140:143]\n"
"s_waitcnt lgkmcnt(10)\n"
"v_mfma_f32_16x16x32_bf16 v[140:143], a[232:235], v[80:83], v[140:143]\n"
"s_waitcnt lgkmcnt(8)\n"
"v_accvgpr_read_b32 v223, a0\n"
"v_accvgpr_read_b32 v224, a4\n"
"v_mfma_f32_16x16x32_bf16 v[140:143], a[236:239], v[92:95], v[140:143]\n"
"v_accvgpr_read_b32 v225, a8\n"
"v_add_f32 v223, v223, v224\n"
"v_mfma_f32_16x16x32_bf16 v[136:139], a[236:239], v[88:91], v[136:139]\n"
"v_add_f32 v223, v223, v225\n"
"v_cmp_ngt_f32 vcc, 0x47800000, v223\n"
"v_mfma_f32_16x16x32_bf16 v[136:139], a[232:235], v[76:79], v[136:139]\n"
"v_mfma_f32_16x16x32_bf16 v[136:139], a[228:231], v[64:67], v[136:139]\n"
"v_mfma_f32_16x16x32_bf16 v[136:139], a[224:227], v[52:55], v[136:139]\n"
"v_mfma_f32_16x16x32_bf16 v[132:135], a[224:227], v[48:51], v[132:135]\n"
"ds_read_b64_tr_b16 a[224:225], v194 offset:20480\n"
"ds_read_b64_tr_b16 a[226:227], v194 offset:22528\n"
"s_nop 7\n"
"v_mfma_f32_16x16x32_bf16 v[132:135], a[228:231], v[60:63], v[132:135]\n"
"s_add_u32 m0, s60, 155392\n"
"ds_read_b64_tr_b16 a[228:229], v195 offset:20480\n"
"ds_read_b64_tr_b16 a[230:231], v195 offset:22528\n"
"v_mfma_f32_16x16x32_bf16 v[132:135], a[232:235], v[72:75], v[132:135]\n"
"buffer_load_dwordx4 v204, s[32:35], s63 offen offset:256 nt lds\n"
"v_mfma_f32_16x16x32_bf16 v[132:135], a[236:239], v[84:87], v[132:135]\n"
"s_waitcnt lgkmcnt(10)\n"
"s_cbranch_vccnz Lstub8\n"
"Lback8:\n"
"ds_read_b64_tr_b16 a[232:233], v194 offset:20992\n"
"v_mfma_f32_16x16x32_bf16 v[168:171], a[208:211], v[144:147], v[168:171]\n"
"s_waitcnt lgkmcnt(9)\n"
"ds_read_b64_tr_b16 a[234:235], v194 offset:23040\n"
"ds_read_b64_tr_b16 a[236:237], v195 offset:20992\n"
"v_mfma_f32_16x16x32_bf16 v[180:183], a[212:215], v[144:147], v[180:183]\n"
"s_waitcnt lgkmcnt(9)\n"
"ds_read_b64_tr_b16 a[238:239], v195 offset:23040\n"
"v_mfma_f32_16x16x32_bf16 v[228:231], a[216:219], v[144:147], v[228:231]\n"
"s_waitcnt lgkmcnt(8)\n"
"v_exp_f32 v226, v120\n"
"v_mfma_f32_16x16x32_bf16 v[240:243], a[220:223], v[144:147], v[240:243]\n"
"v_exp_f32 v227, v121\n"
"v_mfma_f32_16x16x32_bf16 v[244:247], a[220:223], v[148:151], v[244:247]\n"
"v_exp_f32 v220, v122\n"
"v_mfma_f32_16x16x32_bf16 v[232:235], a[216:219], v[148:151], v[232:235]\n"
"v_exp_f32 v221, v123\n"
"v_mfma_f32_16x16x32_bf16 v[184:187], a[212:215], v[148:151], v[184:187]\n"
"v_exp_f32 v222, v124\n"
"v_mfma_f32_16x16x32_bf16 v[172:175], a[208:211], v[148:151], v[172:175]\n"
"v_exp_f32 v223, v125\n"
"v_mfma_f32_16x16x32_bf16 v[176:179], a[208:211], v[152:155], v[176:179]\n"
"ds_read_b64_tr_b16 a[208:209], v194 offset:24576\n"
"ds_read_b64_tr_b16 a[210:211], v194 offset:26624\n"
"v_mfma_f32_16x16x32_bf16 v[188:191], a[212:215], v[152:155], v[188:191]\n"
"ds_read_b64_tr_b16 a[212:213], v195 offset:24576\n"
"ds_read_b64_tr_b16 a[214:215], v195 offset:26624\n"
"v_mfma_f32_16x16x32_bf16 v[236:239], a[216:219], v[152:155], v[236:239]\n"
"s_add_u32 m0, s60, 159360\n"
"ds_read_b64_tr_b16 a[216:217], v194 offset:25088\n"
"ds_read_b64_tr_b16 a[218:219], v194 offset:27136\n"
"s_nop 7\n"
"v_mfma_f32_16x16x32_bf16 v[248:251], a[220:223], v[152:155], v[248:251]\n"
"s_waitcnt lgkmcnt(12)\n"
"buffer_load_dwordx4 v204, s[32:35], s63 offen offset:384 nt lds\n"
"v_mfma_f32_16x16x32_bf16 a[56:59], a[224:227], v[152:155], a[56:59]\n"
"s_waitcnt lgkmcnt(10)\n"
"ds_read_b64_tr_b16 a[220:221], v195 offset:25088\n"
"ds_read_b64_tr_b16 a[222:223], v195 offset:27136\n"
"v_mfma_f32_16x16x32_bf16 a[68:71], a[228:231], v[152:155], a[68:71]\n"
"s_waitcnt lgkmcnt(10)\n"
"v_cvt_pk_bf16_f32 v156, v226, v227\n"
"v_cvt_pk_bf16_f32 v157, v220, v221\n"
"v_mfma_f32_16x16x32_bf16 a[80:83], a[232:235], v[152:155], a[80:83]\n"
"s_waitcnt lgkmcnt(8)\n"
"v_exp_f32 v224, v126\n"
"v_mfma_f32_16x16x32_bf16 a[92:95], a[236:239], v[152:155], a[92:95]\n"
"v_exp_f32 v225, v127\n"
"v_mfma_f32_16x16x32_bf16 a[88:91], a[236:239], v[148:151], a[88:91]\n"
"v_exp_f32 v226, v128\n"
"v_mfma_f32_16x16x32_bf16 a[76:79], a[232:235], v[148:151], a[76:79]\n"
"v_exp_f32 v227, v129\n"
"v_mfma_f32_16x16x32_bf16 a[64:67], a[228:231], v[148:151], a[64:67]\n"
"v_cvt_pk_bf16_f32 v160, v222, v223\n"
"v_cvt_pk_bf16_f32 v161, v224, v225\n"
"v_mfma_f32_16x16x32_bf16 a[52:55], a[224:227], v[148:151], a[52:55]\n"
"v_exp_f32 v220, v130\n"
"v_mfma_f32_16x16x32_bf16 a[48:51], a[224:227], v[144:147], a[48:51]\n"
"ds_read_b64_tr_b16 a[224:225], v194 offset:28672\n"
"ds_read_b64_tr_b16 a[226:227], v194 offset:30720\n"
"v_mfma_f32_16x16x32_bf16 a[60:63], a[228:231], v[144:147], a[60:63]\n"
"ds_read_b64_tr_b16 a[228:229], v195 offset:28672\n"
"ds_read_b64_tr_b16 a[230:231], v195 offset:30720\n"
"v_mfma_f32_16x16x32_bf16 a[72:75], a[232:235], v[144:147], a[72:75]\n"
"s_mov_b32 s63, 196608\n"
"ds_read_b64_tr_b16 a[232:233], v194 offset:29184\n"
"ds_read_b64_tr_b16 a[234:235], v194 offset:31232\n"
"v_mfma_f32_16x16x32_bf16 a[84:87], a[236:239], v[144:147], a[84:87]\n"
"s_waitcnt lgkmcnt(12)\n"
"s_add_u32 m0, s60, 0\n"
"ds_read_b64_tr_b16 a[236:237], v195 offset:29184\n"
"v_mfma_f32_16x16x32_bf16 a[96:99], a[208:211], v[144:147], a[96:99]\n"
"s_waitcnt lgkmcnt(11)\n"
"buffer_load_dwordx4 v205, s[28:31], s63 offen nt lds\n"
"s_nop 7\n"
"v_mfma_f32_16x16x32_bf16 a[108:111], a[212:215], v[144:147], a[108:111]\n"
"s_waitcnt lgkmcnt(9)\n"
"ds_read_b64_tr_b16 a[238:239], v195 offset:31232\n"
"v_mfma_f32_16x16x32_bf16 a[120:123], a[216:219], v[144:147], a[120:123]\n"
"s_waitcnt lgkmcnt(8)\n"
"v_exp_f32 v221, v131\n"
"v_mfma_f32_16x16x32_bf16 a[132:135], a[220:223], v[144:147], a[132:135]\n"
"v_exp_f32 v222, v132\n"
"v_mfma_f32_16x16x32_bf16 a[136:139], a[220:223], v[148:151], a[136:139]\n"
"v_exp_f32 v223, v133\n"
"v_mfma_f32_16x16x32_bf16 a[124:127], a[216:219], v[148:151], a[124:127]\n"
"v_cvt_pk_bf16_f32 v164, v226, v227\n"
"v_cvt_pk_bf16_f32 v165, v220, v221\n"
"v_mfma_f32_16x16x32_bf16 a[112:115], a[212:215], v[148:151], a[112:115]\n"
"s_waitcnt vmcnt(13)\n"
"s_barrier\n"
"v_mfma_f32_16x16x32_bf16 a[100:103], a[208:211], v[148:151], a[100:103]\n"
"v_exp_f32 v224, v134\n"
"v_mfma_f32_16x16x32_bf16 a[104:107], a[208:211], v[152:155], a[104:107]\n"
"ds_read_b64 a[208:209], v200 offset:32768\n"
"ds_read_b64 a[210:211], v201 offset:32768\n"
"v_mfma_f32_16x16x32_bf16 a[116:119], a[212:215], v[152:155], a[116:119]\n"
"ds_read_b64 a[212:213], v200 offset:33280\n"
"ds_read_b64 a[214:215], v201 offset:33280\n"
"v_mfma_f32_16x16x32_bf16 a[128:131], a[216:219], v[152:155], a[128:131]\n"
"ds_read_b64 a[216:217], v200 offset:36864\n"
"ds_read_b64 a[218:219], v201 offset:36864\n"
"v_mfma_f32_16x16x32_bf16 a[140:143], a[220:223], v[152:155], a[140:143]\n"
"s_waitcnt lgkmcnt(12)\n"
"s_add_u32 m0, s60, 3968\n"
"ds_read_b64 a[220:221], v200 offset:37376\n"
"v_mfma_f32_16x16x32_bf16 a[152:155], a[224:227], v[152:155], a[152:155]\n"
"s_waitcnt lgkmcnt(11)\n"
"buffer_load_dwordx4 v205, s[28:31], s63 offen offset:128 nt lds\n"
"v_mfma_f32_16x16x32_bf16 a[164:167], a[228:231], v[152:155], a[164:167]\n"
"s_waitcnt lgkmcnt(9)\n"
"ds_read_b64 a[222:223], v201 offset:37376\n"
"v_mfma_f32_16x16x32_bf16 a[176:179], a[232:235], v[152:155], a[176:179]\n"
"s_waitcnt lgkmcnt(8)\n"
"v_exp_f32 v225, v135\n"
"s_nop 7\n"
"v_mfma_f32_16x16x32_bf16 a[188:191], a[236:239], v[152:155], a[188:191]\n"
"v_exp_f32 v226, v136\n"
"v_mfma_f32_16x16x32_bf16 a[184:187], a[236:239], v[148:151], a[184:187]\n"
"v_exp_f32 v227, v137\n"
"v_mfma_f32_16x16x32_bf16 a[172:175], a[232:235], v[148:151], a[172:175]\n"
"v_cvt_pk_bf16_f32 v158, v222, v223\n"
"v_cvt_pk_bf16_f32 v159, v224, v225\n"
"v_mfma_f32_16x16x32_bf16 a[160:163], a[228:231], v[148:151], a[160:163]\n"
"v_exp_f32 v220, v138\n"
"v_mfma_f32_16x16x32_bf16 a[148:151], a[224:227], v[148:151], a[148:151]\n"
"v_exp_f32 v221, v139\n"
"v_mfma_f32_16x16x32_bf16 a[144:147], a[224:227], v[144:147], a[144:147]\n"
"ds_read_b64 a[224:225], v200 offset:40960\n"
"ds_read_b64 a[226:227], v201 offset:40960\n"
"v_mfma_f32_16x16x32_bf16 a[156:159], a[228:231], v[144:147], a[156:159]\n"
"ds_read_b64 a[228:229], v200 offset:41472\n"
"ds_read_b64 a[230:231], v201 offset:41472\n"
"v_mfma_f32_16x16x32_bf16 a[168:171], a[232:235], v[144:147], a[168:171]\n"
"ds_read_b64 a[232:233], v200 offset:45056\n"
"ds_read_b64 a[234:235], v201 offset:45056\n"
"v_mfma_f32_16x16x32_bf16 a[180:183], a[236:239], v[144:147], a[180:183]\n"
"s_waitcnt lgkmcnt(12)\n"
"ds_read_b64 a[236:237], v200 offset:45568\n"
"ds_read_b64 a[238:239], v201 offset:45568\n"
"v_mfma_f32_16x16x32_bf16 v[96:99], a[208:211], v[0:3], v[208:211]\n"
"s_waitcnt lgkmcnt(12)\n"
"s_add_u32 m0, s60, 7936\n"
"v_mfma_f32_16x16x32_bf16 v[96:99], a[212:215], v[12:15], v[96:99]\n"
"s_waitcnt lgkmcnt(10)\n"
"buffer_load_dwordx4 v205, s[28:31], s63 offen offset:256 nt lds\n"
"v_mfma_f32_16x16x32_bf16 v[96:99], a[216:219], v[24:27], v[96:99]\n"
"s_waitcnt lgkmcnt(8)\n"
"v_exp_f32 v222, v140\n"
"v_mfma_f32_16x16x32_bf16 v[96:99], a[220:223], v[36:39], v[96:99]\n"
"v_exp_f32 v223, v141\n"
"v_mfma_f32_16x16x32_bf16 v[100:103], a[220:223], v[40:43], v[212:215]\n"
"v_cvt_pk_bf16_f32 v162, v226, v227\n"
"v_cvt_pk_bf16_f32 v163, v220, v221\n"
"s_nop 7\n"
"v_mfma_f32_16x16x32_bf16 v[100:103], a[216:219], v[28:31], v[100:103]\n"
"v_exp_f32 v224, v142\n"
"v_mfma_f32_16x16x32_bf16 v[100:103], a[212:215], v[16:19], v[100:103]\n"
"v_exp_f32 v225, v143\n"
"v_mfma_f32_16x16x32_bf16 v[100:103], a[208:211], v[4:7], v[100:103]\n"
"v_cvt_pk_bf16_f32 v166, v222, v223\n"
"v_cvt_pk_bf16_f32 v167, v224, v225\n"
"v_mfma_f32_16x16x32_bf16 v[104:107], a[208:211], v[8:11], v[216:219]\n"
"ds_read_b64 a[208:209], v200 offset:34816\n"
"ds_read_b64 a[210:211], v201 offset:34816\n"
"v_mfma_f32_16x16x32_bf16 v[104:107], a[212:215], v[20:23], v[104:107]\n"
"ds_read_b64 a[212:213], v200 offset:35328\n"
"ds_read_b64 a[214:215], v201 offset:35328\n"
"v_mfma_f32_16x16x32_bf16 v[104:107], a[216:219], v[32:35], v[104:107]\n"
"ds_read_b64 a[216:217], v200 offset:38912\n"
"ds_read_b64 a[218:219], v201 offset:38912\n"
"v_mfma_f32_16x16x32_bf16 v[104:107], a[220:223], v[44:47], v[104:107]\n"
"s_waitcnt lgkmcnt(12)\n"
"ds_read_b64 a[220:221], v200 offset:39424\n"
"ds_read_b64 a[222:223], v201 offset:39424\n"
"v_mfma_f32_16x16x32_bf16 v[104:107], a[224:227], v[56:59], v[104:107]\n"
"s_waitcnt lgkmcnt(12)\n"
"v_mfma_f32_16x16x32_bf16 v[104:107], a[228:231], v[68:71], v[104:107]\n"
"s_waitcnt lgkmcnt(10)\n"
"s_add_u32 m0, s60, 11904\n"
"v_mfma_f32_16x16x32_bf16 v[104:107], a[232:235], v[80:83], v[104:107]\n"
"s_waitcnt lgkmcnt(8)\n"
"buffer_load_dwordx4 v205, s[28:31], s63 offen offset:384 nt lds\n"
"v_mfma_f32_16x16x32_bf16 v[104:107], a[236:239], v[92:95], v[104:107]\n"
"v_mfma_f32_16x16x32_bf16 v[100:103], a[236:239], v[88:91], v[100:103]\n"
"v_mfma_f32_16x16x32_bf16 v[100:103], a[232:235], v[76:79], v[100:103]\n"
"v_mfma_f32_16x16x32_bf16 v[100:103], a[228:231], v[64:67], v[100:103]\n"
"s_nop 7\n"
"v_mfma_f32_16x16x32_bf16 v[100:103], a[224:227], v[52:55], v[100:103]\n"
"v_mfma_f32_16x16x32_bf16 v[96:99], a[224:227], v[48:51], v[96:99]\n"
"ds_read_b64 a[224:225], v200 offset:43008\n"
"ds_read_b64 a[226:227], v201 offset:43008\n"
"v_mfma_f32_16x16x32_bf16 v[96:99], a[228:231], v[60:63], v[96:99]\n"
"ds_read_b64 a[228:229], v200 offset:43520\n"
"ds_read_b64 a[230:231], v201 offset:43520\n"
"v_mfma_f32_16x16x32_bf16 v[96:99], a[232:235], v[72:75], v[96:99]\n"
"ds_read_b64 a[232:233], v200 offset:47104\n"
"ds_read_b64 a[234:235], v201 offset:47104\n"
"v_mfma_f32_16x16x32_bf16 v[96:99], a[236:239], v[84:87], v[96:99]\n"
"s_waitcnt lgkmcnt(12)\n"
"ds_read_b64 a[236:237], v200 offset:47616\n"
"ds_read_b64 a[238:239], v201 offset:47616\n"
"v_mfma_f32_16x16x32_bf16 v[108:111], a[208:211], v[0:3], v[208:211]\n"
"s_waitcnt lgkmcnt(12)\n"
"v_mfma_f32_16x16x32_bf16 v[108:111], a[212:215], v[12:15], v[108:111]\n"
"s_waitcnt lgkmcnt(10)\n"
"s_mov_b32 s63, 180224\n"
"v_mfma_f32_16x16x32_bf16 v[108:111], a[216:219], v[24:27], v[108:111]\n"
"s_waitcnt lgkmcnt(8)\n"
"s_add_u32 m0, s60, 16384\n"
"v_mfma_f32_16x16x32_bf16 v[108:111], a[220:223], v[36:39], v[108:111]\n"
"buffer_load_dwordx4 v204, s[32:35], s63 offen nt lds\n"
"v_mfma_f32_16x16x32_bf16 v[112:115], a[220:223], v[40:43], v[212:215]\n"
"v_mfma_f32_16x16x32_bf16 v[112:115], a[216:219], v[28:31], v[112:115]\n"
"v_mfma_f32_16x16x32_bf16 v[112:115], a[212:215], v[16:19], v[112:115]\n"
"s_waitcnt vmcnt(13)\n"
"s_barrier\n"
"v_mfma_f32_16x16x32_bf16 v[112:115], a[208:211], v[4:7], v[112:115]\n"
"v_mfma_f32_16x16x32_bf16 v[116:119], a[208:211], v[8:11], v[216:219]\n"
"ds_read_b64_tr_b16 a[208:209], v194 offset:49152\n"
"ds_read_b64_tr_b16 a[210:211], v194 offset:51200\n"
"s_nop 7\n"
"v_mfma_f32_16x16x32_bf16 v[116:119], a[212:215], v[20:23], v[116:119]\n"
"ds_read_b64_tr_b16 a[212:213], v195 offset:49152\n"
"ds_read_b64_tr_b16 a[214:215], v195 offset:51200\n"
"v_mfma_f32_16x16x32_bf16 v[116:119], a[216:219], v[32:35], v[116:119]\n"
"ds_read_b64_tr_b16 a[216:217], v194 offset:49664\n"
"ds_read_b64_tr_b16 a[218:219], v194 offset:51712\n"
"v_mfma_f32_16x16x32_bf16 v[116:119], a[220:223], v[44:47], v[116:119]\n"
"ds_read_b64_tr_b16 a[220:221], v195 offset:49664\n"
"s_waitcnt lgkmcnt(14)\n"
"ds_read_b64_tr_b16 a[222:223], v195 offset:51712\n"
"v_mfma_f32_16x16x32_bf16 a[192:195], a[204:207], v[156:159], a[0:3]\n"
"v_mfma_f32_16x16x32_bf16 a[196:199], a[204:207], v[160:163], a[4:7]\n"
"v_mfma_f32_16x16x32_bf16 a[200:203], a[204:207], v[164:167], a[8:11]\n"
"s_waitcnt lgkmcnt(14)\n"
"s_add_u32 m0, s60, 20352\n"
"v_mfma_f32_16x16x32_bf16 v[116:119], a[224:227], v[56:59], v[116:119]\n"
"s_waitcnt lgkmcnt(12)\n"
"buffer_load_dwordx4 v204, s[32:35], s63 offen offset:128 nt lds\n"
"v_mfma_f32_16x16x32_bf16 v[116:119], a[228:231], v[68:71], v[116:119]\n"
"s_waitcnt lgkmcnt(10)\n"
"v_mfma_f32_16x16x32_bf16 v[116:119], a[232:235], v[80:83], v[116:119]\n"
"s_waitcnt lgkmcnt(8)\n"
"v_accvgpr_read_b32 v226, a192\n"
"v_accvgpr_read_b32 v227, a196\n"
"v_mfma_f32_16x16x32_bf16 v[116:119], a[236:239], v[92:95], v[116:119]\n"
"v_accvgpr_read_b32 v220, a200\n"
"v_add_f32 v226, v226, v227\n"
"v_mfma_f32_16x16x32_bf16 v[112:115], a[236:239], v[88:91], v[112:115]\n"
"v_add_f32 v226, v226, v220\n"
"v_cmp_ngt_f32 vcc, 0x47800000, v226\n"
"v_mfma_f32_16x16x32_bf16 v[112:115], a[232:235], v[76:79], v[112:115]\n"
"v_mfma_f32_16x16x32_bf16 v[112:115], a[228:231], v[64:67], v[112:115]\n"
"v_mfma_f32_16x16x32_bf16 v[112:115], a[224:227], v[52:55], v[112:115]\n"
"s_nop 7\n"
"v_mfma_f32_16x16x32_bf16 v[108:111], a[224:227], v[48:51], v[108:111]\n"
"ds_read_b64_tr_b16 a[224:225], v194 offset:53248\n"
"ds_read_b64_tr_b16 a[226:227], v194 offset:55296\n"
"v_mfma_f32_16x16x32_bf16 v[108:111], a[228:231], v[60:63], v[108:111]\n"
"ds_read_b64_tr_b16 a[228:229], v195 offset:53248\n"
"ds_read_b64_tr_b16 a[230:231], v195 offset:55296\n"
"v_mfma_f32_16x16x32_bf16 v[108:111], a[232:235], v[72:75], v[108:111]\n"
"ds_read_b64_tr_b16 a[232:233], v194 offset:53760\n"
"ds_read_b64_tr_b16 a[234:235], v194 offset:55808\n"
"v_mfma_f32_16x16x32_bf16 v[108:111], a[236:239], v[84:87], v[108:111]\n"
"s_waitcnt lgkmcnt(12)\n"
"s_cbranch_vccnz Lstub9\n"
"Lback9:\n"
"ds_read_b64_tr_b16 a[236:237], v195 offset:53760\n"
"v_mfma_f32_16x16x32_bf16 v[168:171], a[208:211], v[156:159], v[168:171]\n"
"s_waitcnt lgkmcnt(11)\n"
"s_add_u32 m0, s60, 24320\n"
"ds_read_b64_tr_b16 a[238:239], v195 offset:55808\n"
"v_mfma_f32_16x16x32_bf16 v[180:183], a[212:215], v[156:159], v[180:183]\n"
"s_waitcnt lgkmcnt(10)\n"
"buffer_load_dwordx4 v204, s[32:35], s63 offen offset:256 nt lds\n"
"v_mfma_f32_16x16x32_bf16 v[228:231], a[216:219], v[156:159], v[228:231]\n"
"s_waitcnt lgkmcnt(8)\n"
"v_exp_f32 v221, v96\n"
"v_mfma_f32_16x16x32_bf16 v[240:243], a[220:223], v[156:159], v[240:243]\n"
"v_exp_f32 v222, v97\n"
"v_mfma_f32_16x16x32_bf16 v[244:247], a[220:223], v[160:163], v[244:247]\n"
"v_exp_f32 v223, v98\n"
"s_nop 7\n"
"v_mfma_f32_16x16x32_bf16 v[232:235], a[216:219], v[160:163], v[232:235]\n"
"v_exp_f32 v224, v99\n"
"v_mfma_f32_16x16x32_bf16 v[184:187], a[212:215], v[160:163], v[184:187]\n"
"v_exp_f32 v225, v100\n"
"v_mfma_f32_16x16x32_bf16 v[172:175], a[208:211], v[160:163], v[172:175]\n"
"v_exp_f32 v226, v101\n"
"v_mfma_f32_16x16x32_bf16 v[176:179], a[208:211], v[164:167], v[176:179]\n"
"ds_read_b64_tr_b16 a[208:209], v194 offset:57344\n"
"ds_read_b64_tr_b16 a[210:211], v194 offset:59392\n"
"v_mfma_f32_16x16x32_bf16 v[188:191], a[212:215], v[164:167], v[188:191]\n"
"ds_read_b64_tr_b16 a[212:213], v195 offset:57344\n"
"ds_read_b64_tr_b16 a[214:215], v195 offset:59392\n"
"v_mfma_f32_16x16x32_bf16 v[236:239], a[216:219], v[164:167], v[236:239]\n"
"ds_read_b64_tr_b16 a[216:217], v194 offset:57856\n"
"ds_read_b64_tr_b16 a[218:219], v194 offset:59904\n"
"v_mfma_f32_16x16x32_bf16 v[248:251], a[220:223], v[164:167], v[248:251]\n"
"s_waitcnt lgkmcnt(12)\n"
"ds_read_b64_tr_b16 a[220:221], v195 offset:57856\n"
"ds_read_b64_tr_b16 a[222:223], v195 offset:59904\n"
"v_mfma_f32_16x16x32_bf16 a[56:59], a[224:227], v[164:167], a[56:59]\n"
"s_waitcnt lgkmcnt(12)\n"
"v_cvt_pk_bf16_f32 v144, v221, v222\n"
"v_cvt_pk_bf16_f32 v145, v223, v224\n"
"v_mfma_f32_16x16x32_bf16 a[68:71], a[228:231], v[164:167], a[68:71]\n"
"s_waitcnt lgkmcnt(10)\n"
"s_add_u32 m0, s60, 28288\n"
"s_nop 7\n"
"v_mfma_f32_16x16x32_bf16 a[80:83], a[232:235], v[164:167], a[80:83]\n"
"s_waitcnt lgkmcnt(8)\n"
"buffer_load_dwordx4 v204, s[32:35], s63 offen offset:384 nt lds\n"
"v_mfma_f32_16x16x32_bf16 a[92:95], a[236:239], v[164:167], a[92:95]\n"
"v_exp_f32 v227, v102\n"
"v_mfma_f32_16x16x32_bf16 a[88:91], a[236:239], v[160:163], a[88:91]\n"
"v_exp_f32 v220, v103\n"
"v_mfma_f32_16x16x32_bf16 a[76:79], a[232:235], v[160:163], a[76:79]\n"
"v_exp_f32 v221, v104\n"
"v_mfma_f32_16x16x32_bf16 a[64:67], a[228:231], v[160:163], a[64:67]\n"
"v_exp_f32 v222, v105\n"
"v_mfma_f32_16x16x32_bf16 a[52:55], a[224:227], v[160:163], a[52:55]\n"
"v_cvt_pk_bf16_f32 v148, v225, v226\n"
"v_cvt_pk_bf16_f32 v149, v227, v220\n"
"v_mfma_f32_16x16x32_bf16 a[48:51], a[224:227], v[156:159], a[48:51]\n"
"ds_read_b64_tr_b16 a[224:225], v194 offset:61440\n"
"ds_read_b64_tr_b16 a[226:227], v194 offset:63488\n"
"v_mfma_f32_16x16x32_bf16 a[60:63], a[228:231], v[156:159], a[60:63]\n"
"ds_read_b64_tr_b16 a[228:229], v195 offset:61440\n"
"ds_read_b64_tr_b16 a[230:231], v195 offset:63488\n"
"v_mfma_f32_16x16x32_bf16 a[72:75], a[232:235], v[156:159], a[72:75]\n"
"ds_read_b64_tr_b16 a[232:233], v194 offset:61952\n"
"ds_read_b64_tr_b16 a[234:235], v194 offset:64000\n"
"s_nop 7\n"
"v_mfma_f32_16x16x32_bf16 a[84:87], a[236:239], v[156:159], a[84:87]\n"
"s_waitcnt lgkmcnt(12)\n"
"ds_read_b64_tr_b16 a[236:237], v195 offset:61952\n"
"ds_read_b64_tr_b16 a[238:239], v195 offset:64000\n"
"v_mfma_f32_16x16x32_bf16 a[96:99], a[208:211], v[156:159], a[96:99]\n"
"s_waitcnt lgkmcnt(12)\n"
"v_exp_f32 v223, v106\n"
"v_mfma_f32_16x16x32_bf16 a[108:111], a[212:215], v[156:159], a[108:111]\n"
"s_waitcnt lgkmcnt(10)\n"
"s_mov_b32 s63, 212992\n"
"v_mfma_f32_16x16x32_bf16 a[120:123], a[216:219], v[156:159], a[120:123]\n"
"s_waitcnt lgkmcnt(8)\n"
"s_add_u32 m0, s60, 32768\n"
"v_mfma_f32_16x16x32_bf16 a[132:135], a[220:223], v[156:159], a[132:135]\n"
"buffer_load_dwordx4 v205, s[28:31], s63 offen nt lds\n"
"v_mfma_f32_16x16x32_bf16 a[136:139], a[220:223], v[160:163], a[136:139]\n"
"v_exp_f32 v224, v107\n"
"v_mfma_f32_16x16x32_bf16 a[124:127], a[216:219], v[160:163], a[124:127]\n"
"v_exp_f32 v225, v108\n"
"v_mfma_f32_16x16x32_bf16 a[112:115], a[212:215], v[160:163], a[112:115]\n"
"s_waitcnt vmcnt(13)\n"
"s_barrier\n"
"v_mfma_f32_16x16x32_bf16 a[100:103], a[208:211], v[160:163], a[100:103]\n"
"v_exp_f32 v226, v109\n"
"s_nop 7\n"
"v_mfma_f32_16x16x32_bf16 a[104:107], a[208:211], v[164:167], a[104:107]\n"
"ds_read_b64 a[208:209], v202 offset:0\n"
"ds_read_b64 a[210:211], v203 offset:0\n"
"v_mfma_f32_16x16x32_bf16 a[116:119], a[212:215], v[164:167], a[116:119]\n"
"ds_read_b64 a[212:213], v202 offset:512\n"
"ds_read_b64 a[214:215], v203 offset:512\n"
"v_mfma_f32_16x16x32_bf16 a[128:131], a[216:219], v[164:167], a[128:131]\n"
"ds_read_b64 a[216:217], v202 offset:4096\n"
"ds_read_b64 a[218:219], v203 offset:4096\n"
"v_mfma_f32_16x16x32_bf16 a[140:143], a[220:223], v[164:167], a[140:143]\n"
"s_waitcnt lgkmcnt(12)\n"
"ds_read_b64 a[220:221], v202 offset:4608\n"
"ds_read_b64 a[222:223], v203 offset:4608\n"
"v_mfma_f32_16x16x32_bf16 a[152:155], a[224:227], v[164:167], a[152:155]\n"
"s_waitcnt lgkmcnt(12)\n"
"v_cvt_pk_bf16_f32 v152, v221, v222\n"
"v_cvt_pk_bf16_f32 v153, v223, v224\n"
"v_mfma_f32_16x16x32_bf16 a[164:167], a[228:231], v[164:167], a[164:167]\n"
"s_waitcnt lgkmcnt(10)\n"
"v_exp_f32 v227, v110\n"
"v_mfma_f32_16x16x32_bf16 a[176:179], a[232:235], v[164:167], a[176:179]\n"
"s_waitcnt lgkmcnt(8)\n"
"s_add_u32 m0, s60, 36736\n"
"v_mfma_f32_16x16x32_bf16 a[188:191], a[236:239], v[164:167], a[188:191]\n"
"buffer_load_dwordx4 v205, s[28:31], s63 offen offset:128 nt lds\n"
"v_mfma_f32_16x16x32_bf16 a[184:187], a[236:239], v[160:163], a[184:187]\n"
"v_exp_f32 v220, v111\n"
"s_nop 7\n"
"v_mfma_f32_16x16x32_bf16 a[172:175], a[232:235], v[160:163], a[172:175]\n"
"v_exp_f32 v221, v112\n"
"v_mfma_f32_16x16x32_bf16 a[160:163], a[228:231], v[160:163], a[160:163]\n"
"v_exp_f32 v222, v113\n"
"v_mfma_f32_16x16x32_bf16 a[148:151], a[224:227], v[160:163], a[148:151]\n"
"v_cvt_pk_bf16_f32 v146, v225, v226\n"
"v_cvt_pk_bf16_f32 v147, v227, v220\n"
"v_mfma_f32_16x16x32_bf16 a[144:147], a[224:227], v[156:159], a[144:147]\n"
"ds_read_b64 a[224:225], v202 offset:8192\n"
"ds_read_b64 a[226:227], v203 offset:8192\n"
"v_mfma_f32_16x16x32_bf16 a[156:159], a[228:231], v[156:159], a[156:159]\n"
"ds_read_b64 a[228:229], v202 offset:8704\n"
"ds_read_b64 a[230:231], v203 offset:8704\n"
"v_mfma_f32_16x16x32_bf16 a[168:171], a[232:235], v[156:159], a[168:171]\n"
"ds_read_b64 a[232:233], v202 offset:12288\n"
"ds_read_b64 a[234:235], v203 offset:12288\n"
"v_mfma_f32_16x16x32_bf16 a[180:183], a[236:239], v[156:159], a[180:183]\n"
"s_waitcnt lgkmcnt(12)\n"
"ds_read_b64 a[236:237], v202 offset:12800\n"
"ds_read_b64 a[238:239], v203 offset:12800\n"
"v_mfma_f32_16x16x32_bf16 v[120:123], a[208:211], v[0:3], v[208:211]\n"
"s_waitcnt lgkmcnt(12)\n"
"v_exp_f32 v223, v114\n"
"v_mfma_f32_16x16x32_bf16 v[120:123], a[212:215], v[12:15], v[120:123]\n"
"s_waitcnt lgkmcnt(10)\n"
"v_exp_f32 v224, v115\n"
"s_nop 7\n"
"v_mfma_f32_16x16x32_bf16 v[120:123], a[216:219], v[24:27], v[120:123]\n"
"s_waitcnt lgkmcnt(8)\n"
"v_exp_f32 v225, v116\n"
"v_mfma_f32_16x16x32_bf16 v[120:123], a[220:223], v[36:39], v[120:123]\n"
"s_add_u32 m0, s60, 40704\n"
"v_exp_f32 v226, v117\n"
"v_mfma_f32_16x16x32_bf16 v[124:127], a[220:223], v[40:43], v[212:215]\n"
"buffer_load_dwordx4 v205, s[28:31], s63 offen offset:256 nt lds\n"
"v_mfma_f32_16x16x32_bf16 v[124:127], a[216:219], v[28:31], v[124:127]\n"
"v_cvt_pk_bf16_f32 v150, v221, v222\n"
"v_cvt_pk_bf16_f32 v151, v223, v224\n"
"v_mfma_f32_16x16x32_bf16 v[124:127], a[212:215], v[16:19], v[124:127]\n"
"v_exp_f32 v227, v118\n"
"v_mfma_f32_16x16x32_bf16 v[124:127], a[208:211], v[4:7], v[124:127]\n"
"v_exp_f32 v220, v119\n"
"v_mfma_f32_16x16x32_bf16 v[128:131], a[208:211], v[8:11], v[216:219]\n"
"ds_read_b64 a[208:209], v202 offset:2048\n"
"ds_read_b64 a[210:211], v203 offset:2048\n"
"v_mfma_f32_16x16x32_bf16 v[128:131], a[212:215], v[20:23], v[128:131]\n"
"ds_read_b64 a[212:213], v202 offset:2560\n"
"ds_read_b64 a[214:215], v203 offset:2560\n"
"v_mfma_f32_16x16x32_bf16 v[128:131], a[216:219], v[32:35], v[128:131]\n"
"ds_read_b64 a[216:217], v202 offset:6144\n"
"ds_read_b64 a[218:219], v203 offset:6144\n"
"s_nop 7\n"
"v_mfma_f32_16x16x32_bf16 v[128:131], a[220:223], v[44:47], v[128:131]\n"
"s_waitcnt lgkmcnt(12)\n"
"ds_read_b64 a[220:221], v202 offset:6656\n"
"ds_read_b64 a[222:223], v203 offset:6656\n"
"v_mfma_f32_16x16x32_bf16 v[128:131], a[224:227], v[56:59], v[128:131]\n"
"s_waitcnt lgkmcnt(12)\n"
"v_cvt_pk_bf16_f32 v154, v225, v226\n"
"v_cvt_pk_bf16_f32 v155, v227, v220\n"
"v_mfma_f32_16x16x32_bf16 v[128:131], a[228:231], v[68:71], v[128:131]\n"
"s_waitcnt lgkmcnt(10)\n"
"v_mfma_f32_16x16x32_bf16 v[128:131], a[232:235], v[80:83], v[128:131]\n"
"s_waitcnt lgkmcnt(8)\n"
"v_mfma_f32_16x16x32_bf16 v[128:131], a[236:239], v[92:95], v[128:131]\n"
"v_mfma_f32_16x16x32_bf16 v[124:127], a[236:239], v[88:91], v[124:127]\n"
"s_add_u32 m0, s60, 44672\n"
"v_mfma_f32_16x16x32_bf16 v[124:127], a[232:235], v[76:79], v[124:127]\n"
"buffer_load_dwordx4 v205, s[28:31], s63 offen offset:384 nt lds\n"
"v_mfma_f32_16x16x32_bf16 v[124:127], a[228:231], v[64:67], v[124:127]\n"
"v_mfma_f32_16x16x32_bf16 v[124:127], a[224:227], v[52:55], v[124:127]\n"
"s_nop 7\n"
"v_mfma_f32_16x16x32_bf16 v[120:123], a[224:227], v[48:51], v[120:123]\n"
"ds_read_b64 a[224:225], v202 offset:10240\n"
"ds_read_b64 a[226:227], v203 offset:10240\n"
"v_mfma_f32_16x16x32_bf16 v[120:123], a[228:231], v[60:63], v[120:123]\n"
"ds_read_b64 a[228:229], v202 offset:10752\n"
"ds_read_b64 a[230:231], v203 offset:10752\n"
"v_mfma_f32_16x16x32_bf16 v[120:123], a[232:235], v[72:75], v[120:123]\n"
"ds_read_b64 a[232:233], v202 offset:14336\n"
"ds_read_b64 a[234:235], v203 offset:14336\n"
"v_mfma_f32_16x16x32_bf16 v[120:123], a[236:239], v[84:87], v[120:123]\n"
"s_waitcnt lgkmcnt(12)\n"
"ds_read_b64 a[236:237], v202 offset:14848\n"
"ds_read_b64 a[238:239], v203 offset:14848\n"
"v_mfma_f32_16x16x32_bf16 v[132:135], a[208:211], v[0:3], v[208:211]\n"
"s_waitcnt lgkmcnt(12)\n"
"v_mfma_f32_16x16x32_bf16 v[132:135], a[212:215], v[12:15], v[132:135]\n"
"s_waitcnt lgkmcnt(10)\n"
"v_mfma_f32_16x16x32_bf16 v[132:135], a[216:219], v[24:27], v[132:135]\n"
"s_waitcnt lgkmcnt(8)\n"
"v_mfma_f32_16x16x32_bf16 v[132:135], a[220:223], v[36:39], v[132:135]\n"
"v_mfma_f32_16x16x32_bf16 v[136:139], a[220:223], v[40:43], v[212:215]\n"
"s_mov_b32 s63, 196608\n"
"s_nop 7\n"
"v_mfma_f32_16x16x32_bf16 v[136:139], a[216:219], v[28:31], v[136:139]\n"
"s_add_u32 m0, s60, 49152\n"
"v_mfma_f32_16x16x32_bf16 v[136:139], a[212:215], v[16:19], v[136:139]\n"
"s_waitcnt vmcnt(12)\n"
"buffer_load_dwordx4 v204, s[32:35], s63 offen nt lds\n"
"v_mfma_f32_16x16x32_bf16 v[136:139], a[208:211], v[4:7], v[136:139]\n"
"s_barrier\n"
"v_mfma_f32_16x16x32_bf16 v[140:143], a[208:211], v[8:11], v[216:219]\n"
"ds_read_b64_tr_b16 a[208:209], v196 offset:16384\n"
"ds_read_b64_tr_b16 a[210:211], v196 offset:18432\n"
"v_mfma_f32_16x16x32_bf16 v[140:143], a[212:215], v[20:23], v[140:143]\n"
"ds_read_b64_tr_b16 a[212:213], v197 offset:16384\n"
"ds_read_b64_tr_b16 a[214:215], v197 offset:18432\n"
"v_mfma_f32_16x16x32_bf16 v[140:143], a[216:219], v[32:35], v[140:143]\n"
"ds_read_b64_tr_b16 a[216:217], v196 offset:16896\n"
"ds_read_b64_tr_b16 a[218:219], v196 offset:18944\n"
"v_mfma_f32_16x16x32_bf16 v[140:143], a[220:223], v[44:47], v[140:143]\n"
"ds_read_b64_tr_b16 a[220:221], v197 offset:16896\n"
"s_waitcnt lgkmcnt(14)\n"
"ds_read_b64_tr_b16 a[222:223], v197 offset:18944\n"
"v_mfma_f32_16x16x32_bf16 a[0:3], a[204:207], v[144:147], a[192:195]\n"
"v_mfma_f32_16x16x32_bf16 a[4:7], a[204:207], v[148:151], a[196:199]\n"
"s_nop 7\n"
"v_mfma_f32_16x16x32_bf16 a[8:11], a[204:207], v[152:155], a[200:203]\n"
"s_waitcnt lgkmcnt(14)\n"
"v_mfma_f32_16x16x32_bf16 v[140:143], a[224:227], v[56:59], v[140:143]\n"
"s_waitcnt lgkmcnt(12)\n"
"v_mfma_f32_16x16x32_bf16 v[140:143], a[228:231], v[68:71], v[140:143]\n"
"s_waitcnt lgkmcnt(10)\n"
"v_mfma_f32_16x16x32_bf16 v[140:143], a[232:235], v[80:83], v[140:143]\n"
"s_waitcnt lgkmcnt(8)\n"
"s_add_u32 m0, s60, 53120\n"
"v_accvgpr_read_b32 v221, a0\n"
"v_mfma_f32_16x16x32_bf16 v[140:143], a[236:239], v[92:95], v[140:143]\n"
"buffer_load_dwordx4 v204, s[32:35], s63 offen offset:128 nt lds\n"
"v_mfma_f32_16x16x32_bf16 v[136:139], a[236:239], v[88:91], v[136:139]\n"
"v_accvgpr_read_b32 v222, a4\n"
"v_accvgpr_read_b32 v223, a8\n"
"v_mfma_f32_16x16x32_bf16 v[136:139], a[232:235], v[76:79], v[136:139]\n"
"v_add_f32 v221, v221, v222\n"
"v_add_f32 v221, v221, v223\n"
"v_mfma_f32_16x16x32_bf16 v[136:139], a[228:231], v[64:67], v[136:139]\n"
"v_cmp_ngt_f32 vcc, 0x47800000, v221\n"
"v_mfma_f32_16x16x32_bf16 v[136:139], a[224:227], v[52:55], v[136:139]\n"
"s_nop 7\n"
"v_mfma_f32_16x16x32_bf16 v[132:135], a[224:227], v[48:51], v[132:135]\n"
"ds_read_b64_tr_b16 a[224:225], v196 offset:20480\n"
"ds_read_b64_tr_b16 a[226:227], v196 offset:22528\n"
"v_mfma_f32_16x16x32_bf16 v[132:135], a[228:231], v[60:63], v[132:135]\n"
"ds_read_b64_tr_b16 a[228:229], v197 offset:20480\n"
"ds_read_b64_tr_b16 a[230:231], v197 offset:22528\n"
"v_mfma_f32_16x16x32_bf16 v[132:135], a[232:235], v[72:75], v[132:135]\n"
"ds_read_b64_tr_b16 a[232:233], v196 offset:20992\n"
"ds_read_b64_tr_b16 a[234:235], v196 offset:23040\n"
"v_mfma_f32_16x16x32_bf16 v[132:135], a[236:239], v[84:87], v[132:135]\n"
"s_waitcnt lgkmcnt(12)\n"
"s_cbranch_vccnz Lstub10\n"
"Lback10:\n"
"ds_read_b64_tr_b16 a[236:237], v197 offset:20992\n"
"v_mfma_f32_16x16x32_bf16 v[168:171], a[208:211], v[144:147], v[168:171]\n"
"s_waitcnt lgkmcnt(11)\n"
"ds_read_b64_tr_b16 a[238:239], v197 offset:23040\n"
"v_mfma_f32_16x16x32_bf16 v[180:183], a[212:215], v[144:147], v[180:183]\n"
"s_waitcnt lgkmcnt(10)\n"
"v_exp_f32 v224, v120\n"
"v_mfma_f32_16x16x32_bf16 v[228:231], a[216:219], v[144:147], v[228:231]\n"
"s_waitcnt lgkmcnt(8)\n"
"v_exp_f32 v225, v121\n"
"v_mfma_f32_16x16x32_bf16 v[240:243], a[220:223], v[144:147], v[240:243]\n"
"s_add_u32 m0, s60, 57088\n"
"v_exp_f32 v226, v122\n"
"v_mfma_f32_16x16x32_bf16 v[244:247], a[220:223], v[148:151], v[244:247]\n"
"buffer_load_dwordx4 v204, s[32:35], s63 offen offset:256 nt lds\n"
"s_nop 7\n"
"v_mfma_f32_16x16x32_bf16 v[232:235], a[216:219], v[148:151], v[232:235]\n"
"v_exp_f32 v227, v123\n"
"v_mfma_f32_16x16x32_bf16 v[184:187], a[212:215], v[148:151], v[184:187]\n"
"v_exp_f32 v220, v124\n"
"v_mfma_f32_16x16x32_bf16 v[172:175], a[208:211], v[148:151], v[172:175]\n"
"v_exp_f32 v221, v125\n"
"v_mfma_f32_16x16x32_bf16 v[176:179], a[208:211], v[152:155], v[176:179]\n"
"ds_read_b64_tr_b16 a[208:209], v196 offset:24576\n"
"ds_read_b64_tr_b16 a[210:211], v196 offset:26624\n"
"v_mfma_f32_16x16x32_bf16 v[188:191], a[212:215], v[152:155], v[188:191]\n"
"ds_read_b64_tr_b16 a[212:213], v197 offset:24576\n"
"ds_read_b64_tr_b16 a[214:215], v197 offset:26624\n"
"v_mfma_f32_16x16x32_bf16 v[236:239], a[216:219], v[152:155], v[236:239]\n"
"ds_read_b64_tr_b16 a[216:217], v196 offset:25088\n"
"ds_read_b64_tr_b16 a[218:219], v196 offset:27136\n"
"v_mfma_f32_16x16x32_bf16 v[248:251], a[220:223], v[152:155], v[248:251]\n"
"s_waitcnt lgkmcnt(12)\n"
"ds_read_b64_tr_b16 a[220:221], v197 offset:25088\n"
"ds_read_b64_tr_b16 a[222:223], v197 offset:27136\n"
"v_mfma_f32_16x16x32_bf16 a[56:59], a[224:227], v[152:155], a[56:59]\n"
"s_waitcnt lgkmcnt(12)\n"
"v_cvt_pk_bf16_f32 v156, v224, v225\n"
"v_cvt_pk_bf16_f32 v157, v226, v227\n"
"v_mfma_f32_16x16x32_bf16 a[68:71], a[228:231], v[152:155], a[68:71]\n"
"s_waitcnt lgkmcnt(10)\n"
"v_exp_f32 v222, v126\n"
"s_nop 7\n"
"v_mfma_f32_16x16x32_bf16 a[80:83], a[232:235], v[152:155], a[80:83]\n"
"s_waitcnt lgkmcnt(8)\n"
"v_exp_f32 v223, v127\n"
"v_mfma_f32_16x16x32_bf16 a[92:95], a[236:239], v[152:155], a[92:95]\n"
"v_exp_f32 v224, v128\n"
"v_mfma_f32_16x16x32_bf16 a[88:91], a[236:239], v[148:151], a[88:91]\n"
"s_add_u32 m0, s60, 61056\n"
"v_exp_f32 v225, v129\n"
"v_mfma_f32_16x16x32_bf16 a[76:79], a[232:235], v[148:151], a[76:79]\n"
"buffer_load_dwordx4 v204, s[32:35], s63 offen offset:384 nt lds\n"
"v_mfma_f32_16x16x32_bf16 a[64:67], a[228:231], v[148:151], a[64:67]\n"
"v_cvt_pk_bf16_f32 v160, v220, v221\n"
"v_cvt_pk_bf16_f32 v161, v222, v223\n"
"v_mfma_f32_16x16x32_bf16 a[52:55], a[224:227], v[148:151], a[52:55]\n"
"v_exp_f32 v226, v130\n"
"v_mfma_f32_16x16x32_bf16 a[48:51], a[224:227], v[144:147], a[48:51]\n"
"ds_read_b64_tr_b16 a[224:225], v196 offset:28672\n"
"ds_read_b64_tr_b16 a[226:227], v196 offset:30720\n"
"v_mfma_f32_16x16x32_bf16 a[60:63], a[228:231], v[144:147], a[60:63]\n"
"ds_read_b64_tr_b16 a[228:229], v197 offset:28672\n"
"ds_read_b64_tr_b16 a[230:231], v197 offset:30720\n"
"v_mfma_f32_16x16x32_bf16 a[72:75], a[232:235], v[144:147], a[72:75]\n"
"ds_read_b64_tr_b16 a[232:233], v196 offset:29184\n"
"ds_read_b64_tr_b16 a[234:235], v196 offset:31232\n"
"s_nop 7\n"
"v_mfma_f32_16x16x32_bf16 a[84:87], a[236:239], v[144:147], a[84:87]\n"
"s_waitcnt lgkmcnt(12)\n"
"ds_read_b64_tr_b16 a[236:237], v197 offset:29184\n"
"ds_read_b64_tr_b16 a[238:239], v197 offset:31232\n"
"v_mfma_f32_16x16x32_bf16 a[96:99], a[208:211], v[144:147], a[96:99]\n"
"s_waitcnt lgkmcnt(12)\n"
"v_exp_f32 v227, v131\n"
"v_mfma_f32_16x16x32_bf16 a[108:111], a[212:215], v[144:147], a[108:111]\n"
"s_waitcnt lgkmcnt(10)\n"
"v_exp_f32 v220, v132\n"
"v_mfma_f32_16x16x32_bf16 a[120:123], a[216:219], v[144:147], a[120:123]\n"
"s_waitcnt lgkmcnt(8)\n"
"v_exp_f32 v221, v133\n"
"v_mfma_f32_16x16x32_bf16 a[132:135], a[220:223], v[144:147], a[132:135]\n"
"v_cvt_pk_bf16_f32 v164, v224, v225\n"
"v_cvt_pk_bf16_f32 v165, v226, v227\n"
"v_mfma_f32_16x16x32_bf16 a[136:139], a[220:223], v[148:151], a[136:139]\n"
"s_mov_b32 s63, 229376\n"
"v_exp_f32 v222, v134\n"
"v_mfma_f32_16x16x32_bf16 a[124:127], a[216:219], v[148:151], a[124:127]\n"
"s_add_u32 m0, s60, 65536\n"
"v_exp_f32 v223, v135\n"
"v_mfma_f32_16x16x32_bf16 a[112:115], a[212:215], v[148:151], a[112:115]\n"
"s_waitcnt vmcnt(12)\n"
"buffer_load_dwordx4 v205, s[28:31], s63 offen nt lds\n"
"v_mfma_f32_16x16x32_bf16 a[100:103], a[208:211], v[148:151], a[100:103]\n"
"s_barrier\n"
"v_exp_f32 v224, v136\n"
"s_nop 7\n"
"v_mfma_f32_16x16x32_bf16 a[104:107], a[208:211], v[152:155], a[104:107]\n"
"ds_read_b64 a[208:209], v198 offset:0\n"
"ds_read_b64 a[210:211], v199 offset:0\n"
"v_mfma_f32_16x16x32_bf16 a[116:119], a[212:215], v[152:155], a[116:119]\n"
"ds_read_b64 a[212:213], v198 offset:512\n"
"ds_read_b64 a[214:215], v199 offset:512\n"
"v_mfma_f32_16x16x32_bf16 a[128:131], a[216:219], v[152:155], a[128:131]\n"
"ds_read_b64 a[216:217], v198 offset:4096\n"
"ds_read_b64 a[218:219], v199 offset:4096\n"
"v_mfma_f32_16x16x32_bf16 a[140:143], a[220:223], v[152:155], a[140:143]\n"
"s_waitcnt lgkmcnt(12)\n"
"ds_read_b64 a[220:221], v198 offset:4608\n"
"ds_read_b64 a[222:223], v199 offset:4608\n"
"v_mfma_f32_16x16x32_bf16 a[152:155], a[224:227], v[152:155], a[152:155]\n"
"s_waitcnt lgkmcnt(12)\n"
"v_exp_f32 v225, v137\n"
"v_mfma_f32_16x16x32_bf16 a[164:167], a[228:231], v[152:155], a[164:167]\n"
"s_waitcnt lgkmcnt(10)\n"
"v_cvt_pk_bf16_f32 v158, v220, v221\n"
"v_cvt_pk_bf16_f32 v159, v222, v223\n"
"v_mfma_f32_16x16x32_bf16 a[176:179], a[232:235], v[152:155], a[176:179]\n"
"s_waitcnt lgkmcnt(8)\n"
"v_exp_f32 v226, v138\n"
"v_mfma_f32_16x16x32_bf16 a[188:191], a[236:239], v[152:155], a[188:191]\n"
"v_exp_f32 v227, v139\n"
"v_mfma_f32_16x16x32_bf16 a[184:187], a[236:239], v[148:151], a[184:187]\n"
"v_exp_f32 v220, v140\n"
"s_nop 7\n"
"v_mfma_f32_16x16x32_bf16 a[172:175], a[232:235], v[148:151], a[172:175]\n"
"s_add_u32 m0, s60, 69504\n"
"v_exp_f32 v221, v141\n"
"v_mfma_f32_16x16x32_bf16 a[160:163], a[228:231], v[148:151], a[160:163]\n"
"buffer_load_dwordx4 v205, s[28:31], s63 offen offset:128 nt lds\n"
"v_mfma_f32_16x16x32_bf16 a[148:151], a[224:227], v[148:151], a[148:151]\n"
"v_cvt_pk_bf16_f32 v162, v224, v225\n"
"v_cvt_pk_bf16_f32 v163, v226, v227\n"
"v_mfma_f32_16x16x32_bf16 a[144:147], a[224:227], v[144:147], a[144:147]\n"
"ds_read_b64 a[224:225], v198 offset:8192\n"
"ds_read_b64 a[226:227], v199 offset:8192\n"
"v_mfma_f32_16x16x32_bf16 a[156:159], a[228:231], v[144:147], a[156:159]\n"
"ds_read_b64 a[228:229], v198 offset:8704\n"
"ds_read_b64 a[230:231], v199 offset:8704\n"
"v_mfma_f32_16x16x32_bf16 a[168:171], a[232:235], v[144:147], a[168:171]\n"
"ds_read_b64 a[232:233], v198 offset:12288\n"
"ds_read_b64 a[234:235], v199 offset:12288\n"
"v_mfma_f32_16x16x32_bf16 a[180:183], a[236:239], v[144:147], a[180:183]\n"
"s_waitcnt lgkmcnt(12)\n"
"ds_read_b64 a[236:237], v198 offset:12800\n"
"ds_read_b64 a[238:239], v199 offset:12800\n"
"v_mfma_f32_16x16x32_bf16 v[96:99], a[208:211], v[0:3], v[208:211]\n"
"s_waitcnt lgkmcnt(12)\n"
"v_exp_f32 v222, v142\n"
"v_mfma_f32_16x16x32_bf16 v[96:99], a[212:215], v[12:15], v[96:99]\n"
"s_waitcnt lgkmcnt(10)\n"
"v_exp_f32 v223, v143\n"
"s_nop 7\n"
"v_mfma_f32_16x16x32_bf16 v[96:99], a[216:219], v[24:27], v[96:99]\n"
"s_waitcnt lgkmcnt(8)\n"
"v_cvt_pk_bf16_f32 v166, v220, v221\n"
"v_cvt_pk_bf16_f32 v167, v222, v223\n"
"v_mfma_f32_16x16x32_bf16 v[96:99], a[220:223], v[36:39], v[96:99]\n"
"v_mfma_f32_16x16x32_bf16 v[100:103], a[220:223], v[40:43], v[212:215]\n"
"v_mfma_f32_16x16x32_bf16 v[100:103], a[216:219], v[28:31], v[100:103]\n"
"v_mfma_f32_16x16x32_bf16 v[100:103], a[212:215], v[16:19], v[100:103]\n"
"s_add_u32 m0, s60, 73472\n"
"v_mfma_f32_16x16x32_bf16 v[100:103], a[208:211], v[4:7], v[100:103]\n"
"buffer_load_dwordx4 v205, s[28:31], s63 offen offset:256 nt lds\n"
"v_mfma_f32_16x16x32_bf16 v[104:107], a[208:211], v[8:11], v[216:219]\n"
"ds_read_b64 a[208:209], v198 offset:2048\n"
"ds_read_b64 a[210:211], v199 offset:2048\n"
"v_mfma_f32_16x16x32_bf16 v[104:107], a[212:215], v[20:23], v[104:107]\n"
"ds_read_b64 a[212:213], v198 offset:2560\n"
"ds_read_b64 a[214:215], v199 offset:2560\n"
"v_mfma_f32_16x16x32_bf16 v[104:107], a[216:219], v[32:35], v[104:107]\n"
"ds_read_b64 a[216:217], v198 offset:6144\n"
"ds_read_b64 a[218:219], v199 offset:6144\n"
"s_nop 7\n"
"v_mfma_f32_16x16x32_bf16 v[104:107], a[220:223], v[44:47], v[104:107]\n"
"s_waitcnt lgkmcnt(12)\n"
"ds_read_b64 a[220:221], v198 offset:6656\n"
"ds_read_b64 a[222:223], v199 offset:6656\n"
"v_mfma_f32_16x16x32_bf16 v[104:107], a[224:227], v[56:59], v[104:107]\n"
"s_waitcnt lgkmcnt(12)\n"
"v_mfma_f32_16x16x32_bf16 v[104:107], a[228:231], v[68:71], v[104:107]\n"
"s_waitcnt lgkmcnt(10)\n"
"v_mfma_f32_16x16x32_bf16 v[104:107], a[232:235], v[80:83], v[104:107]\n"
"s_waitcnt lgkmcnt(8)\n"
"v_mfma_f32_16x16x32_bf16 v[104:107], a[236:239], v[92:95], v[104:107]\n"
"v_mfma_f32_16x16x32_bf16 v[100:103], a[236:239], v[88:91], v[100:103]\n"
"v_mfma_f32_16x16x32_bf16 v[100:103], a[232:235], v[76:79], v[100:103]\n"
"v_mfma_f32_16x16x32_bf16 v[100:103], a[228:231], v[64:67], v[100:103]\n"
"v_mfma_f32_16x16x32_bf16 v[100:103], a[224:227], v[52:55], v[100:103]\n"
"s_add_u32 m0, s60, 77440\n"
"s_nop 7\n"
"v_mfma_f32_16x16x32_bf16 v[96:99], a[224:227], v[48:51], v[96:99]\n"
"buffer_load_dwordx4 v205, s[28:31], s63 offen offset:384 nt lds\n"
"v_mfma_f32_16x16x32_bf16 v[96:99], a[228:231], v[60:63], v[96:99]\n"
"ds_read_b64 a[224:225], v198 offset:10240\n"
"ds_read_b64 a[226:227], v199 offset:10240\n"
"v_mfma_f32_16x16x32_bf16 v[96:99], a[232:235], v[72:75], v[96:99]\n"
"ds_read_b64 a[228:229], v198 offset:10752\n"
"ds_read_b64 a[230:231], v199 offset:10752\n"
"v_mfma_f32_16x16x32_bf16 v[96:99], a[236:239], v[84:87], v[96:99]\n"
"s_waitcnt lgkmcnt(10)\n"
"ds_read_b64 a[232:233], v198 offset:14336\n"
"ds_read_b64 a[234:235], v199 offset:14336\n"
"v_mfma_f32_16x16x32_bf16 v[108:111], a[208:211], v[0:3], v[208:211]\n"
"s_waitcnt lgkmcnt(10)\n"
"ds_read_b64 a[236:237], v198 offset:14848\n"
"ds_read_b64 a[238:239], v199 offset:14848\n"
"v_mfma_f32_16x16x32_bf16 v[108:111], a[212:215], v[12:15], v[108:111]\n"
"s_waitcnt lgkmcnt(10)\n"
"v_mfma_f32_16x16x32_bf16 v[108:111], a[216:219], v[24:27], v[108:111]\n"
"s_waitcnt lgkmcnt(8)\n"
"v_mfma_f32_16x16x32_bf16 v[108:111], a[220:223], v[36:39], v[108:111]\n"
"v_mfma_f32_16x16x32_bf16 v[112:115], a[220:223], v[40:43], v[212:215]\n"
"s_nop 7\n"
"v_mfma_f32_16x16x32_bf16 v[112:115], a[216:219], v[28:31], v[112:115]\n"
"v_mfma_f32_16x16x32_bf16 v[112:115], a[212:215], v[16:19], v[112:115]\n"
"s_waitcnt vmcnt(12)\n"
"s_barrier\n"
"v_mfma_f32_16x16x32_bf16 v[112:115], a[208:211], v[4:7], v[112:115]\n"
"s_mov_b32 s63, 212992\n"
"v_mfma_f32_16x16x32_bf16 v[116:119], a[208:211], v[8:11], v[216:219]\n"
"s_add_u32 m0, s60, 81920\n"
"ds_read_b64_tr_b16 a[208:209], v192 offset:16384\n"
"ds_read_b64_tr_b16 a[210:211], v192 offset:18432\n"
"v_mfma_f32_16x16x32_bf16 v[116:119], a[212:215], v[20:23], v[116:119]\n"
"buffer_load_dwordx4 v204, s[32:35], s63 offen nt lds\n"
"v_mfma_f32_16x16x32_bf16 v[116:119], a[216:219], v[32:35], v[116:119]\n"
"ds_read_b64_tr_b16 a[212:213], v193 offset:16384\n"
"ds_read_b64_tr_b16 a[214:215], v193 offset:18432\n"
"v_mfma_f32_16x16x32_bf16 v[116:119], a[220:223], v[44:47], v[116:119]\n"
"ds_read_b64_tr_b16 a[216:217], v192 offset:16896\n"
"ds_read_b64_tr_b16 a[218:219], v192 offset:18944\n"
"v_mfma_f32_16x16x32_bf16 a[192:195], a[204:207], v[156:159], a[0:3]\n"
"ds_read_b64_tr_b16 a[220:221], v193 offset:16896\n"
"s_waitcnt lgkmcnt(14)\n"
"ds_read_b64_tr_b16 a[222:223], v193 offset:18944\n"
"v_mfma_f32_16x16x32_bf16 a[196:199], a[204:207], v[160:163], a[4:7]\n"
"s_nop 7\n"
"v_mfma_f32_16x16x32_bf16 a[200:203], a[204:207], v[164:167], a[8:11]\n"
"s_waitcnt lgkmcnt(14)\n"
"v_mfma_f32_16x16x32_bf16 v[116:119], a[224:227], v[56:59], v[116:119]\n"
"s_waitcnt lgkmcnt(12)\n"
"v_mfma_f32_16x16x32_bf16 v[116:119], a[228:231], v[68:71], v[116:119]\n"
"s_waitcnt lgkmcnt(10)\n"
"v_mfma_f32_16x16x32_bf16 v[116:119], a[232:235], v[80:83], v[116:119]\n"
"s_waitcnt lgkmcnt(8)\n"
"v_accvgpr_read_b32 v224, a192\n"
"v_accvgpr_read_b32 v225, a196\n"
"v_mfma_f32_16x16x32_bf16 v[116:119], a[236:239], v[92:95], v[116:119]\n"
"v_accvgpr_read_b32 v226, a200\n"
"v_add_f32 v224, v224, v225\n"
"v_mfma_f32_16x16x32_bf16 v[112:115], a[236:239], v[88:91], v[112:115]\n"
"v_add_f32 v224, v224, v226\n"
"v_cmp_ngt_f32 vcc, 0x47800000, v224\n"
"v_mfma_f32_16x16x32_bf16 v[112:115], a[232:235], v[76:79], v[112:115]\n"
"s_add_u32 m0, s60, 85888\n"
"v_mfma_f32_16x16x32_bf16 v[112:115], a[228:231], v[64:67], v[112:115]\n"
"buffer_load_dwordx4 v204, s[32:35], s63 offen offset:128 nt lds\n"
"v_mfma_f32_16x16x32_bf16 v[112:115], a[224:227], v[52:55], v[112:115]\n"
"s_nop 7\n"
"v_mfma_f32_16x16x32_bf16 v[108:111], a[224:227], v[48:51], v[108:111]\n"
"ds_read_b64_tr_b16 a[224:225], v192 offset:20480\n"
"ds_read_b64_tr_b16 a[226:227], v192 offset:22528\n"
"v_mfma_f32_16x16x32_bf16 v[108:111], a[228:231], v[60:63], v[108:111]\n"
"ds_read_b64_tr_b16 a[228:229], v193 offset:20480\n"
"ds_read_b64_tr_b16 a[230:231], v193 offset:22528\n"
"v_mfma_f32_16x16x32_bf16 v[108:111], a[232:235], v[72:75], v[108:111]\n"
"ds_read_b64_tr_b16 a[232:233], v192 offset:20992\n"
"ds_read_b64_tr_b16 a[234:235], v192 offset:23040\n"
"v_mfma_f32_16x16x32_bf16 v[108:111], a[236:239], v[84:87], v[108:111]\n"
"s_waitcnt lgkmcnt(12)\n"
"s_cbranch_vccnz Lstub11\n"
"Lback11:\n"
"ds_read_b64_tr_b16 a[236:237], v193 offset:20992\n"
"v_mfma_f32_16x16x32_bf16 v[168:171], a[208:211], v[156:159], v[168:171]\n"
"s_waitcnt lgkmcnt(11)\n"
"ds_read_b64_tr_b16 a[238:239], v193 offset:23040\n"
"v_mfma_f32_16x16x32_bf16 v[180:183], a[212:215], v[156:159], v[180:183]\n"
"s_waitcnt lgkmcnt(10)\n"
"v_exp_f32 v227, v96\n"
"v_mfma_f32_16x16x32_bf16 v[228:231], a[216:219], v[156:159], v[228:231]\n"
"s_waitcnt lgkmcnt(8)\n"
"v_exp_f32 v220, v97\n"
"v_mfma_f32_16x16x32_bf16 v[240:243], a[220:223], v[156:159], v[240:243]\n"
"v_exp_f32 v221, v98\n"
"v_mfma_f32_16x16x32_bf16 v[244:247], a[220:223], v[160:163], v[244:247]\n"
"v_exp_f32 v222, v99\n"
"s_nop 7\n"
"v_mfma_f32_16x16x32_bf16 v[232:235], a[216:219], v[160:163], v[232:235]\n"
"v_exp_f32 v223, v100\n"
"v_mfma_f32_16x16x32_bf16 v[184:187], a[212:215], v[160:163], v[184:187]\n"
"s_add_u32 m0, s60, 89856\n"
"v_exp_f32 v224, v101\n"
"v_mfma_f32_16x16x32_bf16 v[172:175], a[208:211], v[160:163], v[172:175]\n"
"buffer_load_dwordx4 v204, s[32:35], s63 offen offset:256 nt lds\n"
"v_mfma_f32_16x16x32_bf16 v[176:179], a[208:211], v[164:167], v[176:179]\n"
"ds_read_b64_tr_b16 a[208:209], v192 offset:24576\n"
"ds_read_b64_tr_b16 a[210:211], v192 offset:26624\n"
"v_mfma_f32_16x16x32_bf16 v[188:191], a[212:215], v[164:167], v[188:191]\n"
"ds_read_b64_tr_b16 a[212:213], v193 offset:24576\n"
"ds_read_b64_tr_b16 a[214:215], v193 offset:26624\n"
"v_mfma_f32_16x16x32_bf16 v[236:239], a[216:219], v[164:167], v[236:239]\n"
"ds_read_b64_tr_b16 a[216:217], v192 offset:25088\n"
"ds_read_b64_tr_b16 a[218:219], v192 offset:27136\n"
"v_mfma_f32_16x16x32_bf16 v[248:251], a[220:223], v[164:167], v[248:251]\n"
"s_waitcnt lgkmcnt(12)\n"
"ds_read_b64_tr_b16 a[220:221], v193 offset:25088\n"
"ds_read_b64_tr_b16 a[222:223], v193 offset:27136\n"
"v_mfma_f32_16x16x32_bf16 a[56:59], a[224:227], v[164:167], a[56:59]\n"
"s_waitcnt lgkmcnt(12)\n"
"v_cvt_pk_bf16_f32 v144, v227, v220\n"
"v_cvt_pk_bf16_f32 v145, v221, v222\n"
"v_mfma_f32_16x16x32_bf16 a[68:71], a[228:231], v[164:167], a[68:71]\n"
"s_waitcnt lgkmcnt(10)\n"
"v_exp_f32 v225, v102\n"
"s_nop 7\n"
"v_mfma_f32_16x16x32_bf16 a[80:83], a[232:235], v[164:167], a[80:83]\n"
"s_waitcnt lgkmcnt(8)\n"
"v_exp_f32 v226, v103\n"
"v_mfma_f32_16x16x32_bf16 a[92:95], a[236:239], v[164:167], a[92:95]\n"
"v_exp_f32 v227, v104\n"
"v_mfma_f32_16x16x32_bf16 a[88:91], a[236:239], v[160:163], a[88:91]\n"
"v_exp_f32 v220, v105\n"
"v_mfma_f32_16x16x32_bf16 a[76:79], a[232:235], v[160:163], a[76:79]\n"
"v_cvt_pk_bf16_f32 v148, v223, v224\n"
"v_cvt_pk_bf16_f32 v149, v225, v226\n"
"v_mfma_f32_16x16x32_bf16 a[64:67], a[228:231], v[160:163], a[64:67]\n"
"v_exp_f32 v221, v106\n"
"v_mfma_f32_16x16x32_bf16 a[52:55], a[224:227], v[160:163], a[52:55]\n"
"s_add_u32 m0, s60, 93824\n"
"v_exp_f32 v222, v107\n"
"v_mfma_f32_16x16x32_bf16 a[48:51], a[224:227], v[156:159], a[48:51]\n"
"buffer_load_dwordx4 v204, s[32:35], s63 offen offset:384 nt lds\n"
"v_mfma_f32_16x16x32_bf16 a[60:63], a[228:231], v[156:159], a[60:63]\n"
"ds_read_b64_tr_b16 a[224:225], v192 offset:28672\n"
"ds_read_b64_tr_b16 a[226:227], v192 offset:30720\n"
"v_mfma_f32_16x16x32_bf16 a[72:75], a[232:235], v[156:159], a[72:75]\n"
"ds_read_b64_tr_b16 a[228:229], v193 offset:28672\n"
"ds_read_b64_tr_b16 a[230:231], v193 offset:30720\n"
"s_nop 7\n"
"v_mfma_f32_16x16x32_bf16 a[84:87], a[236:239], v[156:159], a[84:87]\n"
"s_waitcnt lgkmcnt(10)\n"
"ds_read_b64_tr_b16 a[232:233], v192 offset:29184\n"
"ds_read_b64_tr_b16 a[234:235], v192 offset:31232\n"
"v_mfma_f32_16x16x32_bf16 a[96:99], a[208:211], v[156:159], a[96:99]\n"
"s_waitcnt lgkmcnt(10)\n"
"ds_read_b64_tr_b16 a[236:237], v193 offset:29184\n"
"ds_read_b64_tr_b16 a[238:239], v193 offset:31232\n"
"v_mfma_f32_16x16x32_bf16 a[108:111], a[212:215], v[156:159], a[108:111]\n"
"s_waitcnt lgkmcnt(10)\n"
"v_exp_f32 v223, v108\n"
"v_mfma_f32_16x16x32_bf16 a[120:123], a[216:219], v[156:159], a[120:123]\n"
"s_waitcnt lgkmcnt(8)\n"
"v_exp_f32 v224, v109\n"
"v_mfma_f32_16x16x32_bf16 a[132:135], a[220:223], v[156:159], a[132:135]\n"
"v_cvt_pk_bf16_f32 v152, v227, v220\n"
"v_cvt_pk_bf16_f32 v153, v221, v222\n"
"v_mfma_f32_16x16x32_bf16 a[136:139], a[220:223], v[160:163], a[136:139]\n"
"v_exp_f32 v225, v110\n"
"v_mfma_f32_16x16x32_bf16 a[124:127], a[216:219], v[160:163], a[124:127]\n"
"v_exp_f32 v226, v111\n"
"v_mfma_f32_16x16x32_bf16 a[112:115], a[212:215], v[160:163], a[112:115]\n"
"s_waitcnt vmcnt(12)\n"
"s_barrier\n"
"v_mfma_f32_16x16x32_bf16 a[100:103], a[208:211], v[160:163], a[100:103]\n"
"s_mov_b32 s63, 245760\n"
"v_exp_f32 v227, v112\n"
"s_nop 7\n"
"v_mfma_f32_16x16x32_bf16 a[104:107], a[208:211], v[164:167], a[104:107]\n"
"s_add_u32 m0, s60, 98304\n"
"ds_read_b64 a[208:209], v198 offset:32768\n"
"ds_read_b64 a[210:211], v199 offset:32768\n"
"v_mfma_f32_16x16x32_bf16 a[116:119], a[212:215], v[164:167], a[116:119]\n"
"buffer_load_dwordx4 v205, s[28:31], s63 offen nt lds\n"
"v_mfma_f32_16x16x32_bf16 a[128:131], a[216:219], v[164:167], a[128:131]\n"
"ds_read_b64 a[212:213], v198 offset:33280\n"
"ds_read_b64 a[214:215], v199 offset:33280\n"
"v_mfma_f32_16x16x32_bf16 a[140:143], a[220:223], v[164:167], a[140:143]\n"
"s_waitcnt lgkmcnt(10)\n"
"ds_read_b64 a[216:217], v198 offset:36864\n"
"ds_read_b64 a[218:219], v199 offset:36864\n"
"v_mfma_f32_16x16x32_bf16 a[152:155], a[224:227], v[164:167], a[152:155]\n"
"s_waitcnt lgkmcnt(10)\n"
"ds_read_b64 a[220:221], v198 offset:37376\n"
"ds_read_b64 a[222:223], v199 offset:37376\n"
"v_mfma_f32_16x16x32_bf16 a[164:167], a[228:231], v[164:167], a[164:167]\n"
"s_waitcnt lgkmcnt(10)\n"
"v_exp_f32 v220, v113\n"
"v_mfma_f32_16x16x32_bf16 a[176:179], a[232:235], v[164:167], a[176:179]\n"
"s_waitcnt lgkmcnt(8)\n"
"v_cvt_pk_bf16_f32 v146, v223, v224\n"
"v_cvt_pk_bf16_f32 v147, v225, v226\n"
"v_mfma_f32_16x16x32_bf16 a[188:191], a[236:239], v[164:167], a[188:191]\n"
"v_exp_f32 v221, v114\n"
"v_mfma_f32_16x16x32_bf16 a[184:187], a[236:239], v[160:163], a[184:187]\n"
"v_exp_f32 v222, v115\n"
"s_nop 7\n"
"v_mfma_f32_16x16x32_bf16 a[172:175], a[232:235], v[160:163], a[172:175]\n"
"v_exp_f32 v223, v116\n"
"v_mfma_f32_16x16x32_bf16 a[160:163], a[228:231], v[160:163], a[160:163]\n"
"v_exp_f32 v224, v117\n"
"v_mfma_f32_16x16x32_bf16 a[148:151], a[224:227], v[160:163], a[148:151]\n"
"v_cvt_pk_bf16_f32 v150, v227, v220\n"
"v_cvt_pk_bf16_f32 v151, v221, v222\n"
"v_mfma_f32_16x16x32_bf16 a[144:147], a[224:227], v[156:159], a[144:147]\n"
"s_add_u32 m0, s60, 102272\n"
"ds_read_b64 a[224:225], v198 offset:40960\n"
"ds_read_b64 a[226:227], v199 offset:40960\n"
"v_mfma_f32_16x16x32_bf16 a[156:159], a[228:231], v[156:159], a[156:159]\n"
"buffer_load_dwordx4 v205, s[28:31], s63 offen offset:128 nt lds\n"
"v_mfma_f32_16x16x32_bf16 a[168:171], a[232:235], v[156:159], a[168:171]\n"
"ds_read_b64 a[228:229], v198 offset:41472\n"
"ds_read_b64 a[230:231], v199 offset:41472\n"
"v_mfma_f32_16x16x32_bf16 a[180:183], a[236:239], v[156:159], a[180:183]\n"
"s_waitcnt lgkmcnt(10)\n"
"ds_read_b64 a[232:233], v198 offset:45056\n"
"ds_read_b64 a[234:235], v199 offset:45056\n"
"v_mfma_f32_16x16x32_bf16 v[120:123], a[208:211], v[0:3], v[208:211]\n"
"s_waitcnt lgkmcnt(10)\n"
"ds_read_b64 a[236:237], v198 offset:45568\n"
"ds_read_b64 a[238:239], v199 offset:45568\n"
"v_mfma_f32_16x16x32_bf16 v[120:123], a[212:215], v[12:15], v[120:123]\n"
"s_waitcnt lgkmcnt(10)\n"
"v_exp_f32 v225, v118\n"
"s_nop 7\n"
"v_mfma_f32_16x16x32_bf16 v[120:123], a[216:219], v[24:27], v[120:123]\n"
"s_waitcnt lgkmcnt(8)\n"
"v_exp_f32 v226, v119\n"
"v_mfma_f32_16x16x32_bf16 v[120:123], a[220:223], v[36:39], v[120:123]\n"
"v_cvt_pk_bf16_f32 v154, v223, v224\n"
"v_cvt_pk_bf16_f32 v155, v225, v226\n"
"v_mfma_f32_16x16x32_bf16 v[124:127], a[220:223], v[40:43], v[212:215]\n"
"v_mfma_f32_16x16x32_bf16 v[124:127], a[216:219], v[28:31], v[124:127]\n"
"v_mfma_f32_16x16x32_bf16 v[124:127], a[212:215], v[16:19], v[124:127]\n"
"v_mfma_f32_16x16x32_bf16 v[124:127], a[208:211], v[4:7], v[124:127]\n"
"v_mfma_f32_16x16x32_bf16 v[128:131], a[208:211], v[8:11], v[216:219]\n"
"ds_read_b64 a[208:209], v198 offset:34816\n"
"ds_read_b64 a[210:211], v199 offset:34816\n"
"v_mfma_f32_16x16x32_bf16 v[128:131], a[212:215], v[20:23], v[128:131]\n"
"s_add_u32 m0, s60, 106240\n"
"ds_read_b64 a[212:213], v198 offset:35328\n"
"ds_read_b64 a[214:215], v199 offset:35328\n"
"v_mfma_f32_16x16x32_bf16 v[128:131], a[216:219], v[32:35], v[128:131]\n"
"buffer_load_dwordx4 v205, s[28:31], s63 offen offset:256 nt lds\n"
"s_nop 7\n"
"v_mfma_f32_16x16x32_bf16 v[128:131], a[220:223], v[44:47], v[128:131]\n"
"s_waitcnt lgkmcnt(10)\n"
"ds_read_b64 a[216:217], v198 offset:38912\n"
"ds_read_b64 a[218:219], v199 offset:38912\n"
"v_mfma_f32_16x16x32_bf16 v[128:131], a[224:227], v[56:59], v[128:131]\n"
"s_waitcnt lgkmcnt(10)\n"
"ds_read_b64 a[220:221], v198 offset:39424\n"
"ds_read_b64 a[222:223], v199 offset:39424\n"
"v_mfma_f32_16x16x32_bf16 v[128:131], a[228:231], v[68:71], v[128:131]\n"
"s_waitcnt lgkmcnt(10)\n"
"v_mfma_f32_16x16x32_bf16 v[128:131], a[232:235], v[80:83], v[128:131]\n"
"s_waitcnt lgkmcnt(8)\n"
"v_mfma_f32_16x16x32_bf16 v[128:131], a[236:239], v[92:95], v[128:131]\n"
"v_mfma_f32_16x16x32_bf16 v[124:127], a[236:239], v[88:91], v[124:127]\n"
"v_mfma_f32_16x16x32_bf16 v[124:127], a[232:235], v[76:79], v[124:127]\n"
"v_mfma_f32_16x16x32_bf16 v[124:127], a[228:231], v[64:67], v[124:127]\n"
"v_mfma_f32_16x16x32_bf16 v[124:127], a[224:227], v[52:55], v[124:127]\n"
"s_nop 7\n"
"v_mfma_f32_16x16x32_bf16 v[120:123], a[224:227], v[48:51], v[120:123]\n"
"ds_read_b64 a[224:225], v198 offset:43008\n"
"ds_read_b64 a[226:227], v199 offset:43008\n"
"v_mfma_f32_16x16x32_bf16 v[120:123], a[228:231], v[60:63], v[120:123]\n"
"ds_read_b64 a[228:229], v198 offset:43520\n"
"ds_read_b64 a[230:231], v199 offset:43520\n"
"v_mfma_f32_16x16x32_bf16 v[120:123], a[232:235], v[72:75], v[120:123]\n"
"s_add_u32 m0, s60, 110208\n"
"ds_read_b64 a[232:233], v198 offset:47104\n"
"ds_read_b64 a[234:235], v199 offset:47104\n"
"v_mfma_f32_16x16x32_bf16 v[120:123], a[236:239], v[84:87], v[120:123]\n"
"s_waitcnt lgkmcnt(12)\n"
"buffer_load_dwordx4 v205, s[28:31], s63 offen offset:384 nt lds\n"
"v_mfma_f32_16x16x32_bf16 v[132:135], a[208:211], v[0:3], v[208:211]\n"
"s_waitcnt lgkmcnt(10)\n"
"ds_read_b64 a[236:237], v198 offset:47616\n"
"ds_read_b64 a[238:239], v199 offset:47616\n"
"v_mfma_f32_16x16x32_bf16 v[132:135], a[212:215], v[12:15], v[132:135]\n"
"s_waitcnt lgkmcnt(10)\n"
"v_mfma_f32_16x16x32_bf16 v[132:135], a[216:219], v[24:27], v[132:135]\n"
"s_waitcnt lgkmcnt(8)\n"
"v_mfma_f32_16x16x32_bf16 v[132:135], a[220:223], v[36:39], v[132:135]\n"
"v_mfma_f32_16x16x32_bf16 v[136:139], a[220:223], v[40:43], v[212:215]\n"
"s_nop 7\n"
"v_mfma_f32_16x16x32_bf16 v[136:139], a[216:219], v[28:31], v[136:139]\n"
"v_mfma_f32_16x16x32_bf16 v[136:139], a[212:215], v[16:19], v[136:139]\n"
"s_waitcnt vmcnt(12)\n"
"s_barrier\n"
"v_mfma_f32_16x16x32_bf16 v[136:139], a[208:211], v[4:7], v[136:139]\n"
"v_mfma_f32_16x16x32_bf16 v[140:143], a[208:211], v[8:11], v[216:219]\n"
"ds_read_b64_tr_b16 a[208:209], v192 offset:49152\n"
"ds_read_b64_tr_b16 a[210:211], v192 offset:51200\n"
"v_mfma_f32_16x16x32_bf16 v[140:143], a[212:215], v[20:23], v[140:143]\n"
"ds_read_b64_tr_b16 a[212:213], v193 offset:49152\n"
"ds_read_b64_tr_b16 a[214:215], v193 offset:51200\n"
"v_mfma_f32_16x16x32_bf16 v[140:143], a[216:219], v[32:35], v[140:143]\n"
"s_mov_b32 s63, 229376\n"
"ds_read_b64_tr_b16 a[216:217], v192 offset:49664\n"
"ds_read_b64_tr_b16 a[218:219], v192 offset:51712\n"
"v_mfma_f32_16x16x32_bf16 v[140:143], a[220:223], v[44:47], v[140:143]\n"
"s_add_u32 m0, s60, 114688\n"
"ds_read_b64_tr_b16 a[220:221], v193 offset:49664\n"
"s_waitcnt lgkmcnt(14)\n"
"ds_read_b64_tr_b16 a[222:223], v193 offset:51712\n"
"v_mfma_f32_16x16x32_bf16 a[0:3], a[204:207], v[144:147], a[192:195]\n"
"buffer_load_dwordx4 v204, s[32:35], s63 offen nt lds\n"
"v_mfma_f32_16x16x32_bf16 a[4:7], a[204:207], v[148:151], a[196:199]\n"
"s_nop 7\n"
"v_mfma_f32_16x16x32_bf16 a[8:11], a[204:207], v[152:155], a[200:203]\n"
"s_waitcnt lgkmcnt(14)\n"
"v_mfma_f32_16x16x32_bf16 v[140:143], a[224:227], v[56:59], v[140:143]\n"
"s_waitcnt lgkmcnt(12)\n"
"v_mfma_f32_16x16x32_bf16 v[140:143], a[228:231], v[68:71], v[140:143]\n"
"s_waitcnt lgkmcnt(10)\n"
"v_mfma_f32_16x16x32_bf16 v[140:143], a[232:235], v[80:83], v[140:143]\n"
"s_waitcnt lgkmcnt(8)\n"
"v_accvgpr_read_b32 v227, a0\n"
"v_accvgpr_read_b32 v220, a4\n"
"v_mfma_f32_16x16x32_bf16 v[140:143], a[236:239], v[92:95], v[140:143]\n"
"v_accvgpr_read_b32 v221, a8\n"
"v_add_f32 v227, v227, v220\n"
"v_mfma_f32_16x16x32_bf16 v[136:139], a[236:239], v[88:91], v[136:139]\n"
"v_add_f32 v227, v227, v221\n"
"v_cmp_ngt_f32 vcc, 0x47800000, v227\n"
"v_mfma_f32_16x16x32_bf16 v[136:139], a[232:235], v[76:79], v[136:139]\n"
"v_mfma_f32_16x16x32_bf16 v[136:139], a[228:231], v[64:67], v[136:139]\n"
"v_mfma_f32_16x16x32_bf16 v[136:139], a[224:227], v[52:55], v[136:139]\n"
"s_nop 7\n"
"v_mfma_f32_16x16x32_bf16 v[132:135], a[224:227], v[48:51], v[132:135]\n"
"s_add_u32 m0, s60, 118656\n"
"ds_read_b64_tr_b16 a[224:225], v192 offset:53248\n"
"ds_read_b64_tr_b16 a[226:227], v192 offset:55296\n"
"v_mfma_f32_16x16x32_bf16 v[132:135], a[228:231], v[60:63], v[132:135]\n"
"buffer_load_dwordx4 v204, s[32:35], s63 offen offset:128 nt lds\n"
"v_mfma_f32_16x16x32_bf16 v[132:135], a[232:235], v[72:75], v[132:135]\n"
"ds_read_b64_tr_b16 a[228:229], v193 offset:53248\n"
"ds_read_b64_tr_b16 a[230:231], v193 offset:55296\n"
"v_mfma_f32_16x16x32_bf16 v[132:135], a[236:239], v[84:87], v[132:135]\n"
"s_waitcnt lgkmcnt(10)\n"
"s_cbranch_vccnz Lstub12\n"
"Lback12:\n"
"ds_read_b64_tr_b16 a[232:233], v192 offset:53760\n"
"v_mfma_f32_16x16x32_bf16 v[168:171], a[208:211], v[144:147], v[168:171]\n"
"s_waitcnt lgkmcnt(9)\n"
"ds_read_b64_tr_b16 a[234:235], v192 offset:55808\n"
"ds_read_b64_tr_b16 a[236:237], v193 offset:53760\n"
"v_mfma_f32_16x16x32_bf16 v[180:183], a[212:215], v[144:147], v[180:183]\n"
"s_waitcnt lgkmcnt(9)\n"
"ds_read_b64_tr_b16 a[238:239], v193 offset:55808\n"
"v_mfma_f32_16x16x32_bf16 v[228:231], a[216:219], v[144:147], v[228:231]\n"
"s_waitcnt lgkmcnt(8)\n"
"v_exp_f32 v222, v120\n"
"s_nop 7\n"
"v_mfma_f32_16x16x32_bf16 v[240:243], a[220:223], v[144:147], v[240:243]\n"
"v_exp_f32 v223, v121\n"
"v_mfma_f32_16x16x32_bf16 v[244:247], a[220:223], v[148:151], v[244:247]\n"
"v_exp_f32 v224, v122\n"
"v_mfma_f32_16x16x32_bf16 v[232:235], a[216:219], v[148:151], v[232:235]\n"
"v_exp_f32 v225, v123\n"
"v_mfma_f32_16x16x32_bf16 v[184:187], a[212:215], v[148:151], v[184:187]\n"
"v_exp_f32 v226, v124\n"
"v_mfma_f32_16x16x32_bf16 v[172:175], a[208:211], v[148:151], v[172:175]\n"
"v_exp_f32 v227, v125\n"
"v_mfma_f32_16x16x32_bf16 v[176:179], a[208:211], v[152:155], v[176:179]\n"
"ds_read_b64_tr_b16 a[208:209], v192 offset:57344\n"
"ds_read_b64_tr_b16 a[210:211], v192 offset:59392\n"
"v_mfma_f32_16x16x32_bf16 v[188:191], a[212:215], v[152:155], v[188:191]\n"
"s_add_u32 m0, s60, 122624\n"
"ds_read_b64_tr_b16 a[212:213], v193 offset:57344\n"
"ds_read_b64_tr_b16 a[214:215], v193 offset:59392\n"
"s_nop 7\n"
"v_mfma_f32_16x16x32_bf16 v[236:239], a[216:219], v[152:155], v[236:239]\n"
"buffer_load_dwordx4 v204, s[32:35], s63 offen offset:256 nt lds\n"
"v_mfma_f32_16x16x32_bf16 v[248:251], a[220:223], v[152:155], v[248:251]\n"
"s_waitcnt lgkmcnt(10)\n"
"ds_read_b64_tr_b16 a[216:217], v192 offset:57856\n"
"ds_read_b64_tr_b16 a[218:219], v192 offset:59904\n"
"v_mfma_f32_16x16x32_bf16 a[56:59], a[224:227], v[152:155], a[56:59]\n"
"s_waitcnt lgkmcnt(10)\n"
"ds_read_b64_tr_b16 a[220:221], v193 offset:57856\n"
"ds_read_b64_tr_b16 a[222:223], v193 offset:59904\n"
"v_mfma_f32_16x16x32_bf16 a[68:71], a[228:231], v[152:155], a[68:71]\n"
"s_waitcnt lgkmcnt(10)\n"
"v_cvt_pk_bf16_f32 v156, v222, v223\n"
"v_cvt_pk_bf16_f32 v157, v224, v225\n"
"v_mfma_f32_16x16x32_bf16 a[80:83], a[232:235], v[152:155], a[80:83]\n"
"s_waitcnt lgkmcnt(8)\n"
"v_exp_f32 v220, v126\n"
"v_mfma_f32_16x16x32_bf16 a[92:95], a[236:239], v[152:155], a[92:95]\n"
"v_exp_f32 v221, v127\n"
"v_mfma_f32_16x16x32_bf16 a[88:91], a[236:239], v[148:151], a[88:91]\n"
"v_exp_f32 v222, v128\n"
"s_nop 7\n"
"v_mfma_f32_16x16x32_bf16 a[76:79], a[232:235], v[148:151], a[76:79]\n"
"v_exp_f32 v223, v129\n"
"v_mfma_f32_16x16x32_bf16 a[64:67], a[228:231], v[148:151], a[64:67]\n"
"v_cvt_pk_bf16_f32 v160, v226, v227\n"
"v_cvt_pk_bf16_f32 v161, v220, v221\n"
"v_mfma_f32_16x16x32_bf16 a[52:55], a[224:227], v[148:151], a[52:55]\n"
"v_exp_f32 v224, v130\n"
"v_mfma_f32_16x16x32_bf16 a[48:51], a[224:227], v[144:147], a[48:51]\n"
"ds_read_b64_tr_b16 a[224:225], v192 offset:61440\n"
"ds_read_b64_tr_b16 a[226:227], v192 offset:63488\n"
"v_mfma_f32_16x16x32_bf16 a[60:63], a[228:231], v[144:147], a[60:63]\n"
"ds_read_b64_tr_b16 a[228:229], v193 offset:61440\n"
"ds_read_b64_tr_b16 a[230:231], v193 offset:63488\n"
"v_mfma_f32_16x16x32_bf16 a[72:75], a[232:235], v[144:147], a[72:75]\n"
"s_add_u32 m0, s60, 126592\n"
"ds_read_b64_tr_b16 a[232:233], v192 offset:61952\n"
"ds_read_b64_tr_b16 a[234:235], v192 offset:64000\n"
"v_mfma_f32_16x16x32_bf16 a[84:87], a[236:239], v[144:147], a[84:87]\n"
"s_waitcnt lgkmcnt(12)\n"
"buffer_load_dwordx4 v204, s[32:35], s63 offen offset:384 nt lds\n"
"s_nop 7\n"
"v_mfma_f32_16x16x32_bf16 a[96:99], a[208:211], v[144:147], a[96:99]\n"
"s_waitcnt lgkmcnt(10)\n"
"ds_read_b64_tr_b16 a[236:237], v193 offset:61952\n"
"ds_read_b64_tr_b16 a[238:239], v193 offset:64000\n"
"v_mfma_f32_16x16x32_bf16 a[108:111], a[212:215], v[144:147], a[108:111]\n"
"s_waitcnt lgkmcnt(10)\n"
"v_exp_f32 v225, v131\n"
"v_mfma_f32_16x16x32_bf16 a[120:123], a[216:219], v[144:147], a[120:123]\n"
"s_waitcnt lgkmcnt(8)\n"
"v_exp_f32 v226, v132\n"
"v_mfma_f32_16x16x32_bf16 a[132:135], a[220:223], v[144:147], a[132:135]\n"
"v_exp_f32 v227, v133\n"
"v_mfma_f32_16x16x32_bf16 a[136:139], a[220:223], v[148:151], a[136:139]\n"
"v_cvt_pk_bf16_f32 v164, v222, v223\n"
"v_cvt_pk_bf16_f32 v165, v224, v225\n"
"v_mfma_f32_16x16x32_bf16 a[124:127], a[216:219], v[148:151], a[124:127]\n"
"v_exp_f32 v220, v134\n"
"v_mfma_f32_16x16x32_bf16 a[112:115], a[212:215], v[148:151], a[112:115]\n"
"s_waitcnt vmcnt(12)\n"
"s_barrier\n"
"s_nop 7\n"
"v_mfma_f32_16x16x32_bf16 a[100:103], a[208:211], v[148:151], a[100:103]\n"
"v_exp_f32 v221, v135\n"
"v_mfma_f32_16x16x32_bf16 a[104:107], a[208:211], v[152:155], a[104:107]\n"
"ds_read_b64 a[208:209], v200 offset:0\n"
"ds_read_b64 a[210:211], v201 offset:0\n"
"v_mfma_f32_16x16x32_bf16 a[116:119], a[212:215], v[152:155], a[116:119]\n"
"ds_read_b64 a[212:213], v200 offset:512\n"
"ds_read_b64 a[214:215], v201 offset:512\n"
"v_mfma_f32_16x16x32_bf16 a[128:131], a[216:219], v[152:155], a[128:131]\n"
"s_mov_b32 s63, 245760\n"
"ds_read_b64 a[216:217], v200 offset:4096\n"
"ds_read_b64 a[218:219], v201 offset:4096\n"
"v_mfma_f32_16x16x32_bf16 a[140:143], a[220:223], v[152:155], a[140:143]\n"
"s_waitcnt lgkmcnt(12)\n"
"s_add_u32 m0, s60, 131072\n"
"ds_read_b64 a[220:221], v200 offset:4608\n"
"v_mfma_f32_16x16x32_bf16 a[152:155], a[224:227], v[152:155], a[152:155]\n"
"s_waitcnt lgkmcnt(11)\n"
"buffer_load_dwordx4 v204, s[32:35], s63 offen nt lds\n"
"v_mfma_f32_16x16x32_bf16 a[164:167], a[228:231], v[152:155], a[164:167]\n"
"s_waitcnt lgkmcnt(9)\n"
"ds_read_b64 a[222:223], v201 offset:4608\n"
"s_nop 7\n"
"v_mfma_f32_16x16x32_bf16 a[176:179], a[232:235], v[152:155], a[176:179]\n"
"s_waitcnt lgkmcnt(8)\n"
"v_exp_f32 v222, v136\n"
"v_mfma_f32_16x16x32_bf16 a[188:191], a[236:239], v[152:155], a[188:191]\n"
"v_exp_f32 v223, v137\n"
"v_mfma_f32_16x16x32_bf16 a[184:187], a[236:239], v[148:151], a[184:187]\n"
"v_cvt_pk_bf16_f32 v158, v226, v227\n"
"v_cvt_pk_bf16_f32 v159, v220, v221\n"
"v_mfma_f32_16x16x32_bf16 a[172:175], a[232:235], v[148:151], a[172:175]\n"
"v_exp_f32 v224, v138\n"
"v_mfma_f32_16x16x32_bf16 a[160:163], a[228:231], v[148:151], a[160:163]\n"
"v_exp_f32 v225, v139\n"
"v_mfma_f32_16x16x32_bf16 a[148:151], a[224:227], v[148:151], a[148:151]\n"
"v_exp_f32 v226, v140\n"
"v_mfma_f32_16x16x32_bf16 a[144:147], a[224:227], v[144:147], a[144:147]\n"
"ds_read_b64 a[224:225], v200 offset:8192\n"
"ds_read_b64 a[226:227], v201 offset:8192\n"
"s_nop 7\n"
"v_mfma_f32_16x16x32_bf16 a[156:159], a[228:231], v[144:147], a[156:159]\n"
"ds_read_b64 a[228:229], v200 offset:8704\n"
"ds_read_b64 a[230:231], v201 offset:8704\n"
"v_mfma_f32_16x16x32_bf16 a[168:171], a[232:235], v[144:147], a[168:171]\n"
"ds_read_b64 a[232:233], v200 offset:12288\n"
"ds_read_b64 a[234:235], v201 offset:12288\n"
"v_mfma_f32_16x16x32_bf16 a[180:183], a[236:239], v[144:147], a[180:183]\n"
"s_waitcnt lgkmcnt(12)\n"
"s_add_u32 m0, s60, 135040\n"
"ds_read_b64 a[236:237], v200 offset:12800\n"
"v_mfma_f32_16x16x32_bf16 v[96:99], a[208:211], v[0:3], v[208:211]\n"
"s_waitcnt lgkmcnt(11)\n"
"buffer_load_dwordx4 v204, s[32:35], s63 offen offset:128 nt lds\n"
"v_mfma_f32_16x16x32_bf16 v[96:99], a[212:215], v[12:15], v[96:99]\n"
"s_waitcnt lgkmcnt(9)\n"
"ds_read_b64 a[238:239], v201 offset:12800\n"
"v_mfma_f32_16x16x32_bf16 v[96:99], a[216:219], v[24:27], v[96:99]\n"
"s_waitcnt lgkmcnt(8)\n"
"v_exp_f32 v227, v141\n"
"v_mfma_f32_16x16x32_bf16 v[96:99], a[220:223], v[36:39], v[96:99]\n"
"v_cvt_pk_bf16_f32 v162, v222, v223\n"
"v_cvt_pk_bf16_f32 v163, v224, v225\n"
"s_nop 7\n"
"v_mfma_f32_16x16x32_bf16 v[100:103], a[220:223], v[40:43], v[212:215]\n"
"v_exp_f32 v220, v142\n"
"v_mfma_f32_16x16x32_bf16 v[100:103], a[216:219], v[28:31], v[100:103]\n"
"v_exp_f32 v221, v143\n"
"v_mfma_f32_16x16x32_bf16 v[100:103], a[212:215], v[16:19], v[100:103]\n"
"v_cvt_pk_bf16_f32 v166, v226, v227\n"
"v_cvt_pk_bf16_f32 v167, v220, v221\n"
"v_mfma_f32_16x16x32_bf16 v[100:103], a[208:211], v[4:7], v[100:103]\n"
"v_mfma_f32_16x16x32_bf16 v[104:107], a[208:211], v[8:11], v[216:219]\n"
"ds_read_b64 a[208:209], v200 offset:2048\n"
"ds_read_b64 a[210:211], v201 offset:2048\n"
"v_mfma_f32_16x16x32_bf16 v[104:107], a[212:215], v[20:23], v[104:107]\n"
"ds_read_b64 a[212:213], v200 offset:2560\n"
"ds_read_b64 a[214:215], v201 offset:2560\n"
"v_mfma_f32_16x16x32_bf16 v[104:107], a[216:219], v[32:35], v[104:107]\n"
"ds_read_b64 a[216:217], v200 offset:6144\n"
"ds_read_b64 a[218:219], v201 offset:6144\n"
"s_nop 7\n"
"v_mfma_f32_16x16x32_bf16 v[104:107], a[220:223], v[44:47], v[104:107]\n"
"s_waitcnt lgkmcnt(12)\n"
"ds_read_b64 a[220:221], v200 offset:6656\n"
"ds_read_b64 a[222:223], v201 offset:6656\n"
"v_mfma_f32_16x16x32_bf16 v[104:107], a[224:227], v[56:59], v[104:107]\n"
"s_waitcnt lgkmcnt(12)\n"
"s_add_u32 m0, s60, 139008\n"
"v_mfma_f32_16x16x32_bf16 v[104:107], a[228:231], v[68:71], v[104:107]\n"
"s_waitcnt lgkmcnt(10)\n"
"buffer_load_dwordx4 v204, s[32:35], s63 offen offset:256 nt lds\n"
"v_mfma_f32_16x16x32_bf16 v[104:107], a[232:235], v[80:83], v[104:107]\n"
"s_waitcnt lgkmcnt(8)\n"
"v_mfma_f32_16x16x32_bf16 v[104:107], a[236:239], v[92:95], v[104:107]\n"
"v_mfma_f32_16x16x32_bf16 v[100:103], a[236:239], v[88:91], v[100:103]\n"
"v_mfma_f32_16x16x32_bf16 v[100:103], a[232:235], v[76:79], v[100:103]\n"
"s_nop 7\n"
"v_mfma_f32_16x16x32_bf16 v[100:103], a[228:231], v[64:67], v[100:103]\n"
"v_mfma_f32_16x16x32_bf16 v[100:103], a[224:227], v[52:55], v[100:103]\n"
"v_mfma_f32_16x16x32_bf16 v[96:99], a[224:227], v[48:51], v[96:99]\n"
"ds_read_b64 a[224:225], v200 offset:10240\n"
"ds_read_b64 a[226:227], v201 offset:10240\n"
"v_mfma_f32_16x16x32_bf16 v[96:99], a[228:231], v[60:63], v[96:99]\n"
"ds_read_b64 a[228:229], v200 offset:10752\n"
"ds_read_b64 a[230:231], v201 offset:10752\n"
"v_mfma_f32_16x16x32_bf16 v[96:99], a[232:235], v[72:75], v[96:99]\n"
"ds_read_b64 a[232:233], v200 offset:14336\n"
"ds_read_b64 a[234:235], v201 offset:14336\n"
"v_mfma_f32_16x16x32_bf16 v[96:99], a[236:239], v[84:87], v[96:99]\n"
"s_waitcnt lgkmcnt(12)\n"
"ds_read_b64 a[236:237], v200 offset:14848\n"
"ds_read_b64 a[238:239], v201 offset:14848\n"
"v_mfma_f32_16x16x32_bf16 v[108:111], a[208:211], v[0:3], v[208:211]\n"
"s_waitcnt lgkmcnt(12)\n"
"s_nop 7\n"
"v_mfma_f32_16x16x32_bf16 v[108:111], a[212:215], v[12:15], v[108:111]\n"
"s_waitcnt lgkmcnt(10)\n"
"s_add_u32 m0, s60, 142976\n"
"v_mfma_f32_16x16x32_bf16 v[108:111], a[216:219], v[24:27], v[108:111]\n"
"s_waitcnt lgkmcnt(8)\n"
"buffer_load_dwordx4 v204, s[32:35], s63 offen offset:384 nt lds\n"
"v_mfma_f32_16x16x32_bf16 v[108:111], a[220:223], v[36:39], v[108:111]\n"
"v_mfma_f32_16x16x32_bf16 v[112:115], a[220:223], v[40:43], v[212:215]\n"
"v_mfma_f32_16x16x32_bf16 v[112:115], a[216:219], v[28:31], v[112:115]\n"
"v_mfma_f32_16x16x32_bf16 v[112:115], a[212:215], v[16:19], v[112:115]\n"
"s_waitcnt vmcnt(12)\n"
"s_barrier\n"
"v_mfma_f32_16x16x32_bf16 v[112:115], a[208:211], v[4:7], v[112:115]\n"
"s_nop 7\n"
"v_mfma_f32_16x16x32_bf16 v[116:119], a[208:211], v[8:11], v[216:219]\n"
"ds_read_b64_tr_b16 a[208:209], v194 offset:16384\n"
"ds_read_b64_tr_b16 a[210:211], v194 offset:18432\n"
"v_mfma_f32_16x16x32_bf16 v[116:119], a[212:215], v[20:23], v[116:119]\n"
"ds_read_b64_tr_b16 a[212:213], v195 offset:16384\n"
"ds_read_b64_tr_b16 a[214:215], v195 offset:18432\n"
"v_mfma_f32_16x16x32_bf16 v[116:119], a[216:219], v[32:35], v[116:119]\n"
"ds_read_b64_tr_b16 a[216:217], v194 offset:16896\n"
"ds_read_b64_tr_b16 a[218:219], v194 offset:18944\n"
"v_mfma_f32_16x16x32_bf16 v[116:119], a[220:223], v[44:47], v[116:119]\n"
"ds_read_b64_tr_b16 a[220:221], v195 offset:16896\n"
"s_waitcnt lgkmcnt(14)\n"
"ds_read_b64_tr_b16 a[222:223], v195 offset:18944\n"
"v_mfma_f32_16x16x32_bf16 a[192:195], a[204:207], v[156:159], a[0:3]\n"
"v_mfma_f32_16x16x32_bf16 a[196:199], a[204:207], v[160:163], a[4:7]\n"
"v_mfma_f32_16x16x32_bf16 a[200:203], a[204:207], v[164:167], a[8:11]\n"
"s_waitcnt lgkmcnt(14)\n"
"s_nop 7\n"
"v_mfma_f32_16x16x32_bf16 v[116:119], a[224:227], v[56:59], v[116:119]\n"
"s_waitcnt lgkmcnt(12)\n"
"v_mfma_f32_16x16x32_bf16 v[116:119], a[228:231], v[68:71], v[116:119]\n"
"s_waitcnt lgkmcnt(10)\n"
"v_mfma_f32_16x16x32_bf16 v[116:119], a[232:235], v[80:83], v[116:119]\n"
"s_waitcnt lgkmcnt(8)\n"
"v_accvgpr_read_b32 v222, a192\n"
"v_accvgpr_read_b32 v223, a196\n"
"v_mfma_f32_16x16x32_bf16 v[116:119], a[236:239], v[92:95], v[116:119]\n"
"v_accvgpr_read_b32 v224, a200\n"
"v_add_f32 v222, v222, v223\n"
"v_mfma_f32_16x16x32_bf16 v[112:115], a[236:239], v[88:91], v[112:115]\n"
"v_add_f32 v222, v222, v224\n"
"v_cmp_ngt_f32 vcc, 0x47800000, v222\n"
"v_mfma_f32_16x16x32_bf16 v[112:115], a[232:235], v[76:79], v[112:115]\n"
"v_mfma_f32_16x16x32_bf16 v[112:115], a[228:231], v[64:67], v[112:115]\n"
"s_nop 7\n"
"v_mfma_f32_16x16x32_bf16 v[112:115], a[224:227], v[52:55], v[112:115]\n"
"v_mfma_f32_16x16x32_bf16 v[108:111], a[224:227], v[48:51], v[108:111]\n"
"ds_read_b64_tr_b16 a[224:225], v194 offset:20480\n"
"ds_read_b64_tr_b16 a[226:227], v194 offset:22528\n"
"v_mfma_f32_16x16x32_bf16 v[108:111], a[228:231], v[60:63], v[108:111]\n"
"ds_read_b64_tr_b16 a[228:229], v195 offset:20480\n"
"ds_read_b64_tr_b16 a[230:231], v195 offset:22528\n"
"v_mfma_f32_16x16x32_bf16 v[108:111], a[232:235], v[72:75], v[108:111]\n"
"ds_read_b64_tr_b16 a[232:233], v194 offset:20992\n"
"ds_read_b64_tr_b16 a[234:235], v194 offset:23040\n"
"v_mfma_f32_16x16x32_bf16 v[108:111], a[236:239], v[84:87], v[108:111]\n"
"s_waitcnt lgkmcnt(12)\n"
"s_cbranch_vccnz Lstub13\n"
"Lback13:\n"
"ds_read_b64_tr_b16 a[236:237], v195 offset:20992\n"
"v_mfma_f32_16x16x32_bf16 v[168:171], a[208:211], v[156:159], v[168:171]\n"
"s_waitcnt lgkmcnt(11)\n"
"ds_read_b64_tr_b16 a[238:239], v195 offset:23040\n"
"v_mfma_f32_16x16x32_bf16 v[180:183], a[212:215], v[156:159], v[180:183]\n"
"s_waitcnt lgkmcnt(10)\n"
"v_exp_f32 v225, v96\n"
"s_nop 7\n"
"v_mfma_f32_16x16x32_bf16 v[228:231], a[216:219], v[156:159], v[228:231]\n"
"s_waitcnt lgkmcnt(8)\n"
"v_exp_f32 v226, v97\n"
"v_mfma_f32_16x16x32_bf16 v[240:243], a[220:223], v[156:159], v[240:243]\n"
"v_exp_f32 v227, v98\n"
"v_mfma_f32_16x16x32_bf16 v[244:247], a[220:223], v[160:163], v[244:247]\n"
"v_exp_f32 v220, v99\n"
"v_mfma_f32_16x16x32_bf16 v[232:235], a[216:219], v[160:163], v[232:235]\n"
"v_exp_f32 v221, v100\n"
"v_mfma_f32_16x16x32_bf16 v[184:187], a[212:215], v[160:163], v[184:187]\n"
"v_exp_f32 v222, v101\n"
"v_mfma_f32_16x16x32_bf16 v[172:175], a[208:211], v[160:163], v[172:175]\n"
"s_add_u32 s63, s77, 0\n"
"v_cvt_pk_bf16_f32 v144, v225, v226\n"
"v_cvt_pk_bf16_f32 v145, v227, v220\n"
"v_mfma_f32_16x16x32_bf16 v[176:179], a[208:211], v[164:167], v[176:179]\n"
"s_add_u32 m0, s60, 147456\n"
"ds_read_b64_tr_b16 a[208:209], v194 offset:24576\n"
"ds_read_b64_tr_b16 a[210:211], v194 offset:26624\n"
"s_nop 7\n"
"v_mfma_f32_16x16x32_bf16 v[188:191], a[212:215], v[164:167], v[188:191]\n"
"ds_read_b64_tr_b16 a[212:213], v195 offset:24576\n"
"ds_read_b64_tr_b16 a[214:215], v195 offset:26624\n"
"v_mfma_f32_16x16x32_bf16 v[236:239], a[216:219], v[164:167], v[236:239]\n"
"ds_read_b64_tr_b16 a[216:217], v194 offset:25088\n"
"ds_read_b64_tr_b16 a[218:219], v194 offset:27136\n"
"v_mfma_f32_16x16x32_bf16 v[248:251], a[220:223], v[164:167], v[248:251]\n"
"s_waitcnt lgkmcnt(12)\n"
"buffer_load_dwordx4 v206, s[36:39], s63 offen lds\n"
"v_mfma_f32_16x16x32_bf16 a[56:59], a[224:227], v[164:167], a[56:59]\n"
"s_waitcnt lgkmcnt(10)\n"
"ds_read_b64_tr_b16 a[220:221], v195 offset:25088\n"
"ds_read_b64_tr_b16 a[222:223], v195 offset:27136\n"
"v_mfma_f32_16x16x32_bf16 a[68:71], a[228:231], v[164:167], a[68:71]\n"
"s_waitcnt lgkmcnt(10)\n"
"v_exp_f32 v223, v102\n"
"v_mfma_f32_16x16x32_bf16 a[80:83], a[232:235], v[164:167], a[80:83]\n"
"s_waitcnt lgkmcnt(8)\n"
"s_add_u32 s63, s77, 28672\n"
"v_mfma_f32_16x16x32_bf16 a[92:95], a[236:239], v[164:167], a[92:95]\n"
"s_add_u32 m0, s60, 151552\n"
"v_exp_f32 v224, v103\n"
"s_nop 7\n"
"v_mfma_f32_16x16x32_bf16 a[88:91], a[236:239], v[160:163], a[88:91]\n"
"buffer_load_dwordx4 v206, s[36:39], s63 offen lds\n"
"v_mfma_f32_16x16x32_bf16 a[76:79], a[232:235], v[160:163], a[76:79]\n"
"v_exp_f32 v225, v104\n"
"v_mfma_f32_16x16x32_bf16 a[64:67], a[228:231], v[160:163], a[64:67]\n"
"v_exp_f32 v226, v105\n"
"v_mfma_f32_16x16x32_bf16 a[52:55], a[224:227], v[160:163], a[52:55]\n"
"v_cvt_pk_bf16_f32 v148, v221, v222\n"
"v_cvt_pk_bf16_f32 v149, v223, v224\n"
"v_mfma_f32_16x16x32_bf16 a[48:51], a[224:227], v[156:159], a[48:51]\n"
"ds_read_b64_tr_b16 a[224:225], v194 offset:28672\n"
"ds_read_b64_tr_b16 a[226:227], v194 offset:30720\n"
"v_mfma_f32_16x16x32_bf16 a[60:63], a[228:231], v[156:159], a[60:63]\n"
"s_add_u32 s63, s77, 57344\n"
"ds_read_b64_tr_b16 a[228:229], v195 offset:28672\n"
"ds_read_b64_tr_b16 a[230:231], v195 offset:30720\n"
"v_mfma_f32_16x16x32_bf16 a[72:75], a[232:235], v[156:159], a[72:75]\n"
"s_add_u32 m0, s60, 155648\n"
"ds_read_b64_tr_b16 a[232:233], v194 offset:29184\n"
"ds_read_b64_tr_b16 a[234:235], v194 offset:31232\n"
"s_nop 7\n"
"v_mfma_f32_16x16x32_bf16 a[84:87], a[236:239], v[156:159], a[84:87]\n"
"s_waitcnt lgkmcnt(12)\n"
"ds_read_b64_tr_b16 a[236:237], v195 offset:29184\n"
"ds_read_b64_tr_b16 a[238:239], v195 offset:31232\n"
"v_mfma_f32_16x16x32_bf16 a[96:99], a[208:211], v[156:159], a[96:99]\n"
"s_waitcnt lgkmcnt(12)\n"
"buffer_load_dwordx4 v206, s[36:39], s63 offen lds\n"
"v_mfma_f32_16x16x32_bf16 a[108:111], a[212:215], v[156:159], a[108:111]\n"
"s_waitcnt lgkmcnt(10)\n"
"v_exp_f32 v227, v106\n"
"v_mfma_f32_16x16x32_bf16 a[120:123], a[216:219], v[156:159], a[120:123]\n"
"s_waitcnt lgkmcnt(8)\n"
"v_exp_f32 v220, v107\n"
"v_mfma_f32_16x16x32_bf16 a[132:135], a[220:223], v[156:159], a[132:135]\n"
"v_exp_f32 v221, v108\n"
"v_mfma_f32_16x16x32_bf16 a[136:139], a[220:223], v[160:163], a[136:139]\n"
"s_add_u32 s63, s77, 86016\n"
"v_exp_f32 v222, v109\n"
"v_mfma_f32_16x16x32_bf16 a[124:127], a[216:219], v[160:163], a[124:127]\n"
"s_add_u32 m0, s60, 159744\n"
"v_cvt_pk_bf16_f32 v152, v225, v226\n"
"v_cvt_pk_bf16_f32 v153, v227, v220\n"
"s_nop 7\n"
"v_mfma_f32_16x16x32_bf16 a[112:115], a[212:215], v[160:163], a[112:115]\n"
"s_waitcnt vmcnt(11)\n"
"s_barrier\n"
"v_mfma_f32_16x16x32_bf16 a[100:103], a[208:211], v[160:163], a[100:103]\n"
"buffer_load_dwordx4 v206, s[36:39], s63 offen lds\n"
"v_mfma_f32_16x16x32_bf16 a[104:107], a[208:211], v[164:167], a[104:107]\n"
"ds_read_b64 a[208:209], v200 offset:32768\n"
"ds_read_b64 a[210:211], v201 offset:32768\n"
"v_mfma_f32_16x16x32_bf16 a[116:119], a[212:215], v[164:167], a[116:119]\n"
"ds_read_b64 a[212:213], v200 offset:33280\n"
"ds_read_b64 a[214:215], v201 offset:33280\n"
"v_mfma_f32_16x16x32_bf16 a[128:131], a[216:219], v[164:167], a[128:131]\n"
"ds_read_b64 a[216:217], v200 offset:36864\n"
"ds_read_b64 a[218:219], v201 offset:36864\n"
"v_mfma_f32_16x16x32_bf16 a[140:143], a[220:223], v[164:167], a[140:143]\n"
"s_waitcnt lgkmcnt(12)\n"
"ds_read_b64 a[220:221], v200 offset:37376\n"
"ds_read_b64 a[222:223], v201 offset:37376\n"
"v_mfma_f32_16x16x32_bf16 a[152:155], a[224:227], v[164:167], a[152:155]\n"
"s_waitcnt lgkmcnt(12)\n"
"s_add_u32 s63, s77, 114688\n"
"s_add_u32 m0, s60, 0\n"
"s_nop 7\n"
"v_mfma_f32_16x16x32_bf16 a[164:167], a[228:231], v[164:167], a[164:167]\n"
"s_waitcnt lgkmcnt(10)\n"
"buffer_load_dwordx4 v206, s[36:39], s63 offen lds\n"
"v_mfma_f32_16x16x32_bf16 a[176:179], a[232:235], v[164:167], a[176:179]\n"
"s_waitcnt lgkmcnt(8)\n"
"v_exp_f32 v223, v110\n"
"v_mfma_f32_16x16x32_bf16 a[188:191], a[236:239], v[164:167], a[188:191]\n"
"v_exp_f32 v224, v111\n"
"v_mfma_f32_16x16x32_bf16 a[184:187], a[236:239], v[160:163], a[184:187]\n"
"v_exp_f32 v225, v112\n"
"v_mfma_f32_16x16x32_bf16 a[172:175], a[232:235], v[160:163], a[172:175]\n"
"v_exp_f32 v226, v113\n"
"v_mfma_f32_16x16x32_bf16 a[160:163], a[228:231], v[160:163], a[160:163]\n"
"s_add_u32 s63, s77, 143360\n"
"v_cvt_pk_bf16_f32 v146, v221, v222\n"
"v_cvt_pk_bf16_f32 v147, v223, v224\n"
"v_mfma_f32_16x16x32_bf16 a[148:151], a[224:227], v[160:163], a[148:151]\n"
"s_add_u32 m0, s60, 4096\n"
"v_exp_f32 v227, v114\n"
"s_nop 7\n"
"v_mfma_f32_16x16x32_bf16 a[144:147], a[224:227], v[156:159], a[144:147]\n"
"ds_read_b64 a[224:225], v200 offset:40960\n"
"ds_read_b64 a[226:227], v201 offset:40960\n"
"v_mfma_f32_16x16x32_bf16 a[156:159], a[228:231], v[156:159], a[156:159]\n"
"ds_read_b64 a[228:229], v200 offset:41472\n"
"ds_read_b64 a[230:231], v201 offset:41472\n"
"v_mfma_f32_16x16x32_bf16 a[168:171], a[232:235], v[156:159], a[168:171]\n"
"buffer_load_dwordx4 v206, s[36:39], s63 offen lds\n"
"v_mfma_f32_16x16x32_bf16 a[180:183], a[236:239], v[156:159], a[180:183]\n"
"s_waitcnt lgkmcnt(10)\n"
"ds_read_b64 a[232:233], v200 offset:45056\n"
"ds_read_b64 a[234:235], v201 offset:45056\n"
"v_mfma_f32_16x16x32_bf16 v[120:123], a[208:211], v[0:3], v[208:211]\n"
"s_waitcnt lgkmcnt(10)\n"
"ds_read_b64 a[236:237], v200 offset:45568\n"
"ds_read_b64 a[238:239], v201 offset:45568\n"
"v_mfma_f32_16x16x32_bf16 v[120:123], a[212:215], v[12:15], v[120:123]\n"
"s_waitcnt lgkmcnt(10)\n"
"s_add_u32 s63, s77, 172032\n"
"v_mfma_f32_16x16x32_bf16 v[120:123], a[216:219], v[24:27], v[120:123]\n"
"s_waitcnt lgkmcnt(8)\n"
"s_add_u32 m0, s60, 8192\n"
"s_nop 7\n"
"v_mfma_f32_16x16x32_bf16 v[120:123], a[220:223], v[36:39], v[120:123]\n"
"buffer_load_dwordx4 v206, s[36:39], s63 offen lds\n"
"v_mfma_f32_16x16x32_bf16 v[124:127], a[220:223], v[40:43], v[212:215]\n"
"v_exp_f32 v220, v115\n"
"v_mfma_f32_16x16x32_bf16 v[124:127], a[216:219], v[28:31], v[124:127]\n"
"v_exp_f32 v221, v116\n"
"v_mfma_f32_16x16x32_bf16 v[124:127], a[212:215], v[16:19], v[124:127]\n"
"v_exp_f32 v222, v117\n"
"v_mfma_f32_16x16x32_bf16 v[124:127], a[208:211], v[4:7], v[124:127]\n"
"v_cvt_pk_bf16_f32 v150, v225, v226\n"
"v_cvt_pk_bf16_f32 v151, v227, v220\n"
"v_mfma_f32_16x16x32_bf16 v[128:131], a[208:211], v[8:11], v[216:219]\n"
"s_add_u32 s63, s77, 200704\n"
"ds_read_b64 a[208:209], v200 offset:34816\n"
"ds_read_b64 a[210:211], v201 offset:34816\n"
"v_mfma_f32_16x16x32_bf16 v[128:131], a[212:215], v[20:23], v[128:131]\n"
"s_add_u32 m0, s60, 12288\n"
"ds_read_b64 a[212:213], v200 offset:35328\n"
"ds_read_b64 a[214:215], v201 offset:35328\n"
"s_nop 7\n"
"v_mfma_f32_16x16x32_bf16 v[128:131], a[216:219], v[32:35], v[128:131]\n"
"ds_read_b64 a[216:217], v200 offset:38912\n"
"ds_read_b64 a[218:219], v201 offset:38912\n"
"v_mfma_f32_16x16x32_bf16 v[128:131], a[220:223], v[44:47], v[128:131]\n"
"s_waitcnt lgkmcnt(12)\n"
"ds_read_b64 a[220:221], v200 offset:39424\n"
"ds_read_b64 a[222:223], v201 offset:39424\n"
"v_mfma_f32_16x16x32_bf16 v[128:131], a[224:227], v[56:59], v[128:131]\n"
"s_waitcnt lgkmcnt(12)\n"
"buffer_load_dwordx4 v206, s[36:39], s63 offen lds\n"
"v_mfma_f32_16x16x32_bf16 v[128:131], a[228:231], v[68:71], v[128:131]\n"
"s_waitcnt lgkmcnt(10)\n"
"v_exp_f32 v223, v118\n"
"v_mfma_f32_16x16x32_bf16 v[128:131], a[232:235], v[80:83], v[128:131]\n"
"s_waitcnt lgkmcnt(8)\n"
"v_exp_f32 v224, v119\n"
"v_mfma_f32_16x16x32_bf16 v[128:131], a[236:239], v[92:95], v[128:131]\n"
"s_add_u32 s63, s78, 0\n"
"v_cvt_pk_bf16_f32 v154, v221, v222\n"
"v_cvt_pk_bf16_f32 v155, v223, v224\n"
"v_mfma_f32_16x16x32_bf16 v[124:127], a[236:239], v[88:91], v[124:127]\n"
"s_add_u32 m0, s60, 16384\n"
"s_nop 7\n"
"v_mfma_f32_16x16x32_bf16 v[124:127], a[232:235], v[76:79], v[124:127]\n"
"buffer_load_dwordx4 v206, s[36:39], s63 offen lds\n"
"v_mfma_f32_16x16x32_bf16 v[124:127], a[228:231], v[64:67], v[124:127]\n"
"v_mfma_f32_16x16x32_bf16 v[124:127], a[224:227], v[52:55], v[124:127]\n"
"v_mfma_f32_16x16x32_bf16 v[120:123], a[224:227], v[48:51], v[120:123]\n"
"ds_read_b64 a[224:225], v200 offset:43008\n"
"ds_read_b64 a[226:227], v201 offset:43008\n"
"v_mfma_f32_16x16x32_bf16 v[120:123], a[228:231], v[60:63], v[120:123]\n"
"ds_read_b64 a[228:229], v200 offset:43520\n"
"ds_read_b64 a[230:231], v201 offset:43520\n"
"v_mfma_f32_16x16x32_bf16 v[120:123], a[232:235], v[72:75], v[120:123]\n"
"s_add_u32 s63, s78, 28672\n"
"ds_read_b64 a[232:233], v200 offset:47104\n"
"ds_read_b64 a[234:235], v201 offset:47104\n"
"v_mfma_f32_16x16x32_bf16 v[120:123], a[236:239], v[84:87], v[120:123]\n"
"s_waitcnt lgkmcnt(12)\n"
"s_add_u32 m0, s60, 20480\n"
"ds_read_b64 a[236:237], v200 offset:47616\n"
"s_nop 7\n"
"v_mfma_f32_16x16x32_bf16 v[132:135], a[208:211], v[0:3], v[208:211]\n"
"s_waitcnt lgkmcnt(11)\n"
"buffer_load_dwordx4 v206, s[36:39], s63 offen lds\n"
"v_mfma_f32_16x16x32_bf16 v[132:135], a[212:215], v[12:15], v[132:135]\n"
"s_waitcnt lgkmcnt(9)\n"
"ds_read_b64 a[238:239], v201 offset:47616\n"
"v_mfma_f32_16x16x32_bf16 v[132:135], a[216:219], v[24:27], v[132:135]\n"
"s_waitcnt lgkmcnt(8)\n"
"v_mfma_f32_16x16x32_bf16 v[132:135], a[220:223], v[36:39], v[132:135]\n"
"v_mfma_f32_16x16x32_bf16 v[136:139], a[220:223], v[40:43], v[212:215]\n"
"v_mfma_f32_16x16x32_bf16 v[136:139], a[216:219], v[28:31], v[136:139]\n"
"s_add_u32 s63, s78, 57344\n"
"v_mfma_f32_16x16x32_bf16 v[136:139], a[212:215], v[16:19], v[136:139]\n"
"s_waitcnt vmcnt(14)\n"
"s_barrier\n"
"s_add_u32 m0, s60, 24576\n"
"s_nop 7\n"
"v_mfma_f32_16x16x32_bf16 v[136:139], a[208:211], v[4:7], v[136:139]\n"
"buffer_load_dwordx4 v206, s[36:39], s63 offen lds\n"
"v_mfma_f32_16x16x32_bf16 v[140:143], a[208:211], v[8:11], v[216:219]\n"
"ds_read_b64_tr_b16 a[208:209], v194 offset:49152\n"
"ds_read_b64_tr_b16 a[210:211], v194 offset:51200\n"
"v_mfma_f32_16x16x32_bf16 v[140:143], a[212:215], v[20:23], v[140:143]\n"
"ds_read_b64_tr_b16 a[212:213], v195 offset:49152\n"
"ds_read_b64_tr_b16 a[214:215], v195 offset:51200\n"
"v_mfma_f32_16x16x32_bf16 v[140:143], a[216:219], v[32:35], v[140:143]\n"
"ds_read_b64_tr_b16 a[216:217], v194 offset:49664\n"
"ds_read_b64_tr_b16 a[218:219], v194 offset:51712\n"
"v_mfma_f32_16x16x32_bf16 v[140:143], a[220:223], v[44:47], v[140:143]\n"
"ds_read_b64_tr_b16 a[220:221], v195 offset:49664\n"
"s_waitcnt lgkmcnt(14)\n"
"ds_read_b64_tr_b16 a[222:223], v195 offset:51712\n"
"v_mfma_f32_16x16x32_bf16 a[0:3], a[204:207], v[144:147], a[192:195]\n"
"s_add_u32 s63, s78, 86016\n"
"v_mfma_f32_16x16x32_bf16 a[4:7], a[204:207], v[148:151], a[196:199]\n"
"s_add_u32 m0, s60, 28672\n"
"s_nop 7\n"
"v_mfma_f32_16x16x32_bf16 a[8:11], a[204:207], v[152:155], a[200:203]\n"
"s_waitcnt lgkmcnt(14)\n"
"buffer_load_dwordx4 v206, s[36:39], s63 offen lds\n"
"v_mfma_f32_16x16x32_bf16 v[140:143], a[224:227], v[56:59], v[140:143]\n"
"s_waitcnt lgkmcnt(12)\n"
"v_mfma_f32_16x16x32_bf16 v[140:143], a[228:231], v[68:71], v[140:143]\n"
"s_waitcnt lgkmcnt(10)\n"
"v_mfma_f32_16x16x32_bf16 v[140:143], a[232:235], v[80:83], v[140:143]\n"
"s_waitcnt lgkmcnt(8)\n"
"v_accvgpr_read_b32 v225, a0\n"
"v_accvgpr_read_b32 v226, a4\n"
"v_mfma_f32_16x16x32_bf16 v[140:143], a[236:239], v[92:95], v[140:143]\n"
"v_accvgpr_read_b32 v227, a8\n"
"v_add_f32 v225, v225, v226\n"
"v_mfma_f32_16x16x32_bf16 v[136:139], a[236:239], v[88:91], v[136:139]\n"
"s_add_u32 s63, s78, 114688\n"
"v_add_f32 v225, v225, v227\n"
"v_cmp_ngt_f32 vcc, 0x47800000, v225\n"
"v_mfma_f32_16x16x32_bf16 v[136:139], a[232:235], v[76:79], v[136:139]\n"
"s_add_u32 m0, s60, 32768\n"
"s_nop 7\n"
"v_mfma_f32_16x16x32_bf16 v[136:139], a[228:231], v[64:67], v[136:139]\n"
"buffer_load_dwordx4 v206, s[36:39], s63 offen lds\n"
"v_mfma_f32_16x16x32_bf16 v[136:139], a[224:227], v[52:55], v[136:139]\n"
"v_mfma_f32_16x16x32_bf16 v[132:135], a[224:227], v[48:51], v[132:135]\n"
"ds_read_b64_tr_b16 a[224:225], v194 offset:53248\n"
"ds_read_b64_tr_b16 a[226:227], v194 offset:55296\n"
"v_mfma_f32_16x16x32_bf16 v[132:135], a[228:231], v[60:63], v[132:135]\n"
"ds_read_b64_tr_b16 a[228:229], v195 offset:53248\n"
"ds_read_b64_tr_b16 a[230:231], v195 offset:55296\n"
"v_mfma_f32_16x16x32_bf16 v[132:135], a[232:235], v[72:75], v[132:135]\n"
"ds_read_b64_tr_b16 a[232:233], v194 offset:53760\n"
"ds_read_b64_tr_b16 a[234:235], v194 offset:55808\n"
"v_mfma_f32_16x16x32_bf16 v[132:135], a[236:239], v[84:87], v[132:135]\n"
"s_waitcnt lgkmcnt(12)\n"
"s_cbranch_vccnz Lstub14\n"
"Lback14:\n"
"ds_read_b64_tr_b16 a[236:237], v195 offset:53760\n"
"v_mfma_f32_16x16x32_bf16 v[168:171], a[208:211], v[144:147], v[168:171]\n"
"s_waitcnt lgkmcnt(11)\n"
"ds_read_b64_tr_b16 a[238:239], v195 offset:55808\n"
"s_add_u32 s63, s78, 143360\n"
"s_nop 7\n"
"v_mfma_f32_16x16x32_bf16 v[180:183], a[212:215], v[144:147], v[180:183]\n"
"s_waitcnt lgkmcnt(10)\n"
"v_exp_f32 v220, v120\n"
"v_mfma_f32_16x16x32_bf16 v[228:231], a[216:219], v[144:147], v[228:231]\n"
"s_waitcnt lgkmcnt(8)\n"
"s_add_u32 m0, s60, 36864\n"
"v_mfma_f32_16x16x32_bf16 v[240:243], a[220:223], v[144:147], v[240:243]\n"
"v_exp_f32 v221, v121\n"
"v_mfma_f32_16x16x32_bf16 v[244:247], a[220:223], v[148:151], v[244:247]\n"
"buffer_load_dwordx4 v206, s[36:39], s63 offen lds\n"
"v_mfma_f32_16x16x32_bf16 v[232:235], a[216:219], v[148:151], v[232:235]\n"
"v_exp_f32 v222, v122\n"
"v_mfma_f32_16x16x32_bf16 v[184:187], a[212:215], v[148:151], v[184:187]\n"
"s_add_u32 s63, s78, 172032\n"
"v_exp_f32 v223, v123\n"
"v_mfma_f32_16x16x32_bf16 v[172:175], a[208:211], v[148:151], v[172:175]\n"
"s_add_u32 m0, s60, 40960\n"
"v_exp_f32 v224, v124\n"
"s_nop 7\n"
"v_mfma_f32_16x16x32_bf16 v[176:179], a[208:211], v[152:155], v[176:179]\n"
"v_exp_f32 v225, v125\n"
"v_mfma_f32_16x16x32_bf16 v[188:191], a[212:215], v[152:155], v[188:191]\n"
"v_cvt_pk_bf16_f32 v156, v220, v221\n"
"v_cvt_pk_bf16_f32 v157, v222, v223\n"
"v_mfma_f32_16x16x32_bf16 v[236:239], a[216:219], v[152:155], v[236:239]\n"
"v_exp_f32 v226, v126\n"
"v_mfma_f32_16x16x32_bf16 v[248:251], a[220:223], v[152:155], v[248:251]\n"
"s_waitcnt lgkmcnt(6)\n"
"ds_read_b64_tr_b16 a[208:209], v194 offset:57344\n"
"ds_read_b64_tr_b16 a[210:211], v194 offset:59392\n"
"v_mfma_f32_16x16x32_bf16 a[56:59], a[224:227], v[152:155], a[56:59]\n"
"s_waitcnt lgkmcnt(6)\n"
"v_exp_f32 v227, v127\n"
"v_mfma_f32_16x16x32_bf16 a[68:71], a[228:231], v[152:155], a[68:71]\n"
"s_waitcnt lgkmcnt(4)\n"
"ds_read_b64_tr_b16 a[212:213], v195 offset:57344\n"
"ds_read_b64_tr_b16 a[214:215], v195 offset:59392\n"
"v_mfma_f32_16x16x32_bf16 a[80:83], a[232:235], v[152:155], a[80:83]\n"
"s_waitcnt lgkmcnt(4)\n"
"buffer_load_dwordx4 v206, s[36:39], s63 offen lds\n"
"s_nop 7\n"
"v_mfma_f32_16x16x32_bf16 a[92:95], a[236:239], v[152:155], a[92:95]\n"
"s_add_u32 s63, s78, 200704\n"
"v_exp_f32 v220, v128\n"
"v_mfma_f32_16x16x32_bf16 a[88:91], a[236:239], v[148:151], a[88:91]\n"
"s_add_u32 m0, s60, 45056\n"
"ds_read_b64_tr_b16 a[216:217], v194 offset:57856\n"
"ds_read_b64_tr_b16 a[218:219], v194 offset:59904\n"
"v_mfma_f32_16x16x32_bf16 a[76:79], a[232:235], v[148:151], a[76:79]\n"
"v_exp_f32 v221, v129\n"
"v_mfma_f32_16x16x32_bf16 a[64:67], a[228:231], v[148:151], a[64:67]\n"
"ds_read_b64_tr_b16 a[220:221], v195 offset:57856\n"
"v_cvt_pk_bf16_f32 v160, v224, v225\n"
"v_mfma_f32_16x16x32_bf16 a[52:55], a[224:227], v[148:151], a[52:55]\n"
"v_cvt_pk_bf16_f32 v161, v226, v227\n"
"ds_read_b64_tr_b16 a[222:223], v195 offset:59904\n"
"v_mfma_f32_16x16x32_bf16 a[48:51], a[224:227], v[144:147], a[48:51]\n"
"v_exp_f32 v222, v130\n"
"v_mfma_f32_16x16x32_bf16 a[60:63], a[228:231], v[144:147], a[60:63]\n"
"v_exp_f32 v223, v131\n"
"s_nop 7\n"
"v_mfma_f32_16x16x32_bf16 a[72:75], a[232:235], v[144:147], a[72:75]\n"
"v_exp_f32 v224, v132\n"
"v_mfma_f32_16x16x32_bf16 a[84:87], a[236:239], v[144:147], a[84:87]\n"
"s_waitcnt lgkmcnt(6)\n"
"v_exp_f32 v225, v133\n"
"v_mfma_f32_16x16x32_bf16 a[96:99], a[208:211], v[144:147], a[96:99]\n"
"s_waitcnt lgkmcnt(4)\n"
"v_cvt_pk_bf16_f32 v164, v220, v221\n"
"v_cvt_pk_bf16_f32 v165, v222, v223\n"
"v_mfma_f32_16x16x32_bf16 a[108:111], a[212:215], v[144:147], a[108:111]\n"
"s_waitcnt lgkmcnt(2)\n"
"buffer_load_dwordx4 v206, s[36:39], s63 offen lds\n"
"v_mfma_f32_16x16x32_bf16 a[120:123], a[216:219], v[144:147], a[120:123]\n"
"s_waitcnt lgkmcnt(0)\n"
"v_exp_f32 v226, v134\n"
"v_mfma_f32_16x16x32_bf16 a[132:135], a[220:223], v[144:147], a[132:135]\n"
"s_add_u32 s63, s79, 0\n"
"v_exp_f32 v227, v135\n"
"v_mfma_f32_16x16x32_bf16 a[136:139], a[220:223], v[148:151], a[136:139]\n"
"s_add_u32 m0, s60, 49152\n"
"v_exp_f32 v220, v136\n"
"s_nop 7\n"
"v_mfma_f32_16x16x32_bf16 a[124:127], a[216:219], v[148:151], a[124:127]\n"
"v_exp_f32 v221, v137\n"
"v_mfma_f32_16x16x32_bf16 a[112:115], a[212:215], v[148:151], a[112:115]\n"
"s_waitcnt vmcnt(16)\n"
"v_cvt_pk_bf16_f32 v158, v224, v225\n"
"v_cvt_pk_bf16_f32 v159, v226, v227\n"
"v_mfma_f32_16x16x32_bf16 a[100:103], a[208:211], v[148:151], a[100:103]\n"
"s_barrier\n"
"ds_read_b64_tr_b16 a[224:225], v194 offset:61440\n"
"ds_read_b64_tr_b16 a[226:227], v194 offset:63488\n"
"v_mfma_f32_16x16x32_bf16 a[104:107], a[208:211], v[152:155], a[104:107]\n"
"ds_read_b64_tr_b16 a[228:229], v195 offset:61440\n"
"ds_read_b64_tr_b16 a[230:231], v195 offset:63488\n"
"v_mfma_f32_16x16x32_bf16 a[116:119], a[212:215], v[152:155], a[116:119]\n"
"ds_read_b64_tr_b16 a[232:233], v194 offset:61952\n"
"ds_read_b64_tr_b16 a[234:235], v194 offset:64000\n"
"v_mfma_f32_16x16x32_bf16 a[128:131], a[216:219], v[152:155], a[128:131]\n"
"ds_read_b64_tr_b16 a[236:237], v195 offset:61952\n"
"ds_read_b64_tr_b16 a[238:239], v195 offset:64000\n"
"v_mfma_f32_16x16x32_bf16 a[140:143], a[220:223], v[152:155], a[140:143]\n"
"v_exp_f32 v222, v138\n"
"v_exp_f32 v223, v139\n"
"v_exp_f32 v224, v140\n"
"v_exp_f32 v225, v141\n"
"v_cvt_pk_bf16_f32 v162, v220, v221\n"
"v_cvt_pk_bf16_f32 v163, v222, v223\n"
"v_exp_f32 v226, v142\n"
"v_exp_f32 v227, v143\n"
"v_cvt_pk_bf16_f32 v166, v224, v225\n"
"v_cvt_pk_bf16_f32 v167, v226, v227\n"
"s_nop 7\n"
"v_mfma_f32_16x16x32_bf16 a[192:195], a[204:207], v[156:159], a[0:3]\n"
"buffer_load_dwordx4 v206, s[36:39], s63 offen lds\n"
"v_mfma_f32_16x16x32_bf16 a[196:199], a[204:207], v[160:163], a[4:7]\n"
"s_add_u32 s63, s79, 28672\n"
"s_add_u32 m0, s60, 53248\n"
"ds_read_b64_tr_b16 a[208:209], v196 offset:4096\n"
"v_mfma_f32_16x16x32_bf16 a[200:203], a[204:207], v[164:167], a[8:11]\n"
"s_waitcnt lgkmcnt(7)\n"
"buffer_load_dwordx4 v206, s[36:39], s63 offen lds\n"
"v_mfma_f32_16x16x32_bf16 a[152:155], a[224:227], v[152:155], a[152:155]\n"
"s_waitcnt lgkmcnt(5)\n"
"ds_read_b64_tr_b16 a[210:211], v196 offset:6144\n"
"ds_read_b64_tr_b16 a[212:213], v197 offset:4096\n"
"v_mfma_f32_16x16x32_bf16 a[164:167], a[228:231], v[152:155], a[164:167]\n"
"s_waitcnt lgkmcnt(5)\n"
"s_add_u32 s63, s79, 57344\n"
"s_add_u32 m0, s60, 57344\n"
"v_mfma_f32_16x16x32_bf16 a[176:179], a[232:235], v[152:155], a[176:179]\n"
"s_waitcnt lgkmcnt(3)\n"
"ds_read_b64_tr_b16 a[214:215], v197 offset:6144\n"
"ds_read_b64_tr_b16 a[216:217], v196 offset:4608\n"
"v_mfma_f32_16x16x32_bf16 a[188:191], a[236:239], v[152:155], a[188:191]\n"
"buffer_load_dwordx4 v206, s[36:39], s63 offen lds\n"
"s_nop 7\n"
"v_mfma_f32_16x16x32_bf16 a[184:187], a[236:239], v[148:151], a[184:187]\n"
"s_add_u32 s63, s79, 86016\n"
"ds_read_b64_tr_b16 a[218:219], v196 offset:6656\n"
"ds_read_b64_tr_b16 a[220:221], v197 offset:4608\n"
"v_mfma_f32_16x16x32_bf16 a[172:175], a[232:235], v[148:151], a[172:175]\n"
"s_add_u32 m0, s60, 61440\n"
"v_accvgpr_read_b32 v220, a192\n"
"v_accvgpr_read_b32 v221, a196\n"
"v_mfma_f32_16x16x32_bf16 a[160:163], a[228:231], v[148:151], a[160:163]\n"
"ds_read_b64_tr_b16 a[222:223], v197 offset:6656\n"
"v_accvgpr_read_b32 v222, a200\n"
"v_mfma_f32_16x16x32_bf16 a[148:151], a[224:227], v[148:151], a[148:151]\n"
"v_add_f32 v220, v220, v221\n"
"v_add_f32 v220, v220, v222\n"
"v_mfma_f32_16x16x32_bf16 a[144:147], a[224:227], v[144:147], a[144:147]\n"
"buffer_load_dwordx4 v206, s[36:39], s63 offen lds\n"
"v_mfma_f32_16x16x32_bf16 a[156:159], a[228:231], v[144:147], a[156:159]\n"
"s_add_u32 s63, s79, 114688\n"
"v_cmp_ngt_f32 vcc, 0x47800000, v220\n"
"ds_read_b64_tr_b16 a[224:225], v196 offset:0\n"
"v_mfma_f32_16x16x32_bf16 a[168:171], a[232:235], v[144:147], a[168:171]\n"
"s_add_u32 m0, s60, 65536\n"
"ds_read_b64_tr_b16 a[226:227], v196 offset:2048\n"
"ds_read_b64_tr_b16 a[228:229], v197 offset:0\n"
"s_nop 7\n"
"v_mfma_f32_16x16x32_bf16 a[180:183], a[236:239], v[144:147], a[180:183]\n"
"s_waitcnt lgkmcnt(9)\n"
"s_cbranch_vccnz Lstub15\n"
"Lback15:\n"
"ds_read_b64_tr_b16 a[230:231], v197 offset:2048\n"
"v_mfma_f32_16x16x32_bf16 a[48:51], a[208:211], v[156:159], a[48:51]\n"
"s_waitcnt lgkmcnt(8)\n"
"buffer_load_dwordx4 v206, s[36:39], s63 offen lds\n"
"v_mfma_f32_16x16x32_bf16 a[60:63], a[212:215], v[156:159], a[60:63]\n"
"ds_read_b64_tr_b16 a[232:233], v196 offset:512\n"
"ds_read_b64_tr_b16 a[234:235], v196 offset:2560\n"
"v_mfma_f32_16x16x32_bf16 a[64:67], a[212:215], v[160:163], a[64:67]\n"
"ds_read_b64_tr_b16 a[236:237], v197 offset:512\n"
"ds_read_b64_tr_b16 a[238:239], v197 offset:2560\n"
"v_mfma_f32_16x16x32_bf16 a[52:55], a[208:211], v[160:163], a[52:55]\n"
"v_accvgpr_read_b32 v208, a192\n"
"v_accvgpr_read_b32 v209, a196\n"
"v_mfma_f32_16x16x32_bf16 a[56:59], a[208:211], v[164:167], a[56:59]\n"
"s_add_u32 s63, s79, 143360\n"
"v_accvgpr_read_b32 v210, a200\n"
"ds_read_b64_tr_b16 a[208:209], v196 offset:8192\n"
"v_mfma_f32_16x16x32_bf16 a[68:71], a[212:215], v[164:167], a[68:71]\n"
"s_waitcnt lgkmcnt(11)\n"
"v_rcp_f32 v208, v208\n"
"v_mfma_f32_16x16x32_bf16 a[80:83], a[216:219], v[164:167], a[80:83]\n"
"s_waitcnt lgkmcnt(9)\n"
"s_add_u32 m0, s60, 69632\n"
"ds_read_b64_tr_b16 a[210:211], v196 offset:10240\n"
"v_mfma_f32_16x16x32_bf16 a[92:95], a[220:223], v[164:167], a[92:95]\n"
"v_rcp_f32 v209, v209\n"
"v_mfma_f32_16x16x32_bf16 a[88:91], a[220:223], v[160:163], a[88:91]\n"
"buffer_load_dwordx4 v206, s[36:39], s63 offen lds\n"
"v_mfma_f32_16x16x32_bf16 a[76:79], a[216:219], v[160:163], a[76:79]\n"
"v_rcp_f32 v210, v210\n"
"v_mfma_f32_16x16x32_bf16 a[72:75], a[216:219], v[156:159], a[72:75]\n"
"v_accvgpr_read_b32 v223, a48\n"
"ds_read_b64_tr_b16 a[212:213], v197 offset:8192\n"
"v_mfma_f32_16x16x32_bf16 a[84:87], a[220:223], v[156:159], a[84:87]\n"
"s_waitcnt lgkmcnt(9)\n"
"v_accvgpr_read_b32 v224, a49\n"
"v_accvgpr_read_b32 v225, a50\n"
"v_mfma_f32_16x16x32_bf16 v[168:171], a[224:227], v[156:159], v[168:171]\n"
"s_waitcnt lgkmcnt(7)\n"
"ds_read_b64_tr_b16 a[214:215], v197 offset:10240\n"
"v_accvgpr_read_b32 v226, a51\n"
"v_mfma_f32_16x16x32_bf16 v[180:183], a[228:231], v[156:159], v[180:183]\n"
"s_add_u32 s63, s79, 172032\n"
"v_cvt_pk_bf16_f32 v24, v223, v224\n"
"v_accvgpr_read_b32 v227, a60\n"
"v_mfma_f32_16x16x32_bf16 v[184:187], a[228:231], v[160:163], v[184:187]\n"
"s_add_u32 m0, s60, 73728\n"
"v_accvgpr_read_b32 v220, a61\n"
"v_cvt_pk_bf16_f32 v25, v225, v226\n"
"v_mfma_f32_16x16x32_bf16 v[172:175], a[224:227], v[160:163], v[172:175]\n"
"v_accvgpr_read_b32 v221, a62\n"
"v_accvgpr_read_b32 v222, a63\n"
"v_mfma_f32_16x16x32_bf16 v[176:179], a[224:227], v[164:167], v[176:179]\n"
"v_cvt_pk_bf16_f32 v26, v227, v220\n"
"ds_read_b64_tr_b16 a[216:217], v196 offset:8704\n"
"v_mfma_f32_16x16x32_bf16 v[188:191], a[228:231], v[164:167], v[188:191]\n"
"s_waitcnt lgkmcnt(7)\n"
"v_accvgpr_read_b32 v223, a52\n"
"ds_read_b64_tr_b16 a[218:219], v196 offset:10752\n"
"v_mfma_f32_16x16x32_bf16 v[236:239], a[232:235], v[164:167], v[236:239]\n"
"s_waitcnt lgkmcnt(6)\n"
"v_accvgpr_read_b32 v224, a53\n"
"ds_read_b64_tr_b16 a[220:221], v197 offset:8704\n"
"v_mfma_f32_16x16x32_bf16 v[248:251], a[236:239], v[164:167], v[248:251]\n"
"buffer_load_dwordx4 v206, s[36:39], s63 offen lds\n"
"v_mfma_f32_16x16x32_bf16 v[244:247], a[236:239], v[160:163], v[244:247]\n"
"s_add_u32 s63, s79, 200704\n"
"v_cvt_pk_bf16_f32 v27, v221, v222\n"
"v_accvgpr_read_b32 v225, a54\n"
"v_mfma_f32_16x16x32_bf16 v[232:235], a[232:235], v[160:163], v[232:235]\n"
"s_add_u32 m0, s60, 77824\n"
"ds_read_b64_tr_b16 a[222:223], v197 offset:10752\n"
"v_accvgpr_read_b32 v226, a55\n"
"v_mfma_f32_16x16x32_bf16 v[228:231], a[232:235], v[156:159], v[228:231]\n"
"v_cvt_pk_bf16_f32 v28, v223, v224\n"
"v_accvgpr_read_b32 v227, a64\n"
"v_mfma_f32_16x16x32_bf16 v[240:243], a[236:239], v[156:159], v[240:243]\n"
"s_waitcnt lgkmcnt(6)\n"
"v_accvgpr_read_b32 v220, a65\n"
"v_cvt_pk_bf16_f32 v29, v225, v226\n"
"v_mfma_f32_16x16x32_bf16 a[96:99], a[208:211], v[156:159], a[96:99]\n"
"s_waitcnt lgkmcnt(4)\n"
"v_accvgpr_read_b32 v221, a66\n"
"v_accvgpr_read_b32 v222, a67\n"
"v_mfma_f32_16x16x32_bf16 a[108:111], a[212:215], v[156:159], a[108:111]\n"
"v_cvt_pk_bf16_f32 v30, v227, v220\n"
"ds_read_b64_tr_b16 a[224:225], v196 offset:12288\n"
"v_mfma_f32_16x16x32_bf16 a[112:115], a[212:215], v[160:163], a[112:115]\n"
"v_accvgpr_read_b32 v223, a56\n"
"ds_read_b64_tr_b16 a[226:227], v196 offset:14336\n"
"v_mfma_f32_16x16x32_bf16 a[100:103], a[208:211], v[160:163], a[100:103]\n"
"v_accvgpr_read_b32 v224, a57\n"
"ds_read_b64_tr_b16 a[228:229], v197 offset:12288\n"
"v_mfma_f32_16x16x32_bf16 a[104:107], a[208:211], v[164:167], a[104:107]\n"
"v_cvt_pk_bf16_f32 v31, v221, v222\n"
"v_accvgpr_read_b32 v225, a58\n"
"v_mfma_f32_16x16x32_bf16 a[116:119], a[212:215], v[164:167], a[116:119]\n"
"s_waitcnt lgkmcnt(5)\n"
"buffer_load_dwordx4 v206, s[36:39], s63 offen lds\n"
"v_mfma_f32_16x16x32_bf16 a[128:131], a[216:219], v[164:167], a[128:131]\n"
"s_waitcnt lgkmcnt(3)\n"
"ds_read_b64_tr_b16 a[230:231], v197 offset:14336\n"
"v_accvgpr_read_b32 v226, a59\n"
"v_mfma_f32_16x16x32_bf16 a[140:143], a[220:223], v[164:167], a[140:143]\n"
"s_add_u32 s63, s80, 0\n"
"v_cvt_pk_bf16_f32 v32, v223, v224\n"
"v_accvgpr_read_b32 v227, a68\n"
"v_mfma_f32_16x16x32_bf16 a[136:139], a[220:223], v[160:163], a[136:139]\n"
"s_add_u32 m0, s60, 81920\n"
"v_accvgpr_read_b32 v220, a69\n"
"v_cvt_pk_bf16_f32 v33, v225, v226\n"
"v_mfma_f32_16x16x32_bf16 a[124:127], a[216:219], v[160:163], a[124:127]\n"
"s_waitcnt vmcnt(20)\n"
"v_accvgpr_read_b32 v221, a70\n"
"v_accvgpr_read_b32 v222, a71\n"
"v_mfma_f32_16x16x32_bf16 a[120:123], a[216:219], v[156:159], a[120:123]\n"
"s_barrier\n"
"ds_read_b64_tr_b16 a[232:233], v196 offset:12800\n"
"v_cvt_pk_bf16_f32 v34, v227, v220\n"
"v_mfma_f32_16x16x32_bf16 a[132:135], a[220:223], v[156:159], a[132:135]\n"
"s_waitcnt lgkmcnt(3)\n"
"ds_read_b64_tr_b16 a[234:235], v196 offset:14848\n"
"ds_read_b64_tr_b16 a[236:237], v197 offset:12800\n"
"v_mfma_f32_16x16x32_bf16 a[144:147], a[224:227], v[156:159], a[144:147]\n"
"s_waitcnt lgkmcnt(3)\n"
"ds_read_b64_tr_b16 a[238:239], v197 offset:14848\n"
"v_accvgpr_read_b32 v223, a72\n"
"v_mfma_f32_16x16x32_bf16 a[156:159], a[228:231], v[156:159], a[156:159]\n"
"v_accvgpr_read_b32 v224, a73\n"
"v_cvt_pk_bf16_f32 v35, v221, v222\n"
"v_mfma_f32_16x16x32_bf16 a[160:163], a[228:231], v[160:163], a[160:163]\n"
"v_accvgpr_read_b32 v225, a74\n"
"v_accvgpr_read_b32 v226, a75\n"
"v_mfma_f32_16x16x32_bf16 a[148:151], a[224:227], v[160:163], a[148:151]\n"
"v_cvt_pk_bf16_f32 v36, v223, v224\n"
"v_accvgpr_read_b32 v227, a84\n"
"v_mfma_f32_16x16x32_bf16 a[152:155], a[224:227], v[164:167], a[152:155]\n"
"ds_read_b64_tr_b16 a[208:209], v196 offset:24576\n"
"v_accvgpr_read_b32 v220, a85\n"
"v_mfma_f32_16x16x32_bf16 a[164:167], a[228:231], v[164:167], a[164:167]\n"
"s_waitcnt lgkmcnt(3)\n"
"ds_read_b64_tr_b16 a[210:211], v196 offset:26624\n"
"v_cvt_pk_bf16_f32 v37, v225, v226\n"
"v_mfma_f32_16x16x32_bf16 a[176:179], a[232:235], v[164:167], a[176:179]\n"
"s_waitcnt lgkmcnt(2)\n"
"buffer_load_dwordx4 v206, s[36:39], s63 offen lds\n"
"v_mfma_f32_16x16x32_bf16 a[188:191], a[236:239], v[164:167], a[188:191]\n"
"s_add_u32 s63, s80, 28672\n"
"ds_read_b64_tr_b16 a[212:213], v197 offset:24576\n"
"v_accvgpr_read_b32 v221, a86\n"
"v_mfma_f32_16x16x32_bf16 a[184:187], a[236:239], v[160:163], a[184:187]\n"
"s_add_u32 m0, s60, 86016\n"
"ds_read_b64_tr_b16 a[214:215], v197 offset:26624\n"
"v_accvgpr_read_b32 v222, a87\n"
"v_mfma_f32_16x16x32_bf16 a[172:175], a[232:235], v[160:163], a[172:175]\n"
"v_cvt_pk_bf16_f32 v38, v227, v220\n"
"v_accvgpr_read_b32 v223, a76\n"
"v_mfma_f32_16x16x32_bf16 a[168:171], a[232:235], v[156:159], a[168:171]\n"
"ds_read_b64_tr_b16 a[216:217], v196 offset:25088\n"
"v_accvgpr_read_b32 v224, a77\n"
"v_mfma_f32_16x16x32_bf16 a[180:183], a[236:239], v[156:159], a[180:183]\n"
"s_waitcnt lgkmcnt(3)\n"
"ds_read_b64_tr_b16 a[218:219], v196 offset:27136\n"
"v_cvt_pk_bf16_f32 v39, v221, v222\n"
"v_mfma_f32_16x16x32_bf16 v[96:99], a[208:211], v[24:27], 0\n"
"v_accvgpr_read_b32 v225, a78\n"
"v_accvgpr_read_b32 v226, a79\n"
"v_mfma_f32_16x16x32_bf16 v[100:103], a[208:211], v[28:31], 0\n"
"ds_read_b64_tr_b16 a[220:221], v197 offset:25088\n"
"v_cvt_pk_bf16_f32 v40, v223, v224\n"
"v_mfma_f32_16x16x32_bf16 v[104:107], a[208:211], v[32:35], 0\n"
"s_waitcnt lgkmcnt(3)\n"
"ds_read_b64_tr_b16 a[222:223], v197 offset:27136\n"
"v_accvgpr_read_b32 v227, a88\n"
"v_mfma_f32_16x16x32_bf16 v[108:111], a[212:215], v[24:27], 0\n"
"v_accvgpr_read_b32 v220, a89\n"
"v_cvt_pk_bf16_f32 v41, v225, v226\n"
"v_mfma_f32_16x16x32_bf16 v[112:115], a[212:215], v[28:31], 0\n"
"ds_read_b64_tr_b16 a[224:225], v196 offset:28672\n"
"v_accvgpr_read_b32 v221, a90\n"
"v_mfma_f32_16x16x32_bf16 v[116:119], a[212:215], v[32:35], 0\n"
"s_waitcnt lgkmcnt(3)\n"
"ds_read_b64_tr_b16 a[226:227], v196 offset:30720\n"
"v_accvgpr_read_b32 v222, a91\n"
"v_mfma_f32_16x16x32_bf16 v[120:123], a[216:219], v[24:27], 0\n"
"buffer_load_dwordx4 v206, s[36:39], s63 offen lds\n"
"v_mfma_f32_16x16x32_bf16 v[124:127], a[216:219], v[28:31], 0\n"
"s_add_u32 s63, s80, 57344\n"
"ds_read_b64_tr_b16 a[228:229], v197 offset:28672\n"
"v_cvt_pk_bf16_f32 v42, v227, v220\n"
"v_mfma_f32_16x16x32_bf16 v[128:131], a[216:219], v[32:35], 0\n"
"s_waitcnt lgkmcnt(3)\n"
"ds_read_b64_tr_b16 a[230:231], v197 offset:30720\n"
"v_accvgpr_read_b32 v223, a80\n"
"v_mfma_f32_16x16x32_bf16 v[132:135], a[220:223], v[24:27], 0\n"
"s_add_u32 m0, s60, 90112\n"
"v_accvgpr_read_b32 v224, a81\n"
"v_cvt_pk_bf16_f32 v43, v221, v222\n"
"v_mfma_f32_16x16x32_bf16 v[136:139], a[220:223], v[28:31], 0\n"
"ds_read_b64_tr_b16 a[232:233], v196 offset:29184\n"
"v_accvgpr_read_b32 v225, a82\n"
"v_mfma_f32_16x16x32_bf16 v[140:143], a[220:223], v[32:35], 0\n"
"s_waitcnt lgkmcnt(3)\n"
"ds_read_b64_tr_b16 a[234:235], v196 offset:31232\n"
"v_accvgpr_read_b32 v226, a83\n"
"v_mfma_f32_16x16x32_bf16 v[96:99], a[224:227], v[36:39], v[96:99]\n"
"v_cvt_pk_bf16_f32 v44, v223, v224\n"
"v_accvgpr_read_b32 v227, a92\n"
"v_mfma_f32_16x16x32_bf16 v[100:103], a[224:227], v[40:43], v[100:103]\n"
"v_accvgpr_read_b32 v220, a93\n"
"v_cvt_pk_bf16_f32 v45, v225, v226\n"
"v_accvgpr_read_b32 v221, a94\n"
"v_accvgpr_read_b32 v222, a95\n"
"v_cvt_pk_bf16_f32 v46, v227, v220\n"
"v_cvt_pk_bf16_f32 v47, v221, v222\n"
"ds_read_b64_tr_b16 a[236:237], v197 offset:29184\n"
"s_nop 0\n"
"v_mfma_f32_16x16x32_bf16 v[104:107], a[224:227], v[44:47], v[104:107]\n"
"s_waitcnt lgkmcnt(3)\n"
"ds_read_b64_tr_b16 a[238:239], v197 offset:31232\n"
"v_cvt_pk_bf16_f32 v0, v168, v169\n"
"v_mfma_f32_16x16x32_bf16 v[108:111], a[228:231], v[36:39], v[108:111]\n"
"buffer_load_dwordx4 v206, s[36:39], s63 offen lds\n"
"v_mfma_f32_16x16x32_bf16 v[112:115], a[228:231], v[40:43], v[112:115]\n"
"s_add_u32 s63, s80, 86016\n"
"ds_read_b64_tr_b16 a[208:209], v196 offset:16384\n"
"v_cvt_pk_bf16_f32 v1, v170, v171\n"
"v_mfma_f32_16x16x32_bf16 v[116:119], a[228:231], v[44:47], v[116:119]\n"
"s_waitcnt lgkmcnt(3)\n"
"ds_read_b64_tr_b16 a[210:211], v196 offset:18432\n"
"v_cvt_pk_bf16_f32 v2, v180, v181\n"
"v_mfma_f32_16x16x32_bf16 v[120:123], a[232:235], v[36:39], v[120:123]\n"
"s_add_u32 m0, s60, 94208\n"
"v_cvt_pk_bf16_f32 v3, v182, v183\n"
"v_cvt_pk_bf16_f32 v4, v172, v173\n"
"v_mfma_f32_16x16x32_bf16 v[124:127], a[232:235], v[40:43], v[124:127]\n"
"ds_read_b64_tr_b16 a[212:213], v197 offset:16384\n"
"v_cvt_pk_bf16_f32 v5, v174, v175\n"
"v_mfma_f32_16x16x32_bf16 v[128:131], a[232:235], v[44:47], v[128:131]\n"
"s_waitcnt lgkmcnt(3)\n"
"ds_read_b64_tr_b16 a[214:215], v197 offset:18432\n"
"v_cvt_pk_bf16_f32 v6, v184, v185\n"
"v_mfma_f32_16x16x32_bf16 v[132:135], a[236:239], v[36:39], v[132:135]\n"
"v_cvt_pk_bf16_f32 v7, v186, v187\n"
"v_cvt_pk_bf16_f32 v8, v176, v177\n"
"v_mfma_f32_16x16x32_bf16 v[136:139], a[236:239], v[40:43], v[136:139]\n"
"ds_read_b64_tr_b16 a[216:217], v196 offset:16896\n"
"v_cvt_pk_bf16_f32 v9, v178, v179\n"
"v_mfma_f32_16x16x32_bf16 v[140:143], a[236:239], v[44:47], v[140:143]\n"
"s_waitcnt lgkmcnt(3)\n"
"ds_read_b64_tr_b16 a[218:219], v196 offset:18944\n"
"v_cvt_pk_bf16_f32 v10, v188, v189\n"
"v_mfma_f32_16x16x32_bf16 v[96:99], a[208:211], v[0:3], v[96:99]\n"
"buffer_load_dwordx4 v206, s[36:39], s63 offen lds\n"
"v_mfma_f32_16x16x32_bf16 v[100:103], a[208:211], v[4:7], v[100:103]\n"
"s_add_u32 s63, s80, 114688\n"
"v_cvt_pk_bf16_f32 v11, v190, v191\n"
"ds_read_b64_tr_b16 a[220:221], v197 offset:16896\n"
"s_nop 0\n"
"v_mfma_f32_16x16x32_bf16 v[104:107], a[208:211], v[8:11], v[104:107]\n"
"s_waitcnt lgkmcnt(3)\n"
"ds_read_b64_tr_b16 a[222:223], v197 offset:18944\n"
"v_cvt_pk_bf16_f32 v12, v228, v229\n"
"v_mfma_f32_16x16x32_bf16 v[108:111], a[212:215], v[0:3], v[108:111]\n"
"s_add_u32 m0, s60, 98304\n"
"v_cvt_pk_bf16_f32 v13, v230, v231\n"
"v_cvt_pk_bf16_f32 v14, v240, v241\n"
"v_mfma_f32_16x16x32_bf16 v[112:115], a[212:215], v[4:7], v[112:115]\n"
"ds_read_b64_tr_b16 a[224:225], v196 offset:20480\n"
"v_cvt_pk_bf16_f32 v15, v242, v243\n"
"v_mfma_f32_16x16x32_bf16 v[116:119], a[212:215], v[8:11], v[116:119]\n"
"s_waitcnt lgkmcnt(3)\n"
"ds_read_b64_tr_b16 a[226:227], v196 offset:22528\n"
"v_cvt_pk_bf16_f32 v16, v232, v233\n"
"v_mfma_f32_16x16x32_bf16 v[120:123], a[216:219], v[0:3], v[120:123]\n"
"s_waitcnt vmcnt(20)\n"
"v_cvt_pk_bf16_f32 v17, v234, v235\n"
"v_cvt_pk_bf16_f32 v18, v244, v245\n"
"v_mfma_f32_16x16x32_bf16 v[124:127], a[216:219], v[4:7], v[124:127]\n"
"s_barrier\n"
"ds_read_b64_tr_b16 a[228:229], v197 offset:20480\n"
"v_cvt_pk_bf16_f32 v19, v246, v247\n"
"v_mfma_f32_16x16x32_bf16 v[128:131], a[216:219], v[8:11], v[128:131]\n"
"s_waitcnt lgkmcnt(3)\n"
"ds_read_b64_tr_b16 a[230:231], v197 offset:22528\n"
"v_cvt_pk_bf16_f32 v20, v236, v237\n"
"v_mfma_f32_16x16x32_bf16 v[132:135], a[220:223], v[0:3], v[132:135]\n"
"v_cvt_pk_bf16_f32 v21, v238, v239\n"
"v_cvt_pk_bf16_f32 v22, v248, v249\n"
"v_mfma_f32_16x16x32_bf16 v[136:139], a[220:223], v[4:7], v[136:139]\n"
"ds_read_b64_tr_b16 a[232:233], v196 offset:20992\n"
"v_accvgpr_read_b32 v223, a96\n"
"v_mfma_f32_16x16x32_bf16 v[140:143], a[220:223], v[8:11], v[140:143]\n"
"s_waitcnt lgkmcnt(3)\n"
"ds_read_b64_tr_b16 a[234:235], v196 offset:23040\n"
"v_accvgpr_read_b32 v224, a97\n"
"v_mfma_f32_16x16x32_bf16 v[96:99], a[224:227], v[12:15], v[96:99]\n"
"buffer_load_dwordx4 v206, s[36:39], s63 offen lds\n"
"v_mfma_f32_16x16x32_bf16 v[100:103], a[224:227], v[16:19], v[100:103]\n"
"s_add_u32 s63, s80, 143360\n"
"v_cvt_pk_bf16_f32 v23, v250, v251\n"
"ds_read_b64_tr_b16 a[236:237], v197 offset:20992\n"
"s_nop 0\n"
"v_mfma_f32_16x16x32_bf16 v[104:107], a[224:227], v[20:23], v[104:107]\n"
"s_waitcnt lgkmcnt(3)\n"
"ds_read_b64_tr_b16 a[238:239], v197 offset:23040\n"
"v_accvgpr_read_b32 v225, a98\n"
"v_mfma_f32_16x16x32_bf16 v[108:111], a[228:231], v[12:15], v[108:111]\n"
"s_add_u32 m0, s60, 102400\n"
"v_accvgpr_read_b32 v226, a99\n"
"v_cvt_pk_bf16_f32 v48, v223, v224\n"
"v_mfma_f32_16x16x32_bf16 v[112:115], a[228:231], v[16:19], v[112:115]\n"
"ds_read_b64_tr_b16 a[208:209], v192 offset:0\n"
"v_accvgpr_read_b32 v227, a108\n"
"v_mfma_f32_16x16x32_bf16 v[116:119], a[228:231], v[20:23], v[116:119]\n"
"s_waitcnt lgkmcnt(3)\n"
"ds_read_b64_tr_b16 a[210:211], v192 offset:2048\n"
"v_accvgpr_read_b32 v220, a109\n"
"v_mfma_f32_16x16x32_bf16 v[120:123], a[232:235], v[12:15], v[120:123]\n"
"v_cvt_pk_bf16_f32 v49, v225, v226\n"
"v_accvgpr_read_b32 v221, a110\n"
"v_mfma_f32_16x16x32_bf16 v[124:127], a[232:235], v[16:19], v[124:127]\n"
"ds_read_b64_tr_b16 a[212:213], v193 offset:0\n"
"v_accvgpr_read_b32 v222, a111\n"
"v_mfma_f32_16x16x32_bf16 v[128:131], a[232:235], v[20:23], v[128:131]\n"
"s_waitcnt lgkmcnt(3)\n"
"ds_read_b64_tr_b16 a[214:215], v193 offset:2048\n"
"v_cvt_pk_bf16_f32 v50, v227, v220\n"
"v_mfma_f32_16x16x32_bf16 v[132:135], a[236:239], v[12:15], v[132:135]\n"
"v_accvgpr_read_b32 v223, a100\n"
"v_accvgpr_read_b32 v224, a101\n"
"v_mfma_f32_16x16x32_bf16 v[136:139], a[236:239], v[16:19], v[136:139]\n"
"ds_read_b64_tr_b16 a[216:217], v192 offset:512\n"
"v_cvt_pk_bf16_f32 v51, v221, v222\n"
"v_mfma_f32_16x16x32_bf16 v[140:143], a[236:239], v[20:23], v[140:143]\n"
"s_waitcnt lgkmcnt(3)\n"
"ds_read_b64_tr_b16 a[218:219], v192 offset:2560\n"
"v_accvgpr_read_b32 v225, a102\n"
"v_mfma_f32_16x16x32_bf16 v[96:99], a[208:211], v[48:51], v[96:99]\n"
"v_accvgpr_read_b32 v226, a103\n"
"v_cvt_pk_bf16_f32 v52, v223, v224\n"
"v_accvgpr_read_b32 v227, a112\n"
"v_accvgpr_read_b32 v220, a113\n"
"v_cvt_pk_bf16_f32 v53, v225, v226\n"
"v_accvgpr_read_b32 v221, a114\n"
"v_accvgpr_read_b32 v222, a115\n"
"v_cvt_pk_bf16_f32 v54, v227, v220\n"
"v_accvgpr_read_b32 v223, a104\n"
"v_accvgpr_read_b32 v224, a105\n"
"v_cvt_pk_bf16_f32 v55, v221, v222\n"
"s_nop 1\n"
"v_mfma_f32_16x16x32_bf16 v[100:103], a[208:211], v[52:55], v[100:103]\n"
"v_accvgpr_read_b32 v225, a106\n"
"v_accvgpr_read_b32 v226, a107\n"
"v_cvt_pk_bf16_f32 v56, v223, v224\n"
"v_accvgpr_read_b32 v227, a116\n"
"v_accvgpr_read_b32 v220, a117\n"
"v_cvt_pk_bf16_f32 v57, v225, v226\n"
"v_accvgpr_read_b32 v221, a118\n"
"v_accvgpr_read_b32 v222, a119\n"
"v_cvt_pk_bf16_f32 v58, v227, v220\n"
"v_accvgpr_read_b32 v223, a120\n"
"v_accvgpr_read_b32 v224, a121\n"
"v_cvt_pk_bf16_f32 v59, v221, v222\n"
"ds_read_b64_tr_b16 a[220:221], v193 offset:512\n"
"s_nop 0\n"
"v_mfma_f32_16x16x32_bf16 v[104:107], a[208:211], v[56:59], v[104:107]\n"
"s_waitcnt lgkmcnt(3)\n"
"ds_read_b64_tr_b16 a[222:223], v193 offset:2560\n"
"v_accvgpr_read_b32 v225, a122\n"
"v_mfma_f32_16x16x32_bf16 v[108:111], a[212:215], v[48:51], v[108:111]\n"
"buffer_load_dwordx4 v206, s[36:39], s63 offen lds\n"
"v_mfma_f32_16x16x32_bf16 v[112:115], a[212:215], v[52:55], v[112:115]\n"
"s_add_u32 s63, s80, 172032\n"
"ds_read_b64_tr_b16 a[224:225], v192 offset:4096\n"
"s_add_u32 m0, s60, 106496\n"
"v_mfma_f32_16x16x32_bf16 v[116:119], a[212:215], v[56:59], v[116:119]\n"
"s_waitcnt lgkmcnt(3)\n"
"ds_read_b64_tr_b16 a[226:227], v192 offset:6144\n"
"v_accvgpr_read_b32 v226, a123\n"
"v_mfma_f32_16x16x32_bf16 v[120:123], a[216:219], v[48:51], v[120:123]\n"
"buffer_load_dwordx4 v206, s[36:39], s63 offen lds\n"
"v_mfma_f32_16x16x32_bf16 v[124:127], a[216:219], v[52:55], v[124:127]\n"
"s_add_u32 s63, s80, 200704\n"
"ds_read_b64_tr_b16 a[228:229], v193 offset:4096\n"
"s_add_u32 m0, s60, 110592\n"
"v_mfma_f32_16x16x32_bf16 v[128:131], a[216:219], v[56:59], v[128:131]\n"
"s_waitcnt lgkmcnt(3)\n"
"ds_read_b64_tr_b16 a[230:231], v193 offset:6144\n"
"v_cvt_pk_bf16_f32 v60, v223, v224\n"
"v_mfma_f32_16x16x32_bf16 v[132:135], a[220:223], v[48:51], v[132:135]\n"
"v_accvgpr_read_b32 v227, a132\n"
"v_accvgpr_read_b32 v220, a133\n"
"v_mfma_f32_16x16x32_bf16 v[136:139], a[220:223], v[52:55], v[136:139]\n"
"ds_read_b64_tr_b16 a[232:233], v192 offset:4608\n"
"v_cvt_pk_bf16_f32 v61, v225, v226\n"
"v_mfma_f32_16x16x32_bf16 v[140:143], a[220:223], v[56:59], v[140:143]\n"
"s_waitcnt lgkmcnt(3)\n"
"v_accvgpr_read_b32 v221, a134\n"
"v_accvgpr_read_b32 v222, a135\n"
"v_cvt_pk_bf16_f32 v62, v227, v220\n"
"v_accvgpr_read_b32 v223, a124\n"
"v_accvgpr_read_b32 v224, a125\n"
"v_cvt_pk_bf16_f32 v63, v221, v222\n"
"ds_read_b64_tr_b16 a[234:235], v192 offset:6656\n"
"s_nop 0\n"
"v_mfma_f32_16x16x32_bf16 v[96:99], a[224:227], v[60:63], v[96:99]\n"
"v_accvgpr_read_b32 v225, a126\n"
"v_accvgpr_read_b32 v226, a127\n"
"v_cvt_pk_bf16_f32 v64, v223, v224\n"
"v_accvgpr_read_b32 v227, a136\n"
"v_accvgpr_read_b32 v220, a137\n"
"v_cvt_pk_bf16_f32 v65, v225, v226\n"
"v_accvgpr_read_b32 v221, a138\n"
"v_accvgpr_read_b32 v222, a139\n"
"v_cvt_pk_bf16_f32 v66, v227, v220\n"
"v_accvgpr_read_b32 v223, a128\n"
"v_accvgpr_read_b32 v224, a129\n"
"v_cvt_pk_bf16_f32 v67, v221, v222\n"
"s_nop 1\n"
"v_mfma_f32_16x16x32_bf16 v[100:103], a[224:227], v[64:67], v[100:103]\n"
"v_accvgpr_read_b32 v225, a130\n"
"v_accvgpr_read_b32 v226, a131\n"
"v_cvt_pk_bf16_f32 v68, v223, v224\n"
"v_accvgpr_read_b32 v227, a140\n"
"v_accvgpr_read_b32 v220, a141\n"
"v_cvt_pk_bf16_f32 v69, v225, v226\n"
"v_accvgpr_read_b32 v221, a142\n"
"v_accvgpr_read_b32 v222, a143\n"
"v_cvt_pk_bf16_f32 v70, v227, v220\n"
"v_accvgpr_read_b32 v223, a144\n"
"v_accvgpr_read_b32 v224, a145\n"
"v_cvt_pk_bf16_f32 v71, v221, v222\n"
"ds_read_b64_tr_b16 a[236:237], v193 offset:4608\n"
"s_nop 0\n"
"v_mfma_f32_16x16x32_bf16 v[104:107], a[224:227], v[68:71], v[104:107]\n"
"s_waitcnt lgkmcnt(3)\n"
"ds_read_b64_tr_b16 a[238:239], v193 offset:6656\n"
"v_accvgpr_read_b32 v225, a146\n"
"v_mfma_f32_16x16x32_bf16 v[108:111], a[228:231], v[60:63], v[108:111]\n"
"buffer_load_dwordx4 v206, s[36:39], s63 offen lds\n"
"v_mfma_f32_16x16x32_bf16 v[112:115], a[228:231], v[64:67], v[112:115]\n"
"s_add_u32 s63, s81, 0\n"
"ds_read_b64_tr_b16 a[208:209], v192 offset:8192\n"
"s_add_u32 m0, s60, 114688\n"
"v_mfma_f32_16x16x32_bf16 v[116:119], a[228:231], v[68:71], v[116:119]\n"
"s_waitcnt lgkmcnt(3)\n"
"ds_read_b64_tr_b16 a[210:211], v192 offset:10240\n"
"v_accvgpr_read_b32 v226, a147\n"
"v_mfma_f32_16x16x32_bf16 v[120:123], a[232:235], v[60:63], v[120:123]\n"
"buffer_load_dwordx4 v206, s[36:39], s63 offen lds\n"
"v_mfma_f32_16x16x32_bf16 v[124:127], a[232:235], v[64:67], v[124:127]\n"
"s_add_u32 s63, s81, 28672\n"
"ds_read_b64_tr_b16 a[212:213], v193 offset:8192\n"
"s_add_u32 m0, s60, 118784\n"
"v_mfma_f32_16x16x32_bf16 v[128:131], a[232:235], v[68:71], v[128:131]\n"
"s_waitcnt lgkmcnt(3)\n"
"ds_read_b64_tr_b16 a[214:215], v193 offset:10240\n"
"v_cvt_pk_bf16_f32 v72, v223, v224\n"
"v_mfma_f32_16x16x32_bf16 v[132:135], a[236:239], v[60:63], v[132:135]\n"
"buffer_load_dwordx4 v206, s[36:39], s63 offen lds\n"
"v_mfma_f32_16x16x32_bf16 v[136:139], a[236:239], v[64:67], v[136:139]\n"
"s_add_u32 s63, s81, 57344\n"
"ds_read_b64_tr_b16 a[216:217], v192 offset:8704\n"
"v_accvgpr_read_b32 v227, a156\n"
"v_mfma_f32_16x16x32_bf16 v[140:143], a[236:239], v[68:71], v[140:143]\n"
"s_waitcnt lgkmcnt(3)\n"
"v_accvgpr_read_b32 v220, a157\n"
"v_cvt_pk_bf16_f32 v73, v225, v226\n"
"v_accvgpr_read_b32 v221, a158\n"
"v_accvgpr_read_b32 v222, a159\n"
"v_cvt_pk_bf16_f32 v74, v227, v220\n"
"v_accvgpr_read_b32 v223, a148\n"
"v_accvgpr_read_b32 v224, a149\n"
"v_cvt_pk_bf16_f32 v75, v221, v222\n"
"ds_read_b64_tr_b16 a[218:219], v192 offset:10752\n"
"s_nop 0\n"
"v_mfma_f32_16x16x32_bf16 v[96:99], a[208:211], v[72:75], v[96:99]\n"
"s_add_u32 m0, s60, 122880\n"
"v_accvgpr_read_b32 v225, a150\n"
"v_accvgpr_read_b32 v226, a151\n"
"v_cvt_pk_bf16_f32 v76, v223, v224\n"
"v_accvgpr_read_b32 v227, a160\n"
"v_accvgpr_read_b32 v220, a161\n"
"v_cvt_pk_bf16_f32 v77, v225, v226\n"
"v_accvgpr_read_b32 v221, a162\n"
"v_accvgpr_read_b32 v222, a163\n"
"v_cvt_pk_bf16_f32 v78, v227, v220\n"
"v_accvgpr_read_b32 v223, a152\n"
"v_accvgpr_read_b32 v224, a153\n"
"v_cvt_pk_bf16_f32 v79, v221, v222\n"
"s_nop 1\n"
"v_mfma_f32_16x16x32_bf16 v[100:103], a[208:211], v[76:79], v[100:103]\n"
"v_accvgpr_read_b32 v225, a154\n"
"v_accvgpr_read_b32 v226, a155\n"
"v_cvt_pk_bf16_f32 v80, v223, v224\n"
"v_accvgpr_read_b32 v227, a164\n"
"v_accvgpr_read_b32 v220, a165\n"
"v_cvt_pk_bf16_f32 v81, v225, v226\n"
"v_accvgpr_read_b32 v221, a166\n"
"v_accvgpr_read_b32 v222, a167\n"
"v_cvt_pk_bf16_f32 v82, v227, v220\n"
"v_accvgpr_read_b32 v223, a168\n"
"v_accvgpr_read_b32 v224, a169\n"
"v_cvt_pk_bf16_f32 v83, v221, v222\n"
"ds_read_b64_tr_b16 a[220:221], v193 offset:8704\n"
"s_nop 0\n"
"v_mfma_f32_16x16x32_bf16 v[104:107], a[208:211], v[80:83], v[104:107]\n"
"s_waitcnt lgkmcnt(3)\n"
"ds_read_b64_tr_b16 a[222:223], v193 offset:10752\n"
"v_accvgpr_read_b32 v225, a170\n"
"v_mfma_f32_16x16x32_bf16 v[108:111], a[212:215], v[72:75], v[108:111]\n"
"buffer_load_dwordx4 v206, s[36:39], s63 offen lds\n"
"v_mfma_f32_16x16x32_bf16 v[112:115], a[212:215], v[76:79], v[112:115]\n"
"s_add_u32 s63, s81, 86016\n"
"ds_read_b64_tr_b16 a[224:225], v192 offset:12288\n"
"s_add_u32 m0, s60, 126976\n"
"v_mfma_f32_16x16x32_bf16 v[116:119], a[212:215], v[80:83], v[116:119]\n"
"s_waitcnt lgkmcnt(3)\n"
"ds_read_b64_tr_b16 a[226:227], v192 offset:14336\n"
"v_accvgpr_read_b32 v226, a171\n"
"v_mfma_f32_16x16x32_bf16 v[120:123], a[216:219], v[72:75], v[120:123]\n"
"s_waitcnt vmcnt(23)\n"
"buffer_load_dwordx4 v206, s[36:39], s63 offen lds\n"
"v_mfma_f32_16x16x32_bf16 v[124:127], a[216:219], v[76:79], v[124:127]\n"
"s_add_u32 s63, s81, 114688\n"
"ds_read_b64_tr_b16 a[228:229], v193 offset:12288\n"
"s_add_u32 m0, s60, 131072\n"
"v_mfma_f32_16x16x32_bf16 v[128:131], a[216:219], v[80:83], v[128:131]\n"
"s_waitcnt lgkmcnt(3)\n"
"ds_read_b64_tr_b16 a[230:231], v193 offset:14336\n"
"v_cvt_pk_bf16_f32 v84, v223, v224\n"
"v_mfma_f32_16x16x32_bf16 v[132:135], a[220:223], v[72:75], v[132:135]\n"
"s_barrier\n"
"buffer_load_dwordx4 v206, s[36:39], s63 offen lds\n"
"v_mfma_f32_16x16x32_bf16 v[136:139], a[220:223], v[76:79], v[136:139]\n"
"s_add_u32 s63, s81, 143360\n"
"ds_read_b64_tr_b16 a[232:233], v192 offset:12800\n"
"v_accvgpr_read_b32 v227, a180\n"
"v_mfma_f32_16x16x32_bf16 v[140:143], a[220:223], v[80:83], v[140:143]\n"
"s_waitcnt lgkmcnt(3)\n"
"v_accvgpr_read_b32 v220, a181\n"
"v_cvt_pk_bf16_f32 v85, v225, v226\n"
"v_accvgpr_read_b32 v221, a182\n"
"v_accvgpr_read_b32 v222, a183\n"
"v_cvt_pk_bf16_f32 v86, v227, v220\n"
"v_accvgpr_read_b32 v223, a172\n"
"v_accvgpr_read_b32 v224, a173\n"
"v_cvt_pk_bf16_f32 v87, v221, v222\n"
"ds_read_b64_tr_b16 a[234:235], v192 offset:14848\n"
"s_nop 0\n"
"v_mfma_f32_16x16x32_bf16 v[96:99], a[224:227], v[84:87], v[96:99]\n"
"s_add_u32 m0, s60, 135168\n"
"v_accvgpr_read_b32 v225, a174\n"
"v_accvgpr_read_b32 v226, a175\n"
"v_cvt_pk_bf16_f32 v88, v223, v224\n"
"v_accvgpr_read_b32 v227, a184\n"
"v_accvgpr_read_b32 v220, a185\n"
"v_cvt_pk_bf16_f32 v89, v225, v226\n"
"v_accvgpr_read_b32 v221, a186\n"
"v_accvgpr_read_b32 v222, a187\n"
"v_cvt_pk_bf16_f32 v90, v227, v220\n"
"v_accvgpr_read_b32 v223, a176\n"
"v_accvgpr_read_b32 v224, a177\n"
"v_cvt_pk_bf16_f32 v91, v221, v222\n"
"s_nop 1\n"
"v_mfma_f32_16x16x32_bf16 v[100:103], a[224:227], v[88:91], v[100:103]\n"
"v_accvgpr_read_b32 v225, a178\n"
"v_accvgpr_read_b32 v226, a179\n"
"v_cvt_pk_bf16_f32 v92, v223, v224\n"
"v_accvgpr_read_b32 v227, a188\n"
"ds_read_b64_tr_b16 a[236:237], v193 offset:12800\n"
"v_accvgpr_read_b32 v220, a189\n"
"v_cvt_pk_bf16_f32 v93, v225, v226\n"
"v_accvgpr_read_b32 v221, a190\n"
"v_accvgpr_read_b32 v222, a191\n"
"v_cvt_pk_bf16_f32 v94, v227, v220\n"
"v_cvt_pk_bf16_f32 v95, v221, v222\n"
"s_nop 1\n"
"v_mfma_f32_16x16x32_bf16 v[104:107], a[224:227], v[92:95], v[104:107]\n"
"s_waitcnt lgkmcnt(3)\n"
"ds_read_b64_tr_b16 a[238:239], v193 offset:14848\n"
"ds_read_b64_tr_b16 a[208:209], v192 offset:16384\n"
"v_mfma_f32_16x16x32_bf16 v[108:111], a[228:231], v[84:87], v[108:111]\n"
"buffer_load_dwordx4 v206, s[36:39], s63 offen lds\n"
"v_mfma_f32_16x16x32_bf16 v[112:115], a[228:231], v[88:91], v[112:115]\n"
"s_add_u32 s63, s81, 172032\n"
"s_add_u32 m0, s60, 139264\n"
"ds_read_b64_tr_b16 a[210:211], v192 offset:18432\n"
"v_mfma_f32_16x16x32_bf16 v[116:119], a[228:231], v[92:95], v[116:119]\n"
"s_waitcnt lgkmcnt(4)\n"
"ds_read_b64_tr_b16 a[212:213], v192 offset:20480\n"
"ds_read_b64_tr_b16 a[214:215], v192 offset:22528\n"
"v_mfma_f32_16x16x32_bf16 v[120:123], a[232:235], v[84:87], v[120:123]\n"
"ds_read_b64_tr_b16 a[216:217], v192 offset:24576\n"
"ds_read_b64_tr_b16 a[218:219], v192 offset:26624\n"
"v_mfma_f32_16x16x32_bf16 v[124:127], a[232:235], v[88:91], v[124:127]\n"
"ds_read_b64_tr_b16 a[220:221], v192 offset:28672\n"
"ds_read_b64_tr_b16 a[222:223], v192 offset:30720\n"
"v_mfma_f32_16x16x32_bf16 v[128:131], a[232:235], v[92:95], v[128:131]\n"
"s_waitcnt lgkmcnt(8)\n"
"buffer_load_dwordx4 v206, s[36:39], s63 offen lds\n"
"v_mfma_f32_16x16x32_bf16 v[132:135], a[236:239], v[84:87], v[132:135]\n"
"s_add_u32 s63, s81, 200704\n"
"s_add_u32 m0, s60, 143360\n"
"ds_read_b64_tr_b16 a[224:225], v193 offset:16384\n"
"v_mfma_f32_16x16x32_bf16 v[136:139], a[236:239], v[88:91], v[136:139]\n"
"buffer_load_dwordx4 v206, s[36:39], s63 offen lds\n"
"v_mfma_f32_16x16x32_bf16 v[140:143], a[236:239], v[92:95], v[140:143]\n"
"s_waitcnt lgkmcnt(7)\n"
"s_add_u32 s63, s82, 0\n"
"s_add_u32 m0, s60, 147456\n"
"v_mfma_f32_16x16x32_bf16 v[144:147], a[208:211], v[0:3], 0\n"
"s_waitcnt lgkmcnt(5)\n"
"ds_read_b64_tr_b16 a[226:227], v193 offset:18432\n"
"ds_read_b64_tr_b16 a[228:229], v193 offset:20480\n"
"v_mfma_f32_16x16x32_bf16 v[144:147], a[212:215], v[12:15], v[144:147]\n"
"s_waitcnt lgkmcnt(5)\n"
"buffer_load_dwordx4 v206, s[36:39], s63 offen lds\n"
"v_mfma_f32_16x16x32_bf16 v[144:147], a[216:219], v[24:27], v[144:147]\n"
"s_waitcnt lgkmcnt(3)\n"
"ds_read_b64_tr_b16 a[230:231], v193 offset:22528\n"
"ds_read_b64_tr_b16 a[232:233], v193 offset:24576\n"
"v_mfma_f32_16x16x32_bf16 v[144:147], a[220:223], v[36:39], v[144:147]\n"
"s_add_u32 s63, s82, 28672\n"
"ds_read_b64_tr_b16 a[234:235], v193 offset:26624\n"
"ds_read_b64_tr_b16 a[236:237], v193 offset:28672\n"
"v_mfma_f32_16x16x32_bf16 v[148:151], a[220:223], v[40:43], 0\n"
"s_add_u32 m0, s60, 151552\n"
"ds_read_b64_tr_b16 a[238:239], v193 offset:30720\n"
"v_mul_f32 v96, v208, v96\n"
"v_mfma_f32_16x16x32_bf16 v[148:151], a[216:219], v[28:31], v[148:151]\n"
"buffer_load_dwordx4 v206, s[36:39], s63 offen lds\n"
"v_mfma_f32_16x16x32_bf16 v[148:151], a[212:215], v[16:19], v[148:151]\n"
"v_mul_f32 v97, v208, v97\n"
"v_mul_f32 v98, v208, v98\n"
"v_mfma_f32_16x16x32_bf16 v[148:151], a[208:211], v[4:7], v[148:151]\n"
"s_add_u32 s63, s82, 57344\n"
"v_mul_f32 v99, v208, v99\n"
"v_cvt_pk_bf16_f32 v232, v96, v97\n"
"v_mfma_f32_16x16x32_bf16 v[152:155], a[208:211], v[8:11], 0\n"
"s_add_u32 m0, s60, 155648\n"
"ds_read_b64_tr_b16 a[208:209], v192 offset:16896\n"
"ds_read_b64_tr_b16 a[210:211], v192 offset:18944\n"
"v_mfma_f32_16x16x32_bf16 v[152:155], a[212:215], v[20:23], v[152:155]\n"
"ds_read_b64_tr_b16 a[212:213], v192 offset:20992\n"
"ds_read_b64_tr_b16 a[214:215], v192 offset:23040\n"
"v_mfma_f32_16x16x32_bf16 v[152:155], a[216:219], v[32:35], v[152:155]\n"
"ds_read_b64_tr_b16 a[216:217], v192 offset:25088\n"
"ds_read_b64_tr_b16 a[218:219], v192 offset:27136\n"
"v_mfma_f32_16x16x32_bf16 v[152:155], a[220:223], v[44:47], v[152:155]\n"
"s_waitcnt lgkmcnt(12)\n"
"buffer_load_dwordx4 v206, s[36:39], s63 offen lds\n"
"v_mfma_f32_16x16x32_bf16 v[164:167], a[224:227], v[8:11], 0\n"
"s_waitcnt lgkmcnt(10)\n"
"ds_read_b64_tr_b16 a[220:221], v192 offset:29184\n"
"ds_read_b64_tr_b16 a[222:223], v192 offset:31232\n"
"v_mfma_f32_16x16x32_bf16 v[164:167], a[228:231], v[20:23], v[164:167]\n"
"s_waitcnt lgkmcnt(10)\n"
"v_cvt_pk_bf16_f32 v233, v98, v99\n"
"v_mul_f32 v108, v208, v108\n"
"v_mfma_f32_16x16x32_bf16 v[164:167], a[232:235], v[32:35], v[164:167]\n"
"s_waitcnt lgkmcnt(8)\n"
"s_add_u32 s63, s82, 86016\n"
"v_mul_f32 v109, v208, v109\n"
"v_mfma_f32_16x16x32_bf16 v[164:167], a[236:239], v[44:47], v[164:167]\n"
"s_add_u32 m0, s60, 159744\n"
"v_mul_f32 v110, v208, v110\n"
"v_mul_f32 v111, v208, v111\n"
"v_mfma_f32_16x16x32_bf16 v[160:163], a[236:239], v[40:43], 0\n"
"buffer_load_dwordx4 v206, s[36:39], s63 offen lds\n"
"v_mfma_f32_16x16x32_bf16 v[160:163], a[232:235], v[28:31], v[160:163]\n"
"v_cvt_pk_bf16_f32 v234, v108, v109\n"
"v_cvt_pk_bf16_f32 v235, v110, v111\n"
"v_mfma_f32_16x16x32_bf16 v[160:163], a[228:231], v[16:19], v[160:163]\n"
"s_mov_b32 s66, 0\n"
"buffer_store_dwordx4 v[232:235], v207, s[40:43], s66 offen sc1\n"
"v_mfma_f32_16x16x32_bf16 v[160:163], a[224:227], v[4:7], v[160:163]\n"
"v_mul_f32 v100, v209, v100\n"
"v_mul_f32 v101, v209, v101\n"
"v_mfma_f32_16x16x32_bf16 v[156:159], a[224:227], v[0:3], 0\n"
"ds_read_b64_tr_b16 a[224:225], v193 offset:16896\n"
"ds_read_b64_tr_b16 a[226:227], v193 offset:18944\n"
"v_mfma_f32_16x16x32_bf16 v[156:159], a[228:231], v[12:15], v[156:159]\n"
"s_add_u32 s63, s82, 114688\n"
"ds_read_b64_tr_b16 a[228:229], v193 offset:20992\n"
"ds_read_b64_tr_b16 a[230:231], v193 offset:23040\n"
"v_mfma_f32_16x16x32_bf16 v[156:159], a[232:235], v[24:27], v[156:159]\n"
"s_add_u32 m0, s60, 0\n"
"ds_read_b64_tr_b16 a[232:233], v193 offset:25088\n"
"ds_read_b64_tr_b16 a[234:235], v193 offset:27136\n"
"v_mfma_f32_16x16x32_bf16 v[156:159], a[236:239], v[36:39], v[156:159]\n"
"s_waitcnt lgkmcnt(12)\n"
"ds_read_b64_tr_b16 a[236:237], v193 offset:29184\n"
"ds_read_b64_tr_b16 a[238:239], v193 offset:31232\n"
"v_mfma_f32_16x16x32_bf16 v[168:171], a[208:211], v[0:3], 0\n"
"s_waitcnt lgkmcnt(12)\n"
"v_mul_f32 v102, v209, v102\n"
"v_mul_f32 v103, v209, v103\n"
"v_mfma_f32_16x16x32_bf16 v[168:171], a[212:215], v[12:15], v[168:171]\n"
"s_waitcnt lgkmcnt(10)\n"
"v_cvt_pk_bf16_f32 v236, v100, v101\n"
"v_cvt_pk_bf16_f32 v237, v102, v103\n"
"v_mfma_f32_16x16x32_bf16 v[168:171], a[216:219], v[24:27], v[168:171]\n"
"s_waitcnt lgkmcnt(8)\n"
"v_mul_f32 v112, v209, v112\n"
"v_mul_f32 v113, v209, v113\n"
"v_mfma_f32_16x16x32_bf16 v[168:171], a[220:223], v[36:39], v[168:171]\n"
"v_mul_f32 v114, v209, v114\n"
"v_mul_f32 v115, v209, v115\n"
"v_mfma_f32_16x16x32_bf16 v[172:175], a[220:223], v[40:43], 0\n"
"v_cvt_pk_bf16_f32 v238, v112, v113\n"
"v_cvt_pk_bf16_f32 v239, v114, v115\n"
"v_mfma_f32_16x16x32_bf16 v[172:175], a[216:219], v[28:31], v[172:175]\n"
"buffer_store_dwordx4 v[236:239], v207, s[40:43], s66 offen offset:1024 sc1\n"
"v_mfma_f32_16x16x32_bf16 v[172:175], a[212:215], v[16:19], v[172:175]\n"
"s_waitcnt vmcnt(30)\n"
"s_barrier\n"
"v_mul_f32 v104, v210, v104\n"
"v_mfma_f32_16x16x32_bf16 v[172:175], a[208:211], v[4:7], v[172:175]\n"
"buffer_load_dwordx4 v206, s[36:39], s63 offen lds\n"
"v_mfma_f32_16x16x32_bf16 v[176:179], a[208:211], v[8:11], 0\n"
"s_add_u32 s63, s82, 143360\n"
"s_add_u32 m0, s60, 4096\n"
"ds_read_b64_tr_b16 a[208:209], v192 offset:32768\n"
"v_mfma_f32_16x16x32_bf16 v[176:179], a[212:215], v[20:23], v[176:179]\n"
"buffer_load_dwordx4 v206, s[36:39], s63 offen lds\n"
"v_mfma_f32_16x16x32_bf16 v[176:179], a[216:219], v[32:35], v[176:179]\n"
"ds_read_b64_tr_b16 a[210:211], v192 offset:34816\n"
"ds_read_b64_tr_b16 a[212:213], v192 offset:36864\n"
"v_mfma_f32_16x16x32_bf16 v[176:179], a[220:223], v[44:47], v[176:179]\n"
"s_waitcnt lgkmcnt(9)\n"
"ds_read_b64_tr_b16 a[214:215], v192 offset:38912\n"
"ds_read_b64_tr_b16 a[216:217], v192 offset:40960\n"
"v_mfma_f32_16x16x32_bf16 v[188:191], a[224:227], v[8:11], 0\n"
"s_waitcnt lgkmcnt(9)\n"
"ds_read_b64_tr_b16 a[218:219], v192 offset:43008\n"
"ds_read_b64_tr_b16 a[220:221], v192 offset:45056\n"
"v_mfma_f32_16x16x32_bf16 v[188:191], a[228:231], v[20:23], v[188:191]\n"
"s_waitcnt lgkmcnt(9)\n"
"ds_read_b64_tr_b16 a[222:223], v192 offset:47104\n"
"s_add_u32 s63, s82, 172032\n"
"v_mfma_f32_16x16x32_bf16 v[188:191], a[232:235], v[32:35], v[188:191]\n"
"s_waitcnt lgkmcnt(8)\n"
"s_add_u32 m0, s60, 8192\n"
"v_mul_f32 v105, v210, v105\n"
"v_mfma_f32_16x16x32_bf16 v[188:191], a[236:239], v[44:47], v[188:191]\n"
"buffer_load_dwordx4 v206, s[36:39], s63 offen lds\n"
"v_mfma_f32_16x16x32_bf16 v[184:187], a[236:239], v[40:43], 0\n"
"v_mul_f32 v106, v210, v106\n"
"v_mul_f32 v107, v210, v107\n"
"v_mfma_f32_16x16x32_bf16 v[184:187], a[232:235], v[28:31], v[184:187]\n"
"v_cvt_pk_bf16_f32 v240, v104, v105\n"
"v_cvt_pk_bf16_f32 v241, v106, v107\n"
"v_mfma_f32_16x16x32_bf16 v[184:187], a[228:231], v[16:19], v[184:187]\n"
"s_add_u32 s63, s82, 200704\n"
"v_mul_f32 v116, v210, v116\n"
"v_mul_f32 v117, v210, v117\n"
"v_mfma_f32_16x16x32_bf16 v[184:187], a[224:227], v[4:7], v[184:187]\n"
"s_add_u32 m0, s60, 12288\n"
"v_mul_f32 v118, v210, v118\n"
"v_mul_f32 v119, v210, v119\n"
"v_mfma_f32_16x16x32_bf16 v[180:183], a[224:227], v[0:3], 0\n"
"ds_read_b64_tr_b16 a[224:225], v193 offset:32768\n"
"ds_read_b64_tr_b16 a[226:227], v193 offset:34816\n"
"v_mfma_f32_16x16x32_bf16 v[180:183], a[228:231], v[12:15], v[180:183]\n"
"ds_read_b64_tr_b16 a[228:229], v193 offset:36864\n"
"ds_read_b64_tr_b16 a[230:231], v193 offset:38912\n"
"v_mfma_f32_16x16x32_bf16 v[180:183], a[232:235], v[24:27], v[180:183]\n"
"buffer_load_dwordx4 v206, s[36:39], s63 offen lds\n"
"v_mfma_f32_16x16x32_bf16 v[180:183], a[236:239], v[36:39], v[180:183]\n"
"s_waitcnt lgkmcnt(10)\n"
"ds_read_b64_tr_b16 a[232:233], v193 offset:40960\n"
"ds_read_b64_tr_b16 a[234:235], v193 offset:43008\n"
"v_mfma_f32_16x16x32_bf16 v[144:147], a[208:211], v[48:51], v[144:147]\n"
"s_waitcnt lgkmcnt(10)\n"
"ds_read_b64_tr_b16 a[236:237], v193 offset:45056\n"
"ds_read_b64_tr_b16 a[238:239], v193 offset:47104\n"
"v_mfma_f32_16x16x32_bf16 v[144:147], a[212:215], v[60:63], v[144:147]\n"
"s_waitcnt lgkmcnt(10)\n"
"s_add_u32 s63, s83, 0\n"
"v_cvt_pk_bf16_f32 v242, v116, v117\n"
"v_mfma_f32_16x16x32_bf16 v[144:147], a[216:219], v[72:75], v[144:147]\n"
"s_waitcnt lgkmcnt(8)\n"
"s_add_u32 m0, s60, 16384\n"
"v_cvt_pk_bf16_f32 v243, v118, v119\n"
"v_mfma_f32_16x16x32_bf16 v[144:147], a[220:223], v[84:87], v[144:147]\n"
"buffer_store_dwordx4 v[240:243], v207, s[40:43], s66 offen offset:2048 sc1\n"
"v_mfma_f32_16x16x32_bf16 v[148:151], a[220:223], v[88:91], v[148:151]\n"
"v_mul_f32 v120, v208, v120\n"
"v_mul_f32 v121, v208, v121\n"
"v_mfma_f32_16x16x32_bf16 v[148:151], a[216:219], v[76:79], v[148:151]\n"
"v_mul_f32 v122, v208, v122\n"
"v_mul_f32 v123, v208, v123\n"
"v_mfma_f32_16x16x32_bf16 v[148:151], a[212:215], v[64:67], v[148:151]\n"
"v_cvt_pk_bf16_f32 v244, v120, v121\n"
"v_cvt_pk_bf16_f32 v245, v122, v123\n"
"v_mfma_f32_16x16x32_bf16 v[148:151], a[208:211], v[52:55], v[148:151]\n"
"v_mul_f32 v132, v208, v132\n"
"v_mul_f32 v133, v208, v133\n"
"v_mfma_f32_16x16x32_bf16 v[152:155], a[208:211], v[56:59], v[152:155]\n"
"ds_read_b64_tr_b16 a[208:209], v192 offset:33280\n"
"ds_read_b64_tr_b16 a[210:211], v192 offset:35328\n"
"v_mfma_f32_16x16x32_bf16 v[152:155], a[212:215], v[68:71], v[152:155]\n"
"ds_read_b64_tr_b16 a[212:213], v192 offset:37376\n"
"ds_read_b64_tr_b16 a[214:215], v192 offset:39424\n"
"v_mfma_f32_16x16x32_bf16 v[152:155], a[216:219], v[80:83], v[152:155]\n"
"ds_read_b64_tr_b16 a[216:217], v192 offset:41472\n"
"ds_read_b64_tr_b16 a[218:219], v192 offset:43520\n"
"v_mfma_f32_16x16x32_bf16 v[152:155], a[220:223], v[92:95], v[152:155]\n"
"s_waitcnt lgkmcnt(12)\n"
"ds_read_b64_tr_b16 a[220:221], v192 offset:45568\n"
"ds_read_b64_tr_b16 a[222:223], v192 offset:47616\n"
"v_mfma_f32_16x16x32_bf16 v[164:167], a[224:227], v[56:59], v[164:167]\n"
"s_waitcnt lgkmcnt(12)\n"
"v_mul_f32 v134, v208, v134\n"
"v_mul_f32 v135, v208, v135\n"
"v_mfma_f32_16x16x32_bf16 v[164:167], a[228:231], v[68:71], v[164:167]\n"
"s_waitcnt lgkmcnt(10)\n"
"v_cvt_pk_bf16_f32 v246, v132, v133\n"
"v_cvt_pk_bf16_f32 v247, v134, v135\n"
"v_mfma_f32_16x16x32_bf16 v[164:167], a[232:235], v[80:83], v[164:167]\n"
"s_waitcnt lgkmcnt(8)\n"
"buffer_store_dwordx4 v[244:247], v207, s[40:43], s66 offen offset:3072 sc1\n"
"v_mfma_f32_16x16x32_bf16 v[164:167], a[236:239], v[92:95], v[164:167]\n"
"v_mul_f32 v124, v209, v124\n"
"v_mul_f32 v125, v209, v125\n"
"v_mfma_f32_16x16x32_bf16 v[160:163], a[236:239], v[88:91], v[160:163]\n"
"v_mul_f32 v126, v209, v126\n"
"v_mul_f32 v127, v209, v127\n"
"v_mfma_f32_16x16x32_bf16 v[160:163], a[232:235], v[76:79], v[160:163]\n"
"v_cvt_pk_bf16_f32 v232, v124, v125\n"
"v_cvt_pk_bf16_f32 v233, v126, v127\n"
"v_mfma_f32_16x16x32_bf16 v[160:163], a[228:231], v[64:67], v[160:163]\n"
"v_mul_f32 v136, v209, v136\n"
"v_mul_f32 v137, v209, v137\n"
"v_mfma_f32_16x16x32_bf16 v[160:163], a[224:227], v[52:55], v[160:163]\n"
"v_mul_f32 v138, v209, v138\n"
"v_mul_f32 v139, v209, v139\n"
"v_mfma_f32_16x16x32_bf16 v[156:159], a[224:227], v[48:51], v[156:159]\n"
"ds_read_b64_tr_b16 a[224:225], v193 offset:33280\n"
"ds_read_b64_tr_b16 a[226:227], v193 offset:35328\n"
"v_mfma_f32_16x16x32_bf16 v[156:159], a[228:231], v[60:63], v[156:159]\n"
"ds_read_b64_tr_b16 a[228:229], v193 offset:37376\n"
"ds_read_b64_tr_b16 a[230:231], v193 offset:39424\n"
"v_mfma_f32_16x16x32_bf16 v[156:159], a[232:235], v[72:75], v[156:159]\n"
"ds_read_b64_tr_b16 a[232:233], v193 offset:41472\n"
"ds_read_b64_tr_b16 a[234:235], v193 offset:43520\n"
"v_mfma_f32_16x16x32_bf16 v[156:159], a[236:239], v[84:87], v[156:159]\n"
"s_waitcnt lgkmcnt(12)\n"
"ds_read_b64_tr_b16 a[236:237], v193 offset:45568\n"
"ds_read_b64_tr_b16 a[238:239], v193 offset:47616\n"
"v_mfma_f32_16x16x32_bf16 v[168:171], a[208:211], v[48:51], v[168:171]\n"
"s_waitcnt lgkmcnt(12)\n"
"v_cvt_pk_bf16_f32 v234, v136, v137\n"
"v_cvt_pk_bf16_f32 v235, v138, v139\n"
"v_mfma_f32_16x16x32_bf16 v[168:171], a[212:215], v[60:63], v[168:171]\n"
"s_waitcnt lgkmcnt(10)\n"
"s_mov_b32 s66, 4096\n"
"v_mbcnt_lo_u32_b32 v252, -1, 0\n"
"v_mfma_f32_16x16x32_bf16 v[168:171], a[216:219], v[72:75], v[168:171]\n"
"s_waitcnt lgkmcnt(8)\n"
"buffer_store_dwordx4 v[232:235], v207, s[40:43], s66 offen sc1\n"
"v_mfma_f32_16x16x32_bf16 v[168:171], a[220:223], v[84:87], v[168:171]\n"
"v_mul_f32 v128, v210, v128\n"
"v_mul_f32 v129, v210, v129\n"
"v_mfma_f32_16x16x32_bf16 v[172:175], a[220:223], v[88:91], v[172:175]\n"
"v_mul_f32 v130, v210, v130\n"
"v_mul_f32 v131, v210, v131\n"
"v_mfma_f32_16x16x32_bf16 v[172:175], a[216:219], v[76:79], v[172:175]\n"
"v_cvt_pk_bf16_f32 v236, v128, v129\n"
"v_cvt_pk_bf16_f32 v237, v130, v131\n"
"v_mfma_f32_16x16x32_bf16 v[172:175], a[212:215], v[64:67], v[172:175]\n"
"s_waitcnt vmcnt(33)\n"
"s_barrier\n"
"v_mul_f32 v140, v210, v140\n"
"v_mfma_f32_16x16x32_bf16 v[172:175], a[208:211], v[52:55], v[172:175]\n"
"buffer_load_dwordx4 v206, s[36:39], s63 offen lds\n"
"v_mfma_f32_16x16x32_bf16 v[176:179], a[208:211], v[56:59], v[176:179]\n"
"s_add_u32 s63, s83, 28672\n"
"s_add_u32 m0, s60, 20480\n"
"ds_read_b64_tr_b16 a[208:209], v192 offset:49152\n"
"v_mfma_f32_16x16x32_bf16 v[176:179], a[212:215], v[68:71], v[176:179]\n"
"buffer_load_dwordx4 v206, s[36:39], s63 offen lds\n"
"v_mfma_f32_16x16x32_bf16 v[176:179], a[216:219], v[80:83], v[176:179]\n"
"s_add_u32 s63, s83, 57344\n"
"s_add_u32 m0, s60, 24576\n"
"ds_read_b64_tr_b16 a[210:211], v192 offset:51200\n"
"v_mfma_f32_16x16x32_bf16 v[176:179], a[220:223], v[92:95], v[176:179]\n"
"s_waitcnt lgkmcnt(8)\n"
"buffer_load_dwordx4 v206, s[36:39], s63 offen lds\n"
"v_mfma_f32_16x16x32_bf16 v[188:191], a[224:227], v[56:59], v[188:191]\n"
"s_waitcnt lgkmcnt(6)\n"
"s_add_u32 s63, s83, 86016\n"
"s_add_u32 m0, s60, 28672\n"
"v_mfma_f32_16x16x32_bf16 v[188:191], a[228:231], v[68:71], v[188:191]\n"
"s_waitcnt lgkmcnt(4)\n"
"buffer_load_dwordx4 v206, s[36:39], s63 offen lds\n"
"v_mfma_f32_16x16x32_bf16 v[188:191], a[232:235], v[80:83], v[188:191]\n"
"s_waitcnt lgkmcnt(2)\n"
"s_add_u32 s63, s83, 114688\n"
"s_add_u32 m0, s60, 32768\n"
"v_mfma_f32_16x16x32_bf16 v[188:191], a[236:239], v[92:95], v[188:191]\n"
"ds_read_b64_tr_b16 a[212:213], v192 offset:53248\n"
"ds_read_b64_tr_b16 a[214:215], v192 offset:55296\n"
"v_mfma_f32_16x16x32_bf16 v[184:187], a[236:239], v[88:91], v[184:187]\n"
"ds_read_b64_tr_b16 a[216:217], v192 offset:57344\n"
"ds_read_b64_tr_b16 a[218:219], v192 offset:59392\n"
"v_mfma_f32_16x16x32_bf16 v[184:187], a[232:235], v[76:79], v[184:187]\n"
"ds_read_b64_tr_b16 a[220:221], v192 offset:61440\n"
"ds_read_b64_tr_b16 a[222:223], v192 offset:63488\n"
"v_mfma_f32_16x16x32_bf16 v[184:187], a[228:231], v[64:67], v[184:187]\n"
"v_mul_f32 v141, v210, v141\n"
"v_mul_f32 v142, v210, v142\n"
"v_mfma_f32_16x16x32_bf16 v[184:187], a[224:227], v[52:55], v[184:187]\n"
"v_mul_f32 v143, v210, v143\n"
"v_cvt_pk_bf16_f32 v238, v140, v141\n"
"v_mfma_f32_16x16x32_bf16 v[180:183], a[224:227], v[48:51], v[180:183]\n"
"v_cvt_pk_bf16_f32 v239, v142, v143\n"
"ds_read_b64_tr_b16 a[224:225], v193 offset:49152\n"
"v_mfma_f32_16x16x32_bf16 v[180:183], a[228:231], v[60:63], v[180:183]\n"
"buffer_store_dwordx4 v[236:239], v207, s[40:43], s66 offen offset:1024 sc1\n"
"v_mfma_f32_16x16x32_bf16 v[180:183], a[232:235], v[72:75], v[180:183]\n"
"ds_read_b64_tr_b16 a[226:227], v193 offset:51200\n"
"ds_read_b64_tr_b16 a[228:229], v193 offset:53248\n"
"v_mfma_f32_16x16x32_bf16 v[180:183], a[236:239], v[84:87], v[180:183]\n"
"s_waitcnt lgkmcnt(9)\n"
"ds_read_b64_tr_b16 a[230:231], v193 offset:55296\n"
"ds_read_b64_tr_b16 a[232:233], v193 offset:57344\n"
"v_mfma_f32_16x16x32_bf16 v[96:99], a[208:211], v[0:3], 0\n"
"s_waitcnt lgkmcnt(9)\n"
"ds_read_b64_tr_b16 a[234:235], v193 offset:59392\n"
"ds_read_b64_tr_b16 a[236:237], v193 offset:61440\n"
"v_mfma_f32_16x16x32_bf16 v[96:99], a[212:215], v[12:15], v[96:99]\n"
"s_waitcnt lgkmcnt(9)\n"
"ds_read_b64_tr_b16 a[238:239], v193 offset:63488\n"
"v_mbcnt_hi_u32_b32 v252, -1, v252\n"
"v_mfma_f32_16x16x32_bf16 v[96:99], a[216:219], v[24:27], v[96:99]\n"
"s_waitcnt lgkmcnt(8)\n"
"v_bfe_u32 v218, v252, 3, 1\n"
"v_and_b32 v212, 3, v252\n"
"v_mfma_f32_16x16x32_bf16 v[96:99], a[220:223], v[36:39], v[96:99]\n"
"v_lshrrev_b32 v213, 4, v252\n"
"v_lshl_add_u32 v216, v212, 4, s20\n"
"v_mfma_f32_16x16x32_bf16 v[100:103], a[220:223], v[40:43], 0\n"
"v_lshl_add_u32 v216, v213, 6, v216\n"
"v_lshl_add_u32 v216, v218, 19, v216\n"
"v_mfma_f32_16x16x32_bf16 v[100:103], a[216:219], v[28:31], v[100:103]\n"
"v_mul_u32_u24 v217, 896, v212\n"
"v_lshl_add_u32 v217, v213, 4, v217\n"
"v_mfma_f32_16x16x32_bf16 v[100:103], a[212:215], v[16:19], v[100:103]\n"
"v_bfe_u32 v212, v252, 2, 1\n"
"v_lshl_add_u32 v217, v212, 6, v217\n"
"v_mfma_f32_16x16x32_bf16 v[100:103], a[208:211], v[4:7], v[100:103]\n"
"v_mul_u32_u24 v212, 3072, v212\n"
"v_add_u32 v216, v216, v212\n"
"v_mfma_f32_16x16x32_bf16 v[104:107], a[208:211], v[8:11], 0\n"
"ds_read_b64_tr_b16 a[208:209], v192 offset:49664\n"
"ds_read_b64_tr_b16 a[210:211], v192 offset:51712\n"
"v_mfma_f32_16x16x32_bf16 v[104:107], a[212:215], v[20:23], v[104:107]\n"
"ds_read_b64_tr_b16 a[212:213], v192 offset:53760\n"
"ds_read_b64_tr_b16 a[214:215], v192 offset:55808\n"
"v_mfma_f32_16x16x32_bf16 v[104:107], a[216:219], v[32:35], v[104:107]\n"
"ds_read_b64_tr_b16 a[216:217], v192 offset:57856\n"
"ds_read_b64_tr_b16 a[218:219], v192 offset:59904\n"
"v_mfma_f32_16x16x32_bf16 v[104:107], a[220:223], v[44:47], v[104:107]\n"
"s_waitcnt lgkmcnt(12)\n"
"ds_read_b64_tr_b16 a[220:221], v192 offset:61952\n"
"ds_read_b64_tr_b16 a[222:223], v192 offset:64000\n"
"v_mfma_f32_16x16x32_bf16 v[116:119], a[224:227], v[8:11], 0\n"
"s_waitcnt lgkmcnt(12)\n"
"v_lshl_add_u32 v217, v218, 29, v217\n"
"v_add_u32 v217, s21, v217\n"
"v_mfma_f32_16x16x32_bf16 v[116:119], a[228:231], v[20:23], v[116:119]\n"
"s_waitcnt lgkmcnt(10)\n"
"v_mul_f32 v144, v208, v144\n"
"v_and_b32 v214, 15, v252\n"
"v_mfma_f32_16x16x32_bf16 v[116:119], a[232:235], v[32:35], v[116:119]\n"
"s_waitcnt lgkmcnt(8)\n"
"v_mul_f32 v145, v208, v145\n"
"v_lshlrev_b32 v214, 7, v214\n"
"v_mfma_f32_16x16x32_bf16 v[116:119], a[236:239], v[44:47], v[116:119]\n"
"v_mul_f32 v146, v208, v146\n"
"v_lshl_add_u32 v219, v252, 24, s23\n"
"v_mfma_f32_16x16x32_bf16 v[112:115], a[236:239], v[40:43], 0\n"
"v_mul_f32 v147, v208, v147\n"
"v_lshl_add_u32 v215, v252, 24, s22\n"
"v_mfma_f32_16x16x32_bf16 v[112:115], a[232:235], v[28:31], v[112:115]\n"
"v_cvt_pk_bf16_f32 v240, v144, v145\n"
"v_mov_b32 v255, s57\n"
"v_mfma_f32_16x16x32_bf16 v[112:115], a[228:231], v[16:19], v[112:115]\n"
"v_cvt_pk_bf16_f32 v241, v146, v147\n"
"v_mul_f32 v156, v208, v156\n"
"v_mfma_f32_16x16x32_bf16 v[112:115], a[224:227], v[4:7], v[112:115]\n"
"v_mul_f32 v157, v208, v157\n"
"v_mul_f32 v158, v208, v158\n"
"v_mfma_f32_16x16x32_bf16 v[108:111], a[224:227], v[0:3], 0\n"
"ds_read_b64_tr_b16 a[224:225], v193 offset:49664\n"
"ds_read_b64_tr_b16 a[226:227], v193 offset:51712\n"
"v_mfma_f32_16x16x32_bf16 v[108:111], a[228:231], v[12:15], v[108:111]\n"
"ds_read_b64_tr_b16 a[228:229], v193 offset:53760\n"
"ds_read_b64_tr_b16 a[230:231], v193 offset:55808\n"
"v_mfma_f32_16x16x32_bf16 v[108:111], a[232:235], v[24:27], v[108:111]\n"
"ds_read_b64_tr_b16 a[232:233], v193 offset:57856\n"
"ds_read_b64_tr_b16 a[234:235], v193 offset:59904\n"
"v_mfma_f32_16x16x32_bf16 v[108:111], a[236:239], v[36:39], v[108:111]\n"
"s_waitcnt lgkmcnt(12)\n"
"ds_read_b64_tr_b16 a[236:237], v193 offset:61952\n"
"ds_read_b64_tr_b16 a[238:239], v193 offset:64000\n"
"v_mfma_f32_16x16x32_bf16 v[120:123], a[208:211], v[0:3], 0\n"
"s_waitcnt lgkmcnt(12)\n"
"v_mul_f32 v159, v208, v159\n"
"v_cvt_pk_bf16_f32 v242, v156, v157\n"
"v_mfma_f32_16x16x32_bf16 v[120:123], a[212:215], v[12:15], v[120:123]\n"
"s_waitcnt lgkmcnt(10)\n"
"v_cvt_pk_bf16_f32 v243, v158, v159\n"
"s_mov_b32 s66, 16384\n"
"v_mfma_f32_16x16x32_bf16 v[120:123], a[216:219], v[24:27], v[120:123]\n"
"s_waitcnt lgkmcnt(8)\n"
"buffer_store_dwordx4 v[240:243], v207, s[40:43], s66 offen sc1\n"
"v_mfma_f32_16x16x32_bf16 v[120:123], a[220:223], v[36:39], v[120:123]\n"
"v_mul_f32 v148, v209, v148\n"
"v_mul_f32 v149, v209, v149\n"
"v_mfma_f32_16x16x32_bf16 v[124:127], a[220:223], v[40:43], 0\n"
"v_mul_f32 v150, v209, v150\n"
"v_mul_f32 v151, v209, v151\n"
"v_mfma_f32_16x16x32_bf16 v[124:127], a[216:219], v[28:31], v[124:127]\n"
"v_cvt_pk_bf16_f32 v244, v148, v149\n"
"v_cvt_pk_bf16_f32 v245, v150, v151\n"
"v_mfma_f32_16x16x32_bf16 v[124:127], a[212:215], v[16:19], v[124:127]\n"
"s_waitcnt vmcnt(35)\n"
"s_barrier\n"
"v_mul_f32 v160, v209, v160\n"
"v_mfma_f32_16x16x32_bf16 v[124:127], a[208:211], v[4:7], v[124:127]\n"
"buffer_load_dwordx4 v206, s[36:39], s63 offen lds\n"
"v_mfma_f32_16x16x32_bf16 v[128:131], a[208:211], v[8:11], 0\n"
"s_add_u32 s63, s83, 143360\n"
"s_add_u32 m0, s60, 36864\n"
"ds_read_b64_tr_b16 a[208:209], v194 offset:0\n"
"v_mfma_f32_16x16x32_bf16 v[128:131], a[212:215], v[20:23], v[128:131]\n"
"buffer_load_dwordx4 v206, s[36:39], s63 offen lds\n"
"v_mfma_f32_16x16x32_bf16 v[128:131], a[216:219], v[32:35], v[128:131]\n"
"s_add_u32 s63, s83, 172032\n"
"s_add_u32 m0, s60, 40960\n"
"ds_read_b64_tr_b16 a[210:211], v194 offset:2048\n"
"v_mfma_f32_16x16x32_bf16 v[128:131], a[220:223], v[44:47], v[128:131]\n"
"s_waitcnt lgkmcnt(8)\n"
"buffer_load_dwordx4 v206, s[36:39], s63 offen lds\n"
"v_mfma_f32_16x16x32_bf16 v[140:143], a[224:227], v[8:11], 0\n"
"s_waitcnt lgkmcnt(6)\n"
"s_add_u32 s63, s83, 200704\n"
"s_add_u32 m0, s60, 45056\n"
"v_mfma_f32_16x16x32_bf16 v[140:143], a[228:231], v[20:23], v[140:143]\n"
"s_waitcnt lgkmcnt(4)\n"
"buffer_load_dwordx4 v206, s[36:39], s63 offen lds\n"
"v_mfma_f32_16x16x32_bf16 v[140:143], a[232:235], v[32:35], v[140:143]\n"
"s_waitcnt lgkmcnt(2)\n"
"ds_read_b64_tr_b16 a[212:213], v194 offset:4096\n"
"ds_read_b64_tr_b16 a[214:215], v194 offset:6144\n"
"v_mfma_f32_16x16x32_bf16 v[140:143], a[236:239], v[44:47], v[140:143]\n"
"ds_read_b64_tr_b16 a[216:217], v194 offset:8192\n"
"ds_read_b64_tr_b16 a[218:219], v194 offset:10240\n"
"v_mfma_f32_16x16x32_bf16 v[136:139], a[236:239], v[40:43], 0\n"
"ds_read_b64_tr_b16 a[220:221], v194 offset:12288\n"
"ds_read_b64_tr_b16 a[222:223], v194 offset:14336\n"
"v_mfma_f32_16x16x32_bf16 v[136:139], a[232:235], v[28:31], v[136:139]\n"
"v_mul_f32 v161, v209, v161\n"
"v_mul_f32 v162, v209, v162\n"
"v_mfma_f32_16x16x32_bf16 v[136:139], a[228:231], v[16:19], v[136:139]\n"
"v_mul_f32 v163, v209, v163\n"
"v_cvt_pk_bf16_f32 v246, v160, v161\n"
"v_mfma_f32_16x16x32_bf16 v[136:139], a[224:227], v[4:7], v[136:139]\n"
"v_cvt_pk_bf16_f32 v247, v162, v163\n"
"v_mfma_f32_16x16x32_bf16 v[132:135], a[224:227], v[0:3], 0\n"
"ds_read_b64_tr_b16 a[224:225], v195 offset:0\n"
"ds_read_b64_tr_b16 a[226:227], v195 offset:2048\n"
"v_mfma_f32_16x16x32_bf16 v[132:135], a[228:231], v[12:15], v[132:135]\n"
"ds_read_b64_tr_b16 a[228:229], v195 offset:4096\n"
"ds_read_b64_tr_b16 a[230:231], v195 offset:6144\n"
"v_mfma_f32_16x16x32_bf16 v[132:135], a[232:235], v[24:27], v[132:135]\n"
"ds_read_b64_tr_b16 a[232:233], v195 offset:8192\n"
"ds_read_b64_tr_b16 a[234:235], v195 offset:10240\n"
"v_mfma_f32_16x16x32_bf16 v[132:135], a[236:239], v[36:39], v[132:135]\n"
"s_waitcnt lgkmcnt(12)\n"
"ds_read_b64_tr_b16 a[236:237], v195 offset:12288\n"
"ds_read_b64_tr_b16 a[238:239], v195 offset:14336\n"
"v_mfma_f32_16x16x32_bf16 v[96:99], a[208:211], v[48:51], v[96:99]\n"
"s_waitcnt lgkmcnt(12)\n"
"buffer_store_dwordx4 v[244:247], v207, s[40:43], s66 offen offset:1024 sc1\n"
"v_mfma_f32_16x16x32_bf16 v[96:99], a[212:215], v[60:63], v[96:99]\n"
"s_waitcnt lgkmcnt(10)\n"
"v_mul_f32 v152, v210, v152\n"
"v_mul_f32 v153, v210, v153\n"
"v_mfma_f32_16x16x32_bf16 v[96:99], a[216:219], v[72:75], v[96:99]\n"
"s_waitcnt lgkmcnt(8)\n"
"v_mul_f32 v154, v210, v154\n"
"v_mul_f32 v155, v210, v155\n"
"v_mfma_f32_16x16x32_bf16 v[96:99], a[220:223], v[84:87], v[96:99]\n"
"v_cvt_pk_bf16_f32 v232, v152, v153\n"
"v_cvt_pk_bf16_f32 v233, v154, v155\n"
"v_mfma_f32_16x16x32_bf16 v[100:103], a[220:223], v[88:91], v[100:103]\n"
"v_mul_f32 v164, v210, v164\n"
"v_mul_f32 v165, v210, v165\n"
"v_mfma_f32_16x16x32_bf16 v[100:103], a[216:219], v[76:79], v[100:103]\n"
"v_mul_f32 v166, v210, v166\n"
"v_mul_f32 v167, v210, v167\n"
"v_mfma_f32_16x16x32_bf16 v[100:103], a[212:215], v[64:67], v[100:103]\n"
"v_cvt_pk_bf16_f32 v234, v164, v165\n"
"v_cvt_pk_bf16_f32 v235, v166, v167\n"
"v_mfma_f32_16x16x32_bf16 v[100:103], a[208:211], v[52:55], v[100:103]\n"
"buffer_store_dwordx4 v[232:235], v207, s[40:43], s66 offen offset:2048 sc1\n"
"v_mfma_f32_16x16x32_bf16 v[104:107], a[208:211], v[56:59], v[104:107]\n"
"ds_read_b64_tr_b16 a[208:209], v194 offset:512\n"
"ds_read_b64_tr_b16 a[210:211], v194 offset:2560\n"
"v_mfma_f32_16x16x32_bf16 v[104:107], a[212:215], v[68:71], v[104:107]\n"
"ds_read_b64_tr_b16 a[212:213], v194 offset:4608\n"
"ds_read_b64_tr_b16 a[214:215], v194 offset:6656\n"
"v_mfma_f32_16x16x32_bf16 v[104:107], a[216:219], v[80:83], v[104:107]\n"
"ds_read_b64_tr_b16 a[216:217], v194 offset:8704\n"
"ds_read_b64_tr_b16 a[218:219], v194 offset:10752\n"
"v_mfma_f32_16x16x32_bf16 v[104:107], a[220:223], v[92:95], v[104:107]\n"
"s_waitcnt lgkmcnt(12)\n"
"ds_read_b64_tr_b16 a[220:221], v194 offset:12800\n"
"ds_read_b64_tr_b16 a[222:223], v194 offset:14848\n"
"v_mfma_f32_16x16x32_bf16 v[116:119], a[224:227], v[56:59], v[116:119]\n"
"s_waitcnt lgkmcnt(12)\n"
"v_mul_f32 v168, v208, v168\n"
"v_mul_f32 v169, v208, v169\n"
"v_mfma_f32_16x16x32_bf16 v[116:119], a[228:231], v[68:71], v[116:119]\n"
"s_waitcnt lgkmcnt(10)\n"
"v_mul_f32 v170, v208, v170\n"
"v_mul_f32 v171, v208, v171\n"
"v_mfma_f32_16x16x32_bf16 v[116:119], a[232:235], v[80:83], v[116:119]\n"
"s_waitcnt lgkmcnt(8)\n"
"v_cvt_pk_bf16_f32 v236, v168, v169\n"
"v_cvt_pk_bf16_f32 v237, v170, v171\n"
"v_mfma_f32_16x16x32_bf16 v[116:119], a[236:239], v[92:95], v[116:119]\n"
"v_mul_f32 v180, v208, v180\n"
"v_mul_f32 v181, v208, v181\n"
"v_mfma_f32_16x16x32_bf16 v[112:115], a[236:239], v[88:91], v[112:115]\n"
"v_mul_f32 v182, v208, v182\n"
"v_mul_f32 v183, v208, v183\n"
"v_mfma_f32_16x16x32_bf16 v[112:115], a[232:235], v[76:79], v[112:115]\n"
"v_cvt_pk_bf16_f32 v238, v180, v181\n"
"v_cvt_pk_bf16_f32 v239, v182, v183\n"
"v_mfma_f32_16x16x32_bf16 v[112:115], a[228:231], v[64:67], v[112:115]\n"
"buffer_store_dwordx4 v[236:239], v207, s[40:43], s66 offen offset:3072 sc1\n"
"v_mfma_f32_16x16x32_bf16 v[112:115], a[224:227], v[52:55], v[112:115]\n"
"v_mul_f32 v172, v209, v172\n"
"v_mul_f32 v173, v209, v173\n"
"v_mfma_f32_16x16x32_bf16 v[108:111], a[224:227], v[48:51], v[108:111]\n"
"ds_read_b64_tr_b16 a[224:225], v195 offset:512\n"
"ds_read_b64_tr_b16 a[226:227], v195 offset:2560\n"
"v_mfma_f32_16x16x32_bf16 v[108:111], a[228:231], v[60:63], v[108:111]\n"
"ds_read_b64_tr_b16 a[228:229], v195 offset:4608\n"
"ds_read_b64_tr_b16 a[230:231], v195 offset:6656\n"
"v_mfma_f32_16x16x32_bf16 v[108:111], a[232:235], v[72:75], v[108:111]\n"
"ds_read_b64_tr_b16 a[232:233], v195 offset:8704\n"
"ds_read_b64_tr_b16 a[234:235], v195 offset:10752\n"
"v_mfma_f32_16x16x32_bf16 v[108:111], a[236:239], v[84:87], v[108:111]\n"
"s_waitcnt lgkmcnt(12)\n"
"ds_read_b64_tr_b16 a[236:237], v195 offset:12800\n"
"ds_read_b64_tr_b16 a[238:239], v195 offset:14848\n"
"v_mfma_f32_16x16x32_bf16 v[120:123], a[208:211], v[48:51], v[120:123]\n"
"s_waitcnt lgkmcnt(12)\n"
"v_mul_f32 v174, v209, v174\n"
"v_mul_f32 v175, v209, v175\n"
"v_mfma_f32_16x16x32_bf16 v[120:123], a[212:215], v[60:63], v[120:123]\n"
"s_waitcnt lgkmcnt(10)\n"
"v_cvt_pk_bf16_f32 v240, v172, v173\n"
"v_cvt_pk_bf16_f32 v241, v174, v175\n"
"v_mfma_f32_16x16x32_bf16 v[120:123], a[216:219], v[72:75], v[120:123]\n"
"s_waitcnt lgkmcnt(8)\n"
"v_mul_f32 v184, v209, v184\n"
"v_mul_f32 v185, v209, v185\n"
"v_mfma_f32_16x16x32_bf16 v[120:123], a[220:223], v[84:87], v[120:123]\n"
"v_mul_f32 v186, v209, v186\n"
"v_mul_f32 v187, v209, v187\n"
"v_mfma_f32_16x16x32_bf16 v[124:127], a[220:223], v[88:91], v[124:127]\n"
"v_cvt_pk_bf16_f32 v242, v184, v185\n"
"v_cvt_pk_bf16_f32 v243, v186, v187\n"
"v_mfma_f32_16x16x32_bf16 v[124:127], a[216:219], v[76:79], v[124:127]\n"
"s_mov_b32 s66, 20480\n"
"buffer_store_dwordx4 v[240:243], v207, s[40:43], s66 offen sc1\n"
"v_mfma_f32_16x16x32_bf16 v[124:127], a[212:215], v[64:67], v[124:127]\n"
"s_waitcnt vmcnt(39)\n"
"s_barrier\n"
"v_mul_f32 v176, v210, v176\n"
"v_mfma_f32_16x16x32_bf16 v[124:127], a[208:211], v[52:55], v[124:127]\n"
"v_mul_f32 v177, v210, v177\n"
"v_mul_f32 v178, v210, v178\n"
"v_mfma_f32_16x16x32_bf16 v[128:131], a[208:211], v[56:59], v[128:131]\n"
"ds_read_b64_tr_b16 a[208:209], v194 offset:16384\n"
"ds_read_b64_tr_b16 a[210:211], v194 offset:18432\n"
"v_mfma_f32_16x16x32_bf16 v[128:131], a[212:215], v[68:71], v[128:131]\n"
"ds_read_b64_tr_b16 a[212:213], v194 offset:20480\n"
"ds_read_b64_tr_b16 a[214:215], v194 offset:22528\n"
"v_mfma_f32_16x16x32_bf16 v[128:131], a[216:219], v[80:83], v[128:131]\n"
"ds_read_b64_tr_b16 a[216:217], v194 offset:24576\n"
"ds_read_b64_tr_b16 a[218:219], v194 offset:26624\n"
"v_mfma_f32_16x16x32_bf16 v[128:131], a[220:223], v[92:95], v[128:131]\n"
"s_waitcnt lgkmcnt(12)\n"
"ds_read_b64_tr_b16 a[220:221], v194 offset:28672\n"
"v_mul_f32 v179, v210, v179\n"
"v_mfma_f32_16x16x32_bf16 v[140:143], a[224:227], v[56:59], v[140:143]\n"
"s_waitcnt lgkmcnt(11)\n"
"v_cvt_pk_bf16_f32 v244, v176, v177\n"
"ds_read_b64_tr_b16 a[222:223], v194 offset:30720\n"
"v_mfma_f32_16x16x32_bf16 v[140:143], a[228:231], v[68:71], v[140:143]\n"
"s_waitcnt lgkmcnt(10)\n"
"v_cvt_pk_bf16_f32 v245, v178, v179\n"
"v_mul_f32 v188, v210, v188\n"
"v_mfma_f32_16x16x32_bf16 v[140:143], a[232:235], v[80:83], v[140:143]\n"
"s_waitcnt lgkmcnt(8)\n"
"v_mul_f32 v189, v210, v189\n"
"v_mul_f32 v190, v210, v190\n"
"v_mfma_f32_16x16x32_bf16 v[140:143], a[236:239], v[92:95], v[140:143]\n"
"v_mul_f32 v191, v210, v191\n"
"v_cvt_pk_bf16_f32 v246, v188, v189\n"
"v_mfma_f32_16x16x32_bf16 v[136:139], a[236:239], v[88:91], v[136:139]\n"
"v_cvt_pk_bf16_f32 v247, v190, v191\n"
"v_mfma_f32_16x16x32_bf16 v[136:139], a[232:235], v[76:79], v[136:139]\n"
"buffer_store_dwordx4 v[244:247], v207, s[40:43], s66 offen offset:1024 sc1\n"
"v_mfma_f32_16x16x32_bf16 v[136:139], a[228:231], v[64:67], v[136:139]\n"
"v_mul_f32 v96, v208, v96\n"
"v_mul_f32 v97, v208, v97\n"
"v_mfma_f32_16x16x32_bf16 v[136:139], a[224:227], v[52:55], v[136:139]\n"
"v_mul_f32 v98, v208, v98\n"
"v_mul_f32 v99, v208, v99\n"
"v_mfma_f32_16x16x32_bf16 v[132:135], a[224:227], v[48:51], v[132:135]\n"
"ds_read_b64_tr_b16 a[224:225], v195 offset:16384\n"
"ds_read_b64_tr_b16 a[226:227], v195 offset:18432\n"
"v_mfma_f32_16x16x32_bf16 v[132:135], a[228:231], v[60:63], v[132:135]\n"
"ds_read_b64_tr_b16 a[228:229], v195 offset:20480\n"
"ds_read_b64_tr_b16 a[230:231], v195 offset:22528\n"
"v_mfma_f32_16x16x32_bf16 v[132:135], a[232:235], v[72:75], v[132:135]\n"
"ds_read_b64_tr_b16 a[232:233], v195 offset:24576\n"
"ds_read_b64_tr_b16 a[234:235], v195 offset:26624\n"
"v_mfma_f32_16x16x32_bf16 v[132:135], a[236:239], v[84:87], v[132:135]\n"
"s_waitcnt lgkmcnt(12)\n"
"ds_read_b64_tr_b16 a[236:237], v195 offset:28672\n"
"ds_read_b64_tr_b16 a[238:239], v195 offset:30720\n"
"v_mfma_f32_16x16x32_bf16 v[144:147], a[208:211], v[0:3], 0\n"
"s_waitcnt lgkmcnt(12)\n"
"v_cvt_pk_bf16_f32 v232, v96, v97\n"
"v_cvt_pk_bf16_f32 v233, v98, v99\n"
"v_mfma_f32_16x16x32_bf16 v[144:147], a[212:215], v[12:15], v[144:147]\n"
"s_waitcnt lgkmcnt(10)\n"
"v_mul_f32 v108, v208, v108\n"
"v_mul_f32 v109, v208, v109\n"
"v_mfma_f32_16x16x32_bf16 v[144:147], a[216:219], v[24:27], v[144:147]\n"
"s_waitcnt lgkmcnt(8)\n"
"v_mul_f32 v110, v208, v110\n"
"v_mul_f32 v111, v208, v111\n"
"v_mfma_f32_16x16x32_bf16 v[144:147], a[220:223], v[36:39], v[144:147]\n"
"v_cvt_pk_bf16_f32 v234, v108, v109\n"
"v_cvt_pk_bf16_f32 v235, v110, v111\n"
"v_mfma_f32_16x16x32_bf16 v[148:151], a[220:223], v[40:43], 0\n"
"s_mov_b32 s66, 32768\n"
"buffer_store_dwordx4 v[232:235], v207, s[40:43], s66 offen sc1\n"
"v_mfma_f32_16x16x32_bf16 v[148:151], a[216:219], v[28:31], v[148:151]\n"
"v_mul_f32 v100, v209, v100\n"
"v_mul_f32 v101, v209, v101\n"
"v_mfma_f32_16x16x32_bf16 v[148:151], a[212:215], v[16:19], v[148:151]\n"
"v_mul_f32 v102, v209, v102\n"
"v_mul_f32 v103, v209, v103\n"
"v_mfma_f32_16x16x32_bf16 v[148:151], a[208:211], v[4:7], v[148:151]\n"
"v_cvt_pk_bf16_f32 v236, v100, v101\n"
"v_cvt_pk_bf16_f32 v237, v102, v103\n"
"v_mfma_f32_16x16x32_bf16 v[152:155], a[208:211], v[8:11], 0\n"
"ds_read_b64_tr_b16 a[208:209], v194 offset:16896\n"
"ds_read_b64_tr_b16 a[210:211], v194 offset:18944\n"
"v_mfma_f32_16x16x32_bf16 v[152:155], a[212:215], v[20:23], v[152:155]\n"
"ds_read_b64_tr_b16 a[212:213], v194 offset:20992\n"
"ds_read_b64_tr_b16 a[214:215], v194 offset:23040\n"
"v_mfma_f32_16x16x32_bf16 v[152:155], a[216:219], v[32:35], v[152:155]\n"
"ds_read_b64_tr_b16 a[216:217], v194 offset:25088\n"
"ds_read_b64_tr_b16 a[218:219], v194 offset:27136\n"
"v_mfma_f32_16x16x32_bf16 v[152:155], a[220:223], v[44:47], v[152:155]\n"
"s_waitcnt lgkmcnt(12)\n"
"ds_read_b64_tr_b16 a[220:221], v194 offset:29184\n"
"ds_read_b64_tr_b16 a[222:223], v194 offset:31232\n"
"v_mfma_f32_16x16x32_bf16 v[164:167], a[224:227], v[8:11], 0\n"
"s_waitcnt lgkmcnt(12)\n"
"v_mul_f32 v112, v209, v112\n"
"v_mul_f32 v113, v209, v113\n"
"v_mfma_f32_16x16x32_bf16 v[164:167], a[228:231], v[20:23], v[164:167]\n"
"s_waitcnt lgkmcnt(10)\n"
"v_mul_f32 v114, v209, v114\n"
"v_mul_f32 v115, v209, v115\n"
"v_mfma_f32_16x16x32_bf16 v[164:167], a[232:235], v[32:35], v[164:167]\n"
"s_waitcnt lgkmcnt(8)\n"
"v_cvt_pk_bf16_f32 v238, v112, v113\n"
"v_cvt_pk_bf16_f32 v239, v114, v115\n"
"v_mfma_f32_16x16x32_bf16 v[164:167], a[236:239], v[44:47], v[164:167]\n"
"buffer_store_dwordx4 v[236:239], v207, s[40:43], s66 offen offset:1024 sc1\n"
"v_mfma_f32_16x16x32_bf16 v[160:163], a[236:239], v[40:43], 0\n"
"v_mul_f32 v104, v210, v104\n"
"v_mul_f32 v105, v210, v105\n"
"v_mfma_f32_16x16x32_bf16 v[160:163], a[232:235], v[28:31], v[160:163]\n"
"v_mul_f32 v106, v210, v106\n"
"v_mul_f32 v107, v210, v107\n"
"v_mfma_f32_16x16x32_bf16 v[160:163], a[228:231], v[16:19], v[160:163]\n"
"v_cvt_pk_bf16_f32 v240, v104, v105\n"
"v_cvt_pk_bf16_f32 v241, v106, v107\n"
"v_mfma_f32_16x16x32_bf16 v[160:163], a[224:227], v[4:7], v[160:163]\n"
"v_mul_f32 v116, v210, v116\n"
"v_mul_f32 v117, v210, v117\n"
"v_mfma_f32_16x16x32_bf16 v[156:159], a[224:227], v[0:3], 0\n"
"ds_read_b64_tr_b16 a[224:225], v195 offset:16896\n"
"ds_read_b64_tr_b16 a[226:227], v195 offset:18944\n"
"v_mfma_f32_16x16x32_bf16 v[156:159], a[228:231], v[12:15], v[156:159]\n"
"ds_read_b64_tr_b16 a[228:229], v195 offset:20992\n"
"ds_read_b64_tr_b16 a[230:231], v195 offset:23040\n"
"v_mfma_f32_16x16x32_bf16 v[156:159], a[232:235], v[24:27], v[156:159]\n"
"ds_read_b64_tr_b16 a[232:233], v195 offset:25088\n"
"ds_read_b64_tr_b16 a[234:235], v195 offset:27136\n"
"v_mfma_f32_16x16x32_bf16 v[156:159], a[236:239], v[36:39], v[156:159]\n"
"s_waitcnt lgkmcnt(12)\n"
"ds_read_b64_tr_b16 a[236:237], v195 offset:29184\n"
"ds_read_b64_tr_b16 a[238:239], v195 offset:31232\n"
"v_mfma_f32_16x16x32_bf16 v[168:171], a[208:211], v[0:3], 0\n"
"s_waitcnt lgkmcnt(12)\n"
"v_mul_f32 v118, v210, v118\n"
"v_mul_f32 v119, v210, v119\n"
"v_mfma_f32_16x16x32_bf16 v[168:171], a[212:215], v[12:15], v[168:171]\n"
"s_waitcnt lgkmcnt(10)\n"
"v_cvt_pk_bf16_f32 v242, v116, v117\n"
"v_cvt_pk_bf16_f32 v243, v118, v119\n"
"v_mfma_f32_16x16x32_bf16 v[168:171], a[216:219], v[24:27], v[168:171]\n"
"s_waitcnt lgkmcnt(8)\n"
"buffer_store_dwordx4 v[240:243], v207, s[40:43], s66 offen offset:2048 sc1\n"
"v_mfma_f32_16x16x32_bf16 v[168:171], a[220:223], v[36:39], v[168:171]\n"
"v_mul_f32 v120, v208, v120\n"
"v_mul_f32 v121, v208, v121\n"
"v_mfma_f32_16x16x32_bf16 v[172:175], a[220:223], v[40:43], 0\n"
"v_mul_f32 v122, v208, v122\n"
"v_mul_f32 v123, v208, v123\n"
"v_mfma_f32_16x16x32_bf16 v[172:175], a[216:219], v[28:31], v[172:175]\n"
"v_cvt_pk_bf16_f32 v244, v120, v121\n"
"v_cvt_pk_bf16_f32 v245, v122, v123\n"
"v_mfma_f32_16x16x32_bf16 v[172:175], a[212:215], v[16:19], v[172:175]\n"
"s_waitcnt vmcnt(39)\n"
"s_barrier\n"
"v_mul_f32 v132, v208, v132\n"
"v_mfma_f32_16x16x32_bf16 v[172:175], a[208:211], v[4:7], v[172:175]\n"
"v_mul_f32 v133, v208, v133\n"
"v_mul_f32 v134, v208, v134\n"
"v_mfma_f32_16x16x32_bf16 v[176:179], a[208:211], v[8:11], 0\n"
"ds_read_b64_tr_b16 a[208:209], v194 offset:32768\n"
"ds_read_b64_tr_b16 a[210:211], v194 offset:34816\n"
"v_mfma_f32_16x16x32_bf16 v[176:179], a[212:215], v[20:23], v[176:179]\n"
"ds_read_b64_tr_b16 a[212:213], v194 offset:36864\n"
"ds_read_b64_tr_b16 a[214:215], v194 offset:38912\n"
"v_mfma_f32_16x16x32_bf16 v[176:179], a[216:219], v[32:35], v[176:179]\n"
"ds_read_b64_tr_b16 a[216:217], v194 offset:40960\n"
"ds_read_b64_tr_b16 a[218:219], v194 offset:43008\n"
"v_mfma_f32_16x16x32_bf16 v[176:179], a[220:223], v[44:47], v[176:179]\n"
"s_waitcnt lgkmcnt(12)\n"
"ds_read_b64_tr_b16 a[220:221], v194 offset:45056\n"
"ds_read_b64_tr_b16 a[222:223], v194 offset:47104\n"
"v_mfma_f32_16x16x32_bf16 v[188:191], a[224:227], v[8:11], 0\n"
"s_waitcnt lgkmcnt(12)\n"
"v_mul_f32 v135, v208, v135\n"
"v_cvt_pk_bf16_f32 v246, v132, v133\n"
"v_mfma_f32_16x16x32_bf16 v[188:191], a[228:231], v[20:23], v[188:191]\n"
"s_waitcnt lgkmcnt(10)\n"
"v_cvt_pk_bf16_f32 v247, v134, v135\n"
"v_mfma_f32_16x16x32_bf16 v[188:191], a[232:235], v[32:35], v[188:191]\n"
"s_waitcnt lgkmcnt(8)\n"
"buffer_store_dwordx4 v[244:247], v207, s[40:43], s66 offen offset:3072 sc1\n"
"v_mfma_f32_16x16x32_bf16 v[188:191], a[236:239], v[44:47], v[188:191]\n"
"v_mul_f32 v124, v209, v124\n"
"v_mul_f32 v125, v209, v125\n"
"v_mfma_f32_16x16x32_bf16 v[184:187], a[236:239], v[40:43], 0\n"
"v_mul_f32 v126, v209, v126\n"
"v_mul_f32 v127, v209, v127\n"
"v_mfma_f32_16x16x32_bf16 v[184:187], a[232:235], v[28:31], v[184:187]\n"
"v_cvt_pk_bf16_f32 v232, v124, v125\n"
"v_cvt_pk_bf16_f32 v233, v126, v127\n"
"v_mfma_f32_16x16x32_bf16 v[184:187], a[228:231], v[16:19], v[184:187]\n"
"v_mul_f32 v136, v209, v136\n"
"v_mul_f32 v137, v209, v137\n"
"v_mfma_f32_16x16x32_bf16 v[184:187], a[224:227], v[4:7], v[184:187]\n"
"v_mul_f32 v138, v209, v138\n"
"v_mul_f32 v139, v209, v139\n"
"v_mfma_f32_16x16x32_bf16 v[180:183], a[224:227], v[0:3], 0\n"
"ds_read_b64_tr_b16 a[224:225], v195 offset:32768\n"
"ds_read_b64_tr_b16 a[226:227], v195 offset:34816\n"
"v_mfma_f32_16x16x32_bf16 v[180:183], a[228:231], v[12:15], v[180:183]\n"
"ds_read_b64_tr_b16 a[228:229], v195 offset:36864\n"
"ds_read_b64_tr_b16 a[230:231], v195 offset:38912\n"
"v_mfma_f32_16x16x32_bf16 v[180:183], a[232:235], v[24:27], v[180:183]\n"
"ds_read_b64_tr_b16 a[232:233], v195 offset:40960\n"
"ds_read_b64_tr_b16 a[234:235], v195 offset:43008\n"
"v_mfma_f32_16x16x32_bf16 v[180:183], a[236:239], v[36:39], v[180:183]\n"
"s_waitcnt lgkmcnt(12)\n"
"ds_read_b64_tr_b16 a[236:237], v195 offset:45056\n"
"ds_read_b64_tr_b16 a[238:239], v195 offset:47104\n"
"v_mfma_f32_16x16x32_bf16 v[144:147], a[208:211], v[48:51], v[144:147]\n"
"s_waitcnt lgkmcnt(12)\n"
"v_cvt_pk_bf16_f32 v234, v136, v137\n"
"v_cvt_pk_bf16_f32 v235, v138, v139\n"
"v_mfma_f32_16x16x32_bf16 v[144:147], a[212:215], v[60:63], v[144:147]\n"
"s_waitcnt lgkmcnt(10)\n"
"s_mov_b32 s66, 36864\n"
"v_mfma_f32_16x16x32_bf16 v[144:147], a[216:219], v[72:75], v[144:147]\n"
"s_waitcnt lgkmcnt(8)\n"
"buffer_store_dwordx4 v[232:235], v207, s[40:43], s66 offen sc1\n"
"v_mfma_f32_16x16x32_bf16 v[144:147], a[220:223], v[84:87], v[144:147]\n"
"v_mul_f32 v128, v210, v128\n"
"v_mul_f32 v129, v210, v129\n"
"v_mfma_f32_16x16x32_bf16 v[148:151], a[220:223], v[88:91], v[148:151]\n"
"v_mul_f32 v130, v210, v130\n"
"v_mul_f32 v131, v210, v131\n"
"v_mfma_f32_16x16x32_bf16 v[148:151], a[216:219], v[76:79], v[148:151]\n"
"v_cvt_pk_bf16_f32 v236, v128, v129\n"
"v_cvt_pk_bf16_f32 v237, v130, v131\n"
"v_mfma_f32_16x16x32_bf16 v[148:151], a[212:215], v[64:67], v[148:151]\n"
"v_mul_f32 v140, v210, v140\n"
"v_mul_f32 v141, v210, v141\n"
"v_mfma_f32_16x16x32_bf16 v[148:151], a[208:211], v[52:55], v[148:151]\n"
"v_mul_f32 v142, v210, v142\n"
"v_mul_f32 v143, v210, v143\n"
"v_mfma_f32_16x16x32_bf16 v[152:155], a[208:211], v[56:59], v[152:155]\n"
"ds_read_b64_tr_b16 a[208:209], v194 offset:33280\n"
"ds_read_b64_tr_b16 a[210:211], v194 offset:35328\n"
"v_mfma_f32_16x16x32_bf16 v[152:155], a[212:215], v[68:71], v[152:155]\n"
"ds_read_b64_tr_b16 a[212:213], v194 offset:37376\n"
"ds_read_b64_tr_b16 a[214:215], v194 offset:39424\n"
"v_mfma_f32_16x16x32_bf16 v[152:155], a[216:219], v[80:83], v[152:155]\n"
"ds_read_b64_tr_b16 a[216:217], v194 offset:41472\n"
"ds_read_b64_tr_b16 a[218:219], v194 offset:43520\n"
"v_mfma_f32_16x16x32_bf16 v[152:155], a[220:223], v[92:95], v[152:155]\n"
"s_waitcnt lgkmcnt(12)\n"
"ds_read_b64_tr_b16 a[220:221], v194 offset:45568\n"
"ds_read_b64_tr_b16 a[222:223], v194 offset:47616\n"
"v_mfma_f32_16x16x32_bf16 v[164:167], a[224:227], v[56:59], v[164:167]\n"
"s_waitcnt lgkmcnt(12)\n"
"v_cvt_pk_bf16_f32 v238, v140, v141\n"
"v_cvt_pk_bf16_f32 v239, v142, v143\n"
"v_mfma_f32_16x16x32_bf16 v[164:167], a[228:231], v[68:71], v[164:167]\n"
"s_waitcnt lgkmcnt(10)\n"
"buffer_store_dwordx4 v[236:239], v207, s[40:43], s66 offen offset:1024 sc1\n"
"v_mfma_f32_16x16x32_bf16 v[164:167], a[232:235], v[80:83], v[164:167]\n"
"s_waitcnt lgkmcnt(8)\n"
"v_mul_f32 v144, v208, v144\n"
"v_mul_f32 v145, v208, v145\n"
"v_mfma_f32_16x16x32_bf16 v[164:167], a[236:239], v[92:95], v[164:167]\n"
"v_mul_f32 v146, v208, v146\n"
"v_mul_f32 v147, v208, v147\n"
"v_mfma_f32_16x16x32_bf16 v[160:163], a[236:239], v[88:91], v[160:163]\n"
"v_cvt_pk_bf16_f32 v240, v144, v145\n"
"v_cvt_pk_bf16_f32 v241, v146, v147\n"
"v_mfma_f32_16x16x32_bf16 v[160:163], a[232:235], v[76:79], v[160:163]\n"
"v_mfma_f32_16x16x32_bf16 v[160:163], a[228:231], v[64:67], v[160:163]\n"
"v_mfma_f32_16x16x32_bf16 v[160:163], a[224:227], v[52:55], v[160:163]\n"
"v_mfma_f32_16x16x32_bf16 v[156:159], a[224:227], v[48:51], v[156:159]\n"
"ds_read_b64_tr_b16 a[224:225], v195 offset:33280\n"
"ds_read_b64_tr_b16 a[226:227], v195 offset:35328\n"
"v_mfma_f32_16x16x32_bf16 v[156:159], a[228:231], v[60:63], v[156:159]\n"
"ds_read_b64_tr_b16 a[228:229], v195 offset:37376\n"
"ds_read_b64_tr_b16 a[230:231], v195 offset:39424\n"
"v_mfma_f32_16x16x32_bf16 v[156:159], a[232:235], v[72:75], v[156:159]\n"
"ds_read_b64_tr_b16 a[232:233], v195 offset:41472\n"
"ds_read_b64_tr_b16 a[234:235], v195 offset:43520\n"
"v_mfma_f32_16x16x32_bf16 v[156:159], a[236:239], v[84:87], v[156:159]\n"
"s_waitcnt lgkmcnt(12)\n"
"ds_read_b64_tr_b16 a[236:237], v195 offset:45568\n"
"ds_read_b64_tr_b16 a[238:239], v195 offset:47616\n"
"v_mfma_f32_16x16x32_bf16 v[168:171], a[208:211], v[48:51], v[168:171]\n"
"s_waitcnt lgkmcnt(12)\n"
"v_mfma_f32_16x16x32_bf16 v[168:171], a[212:215], v[60:63], v[168:171]\n"
"s_waitcnt lgkmcnt(10)\n"
"v_mfma_f32_16x16x32_bf16 v[168:171], a[216:219], v[72:75], v[168:171]\n"
"s_waitcnt lgkmcnt(8)\n"
"v_mul_f32 v156, v208, v156\n"
"v_mul_f32 v157, v208, v157\n"
"v_mfma_f32_16x16x32_bf16 v[168:171], a[220:223], v[84:87], v[168:171]\n"
"v_mul_f32 v158, v208, v158\n"
"v_mul_f32 v159, v208, v159\n"
"v_mfma_f32_16x16x32_bf16 v[172:175], a[220:223], v[88:91], v[172:175]\n"
"v_cvt_pk_bf16_f32 v242, v156, v157\n"
"v_cvt_pk_bf16_f32 v243, v158, v159\n"
"v_mfma_f32_16x16x32_bf16 v[172:175], a[216:219], v[76:79], v[172:175]\n"
"s_mov_b32 s66, 49152\n"
"buffer_store_dwordx4 v[240:243], v207, s[40:43], s66 offen sc1\n"
"v_mfma_f32_16x16x32_bf16 v[172:175], a[212:215], v[64:67], v[172:175]\n"
"s_waitcnt vmcnt(12)\n"
"s_barrier\n"
"v_mul_f32 v148, v209, v148\n"
"v_mfma_f32_16x16x32_bf16 v[172:175], a[208:211], v[52:55], v[172:175]\n"
"v_mul_f32 v149, v209, v149\n"
"v_mul_f32 v150, v209, v150\n"
"v_mfma_f32_16x16x32_bf16 v[176:179], a[208:211], v[56:59], v[176:179]\n"
"ds_read_b64_tr_b16 a[208:209], v194 offset:49152\n"
"ds_read_b64_tr_b16 a[210:211], v194 offset:51200\n"
"v_mfma_f32_16x16x32_bf16 v[176:179], a[212:215], v[68:71], v[176:179]\n"
"ds_read_b64_tr_b16 a[212:213], v194 offset:53248\n"
"ds_read_b64_tr_b16 a[214:215], v194 offset:55296\n"
"v_mfma_f32_16x16x32_bf16 v[176:179], a[216:219], v[80:83], v[176:179]\n"
"ds_read_b64_tr_b16 a[216:217], v194 offset:57344\n"
"ds_read_b64_tr_b16 a[218:219], v194 offset:59392\n"
"v_mfma_f32_16x16x32_bf16 v[176:179], a[220:223], v[92:95], v[176:179]\n"
"s_waitcnt lgkmcnt(12)\n"
"ds_read_b64_tr_b16 a[220:221], v194 offset:61440\n"
"ds_read_b64_tr_b16 a[222:223], v194 offset:63488\n"
"v_mfma_f32_16x16x32_bf16 v[188:191], a[224:227], v[56:59], v[188:191]\n"
"s_waitcnt lgkmcnt(12)\n"
"v_mul_f32 v151, v209, v151\n"
"v_cvt_pk_bf16_f32 v244, v148, v149\n"
"v_mfma_f32_16x16x32_bf16 v[188:191], a[228:231], v[68:71], v[188:191]\n"
"s_waitcnt lgkmcnt(10)\n"
"v_cvt_pk_bf16_f32 v245, v150, v151\n"
"v_mul_f32 v160, v209, v160\n"
"v_mfma_f32_16x16x32_bf16 v[188:191], a[232:235], v[80:83], v[188:191]\n"
"s_waitcnt lgkmcnt(8)\n"
"v_mul_f32 v161, v209, v161\n"
"v_mul_f32 v162, v209, v162\n"
"v_mfma_f32_16x16x32_bf16 v[188:191], a[236:239], v[92:95], v[188:191]\n"
"v_mul_f32 v163, v209, v163\n"
"v_cvt_pk_bf16_f32 v246, v160, v161\n"
"v_mfma_f32_16x16x32_bf16 v[184:187], a[236:239], v[88:91], v[184:187]\n"
"v_cvt_pk_bf16_f32 v247, v162, v163\n"
"v_mfma_f32_16x16x32_bf16 v[184:187], a[232:235], v[76:79], v[184:187]\n"
"buffer_store_dwordx4 v[244:247], v207, s[40:43], s66 offen offset:1024 sc1\n"
"v_mfma_f32_16x16x32_bf16 v[184:187], a[228:231], v[64:67], v[184:187]\n"
"v_mul_f32 v152, v210, v152\n"
"v_mul_f32 v153, v210, v153\n"
"v_mfma_f32_16x16x32_bf16 v[184:187], a[224:227], v[52:55], v[184:187]\n"
"v_mul_f32 v154, v210, v154\n"
"v_mul_f32 v155, v210, v155\n"
"v_mfma_f32_16x16x32_bf16 v[180:183], a[224:227], v[48:51], v[180:183]\n"
"ds_read_b64_tr_b16 a[224:225], v195 offset:49152\n"
"ds_read_b64_tr_b16 a[226:227], v195 offset:51200\n"
"v_mfma_f32_16x16x32_bf16 v[180:183], a[228:231], v[60:63], v[180:183]\n"
"ds_read_b64_tr_b16 a[228:229], v195 offset:53248\n"
"ds_read_b64_tr_b16 a[230:231], v195 offset:55296\n"
"v_mfma_f32_16x16x32_bf16 v[180:183], a[232:235], v[72:75], v[180:183]\n"
"ds_read_b64_tr_b16 a[232:233], v195 offset:57344\n"
"ds_read_b64_tr_b16 a[234:235], v195 offset:59392\n"
"v_mfma_f32_16x16x32_bf16 v[180:183], a[236:239], v[84:87], v[180:183]\n"
"s_waitcnt lgkmcnt(12)\n"
"ds_read_b64_tr_b16 a[236:237], v195 offset:61440\n"
"ds_read_b64_tr_b16 a[238:239], v195 offset:63488\n"
"v_mfma_f32_16x16x32_bf16 v[96:99], a[208:211], v[0:3], 0\n"
"s_waitcnt lgkmcnt(12)\n"
"v_cvt_pk_bf16_f32 v232, v152, v153\n"
"v_cvt_pk_bf16_f32 v233, v154, v155\n"
"v_mfma_f32_16x16x32_bf16 v[96:99], a[212:215], v[12:15], v[96:99]\n"
"s_waitcnt lgkmcnt(10)\n"
"v_mul_f32 v164, v210, v164\n"
"v_mul_f32 v165, v210, v165\n"
"v_mfma_f32_16x16x32_bf16 v[96:99], a[216:219], v[24:27], v[96:99]\n"
"s_waitcnt lgkmcnt(8)\n"
"v_mul_f32 v166, v210, v166\n"
"v_mul_f32 v167, v210, v167\n"
"v_mfma_f32_16x16x32_bf16 v[96:99], a[220:223], v[36:39], v[96:99]\n"
"v_cvt_pk_bf16_f32 v234, v164, v165\n"
"v_cvt_pk_bf16_f32 v235, v166, v167\n"
"v_mfma_f32_16x16x32_bf16 v[100:103], a[220:223], v[40:43], 0\n"
"buffer_store_dwordx4 v[232:235], v207, s[40:43], s66 offen offset:2048 sc1\n"
"v_mfma_f32_16x16x32_bf16 v[100:103], a[216:219], v[28:31], v[100:103]\n"
"v_mul_f32 v168, v208, v168\n"
"v_mul_f32 v169, v208, v169\n"
"v_mfma_f32_16x16x32_bf16 v[100:103], a[212:215], v[16:19], v[100:103]\n"
"v_mul_f32 v170, v208, v170\n"
"v_mul_f32 v171, v208, v171\n"
"v_mfma_f32_16x16x32_bf16 v[100:103], a[208:211], v[4:7], v[100:103]\n"
"v_cvt_pk_bf16_f32 v236, v168, v169\n"
"v_cvt_pk_bf16_f32 v237, v170, v171\n"
"v_mfma_f32_16x16x32_bf16 v[104:107], a[208:211], v[8:11], 0\n"
"ds_read_b64_tr_b16 a[208:209], v194 offset:49664\n"
"ds_read_b64_tr_b16 a[210:211], v194 offset:51712\n"
"v_mfma_f32_16x16x32_bf16 v[104:107], a[212:215], v[20:23], v[104:107]\n"
"ds_read_b64_tr_b16 a[212:213], v194 offset:53760\n"
"ds_read_b64_tr_b16 a[214:215], v194 offset:55808\n"
"v_mfma_f32_16x16x32_bf16 v[104:107], a[216:219], v[32:35], v[104:107]\n"
"ds_read_b64_tr_b16 a[216:217], v194 offset:57856\n"
"ds_read_b64_tr_b16 a[218:219], v194 offset:59904\n"
"v_mfma_f32_16x16x32_bf16 v[104:107], a[220:223], v[44:47], v[104:107]\n"
"s_waitcnt lgkmcnt(12)\n"
"ds_read_b64_tr_b16 a[220:221], v194 offset:61952\n"
"ds_read_b64_tr_b16 a[222:223], v194 offset:64000\n"
"v_mfma_f32_16x16x32_bf16 v[116:119], a[224:227], v[8:11], 0\n"
"s_waitcnt lgkmcnt(12)\n"
"v_mul_f32 v180, v208, v180\n"
"v_mul_f32 v181, v208, v181\n"
"v_mfma_f32_16x16x32_bf16 v[116:119], a[228:231], v[20:23], v[116:119]\n"
"s_waitcnt lgkmcnt(10)\n"
"v_mul_f32 v182, v208, v182\n"
"v_mul_f32 v183, v208, v183\n"
"v_mfma_f32_16x16x32_bf16 v[116:119], a[232:235], v[32:35], v[116:119]\n"
"s_waitcnt lgkmcnt(8)\n"
"v_cvt_pk_bf16_f32 v238, v180, v181\n"
"v_cvt_pk_bf16_f32 v239, v182, v183\n"
"v_mfma_f32_16x16x32_bf16 v[116:119], a[236:239], v[44:47], v[116:119]\n"
"buffer_store_dwordx4 v[236:239], v207, s[40:43], s66 offen offset:3072 sc1\n"
"v_mfma_f32_16x16x32_bf16 v[112:115], a[236:239], v[40:43], 0\n"
"v_mul_f32 v172, v209, v172\n"
"v_mul_f32 v173, v209, v173\n"
"v_mfma_f32_16x16x32_bf16 v[112:115], a[232:235], v[28:31], v[112:115]\n"
"v_mul_f32 v174, v209, v174\n"
"v_mul_f32 v175, v209, v175\n"
"v_mfma_f32_16x16x32_bf16 v[112:115], a[228:231], v[16:19], v[112:115]\n"
"v_cvt_pk_bf16_f32 v240, v172, v173\n"
"v_cvt_pk_bf16_f32 v241, v174, v175\n"
"v_mfma_f32_16x16x32_bf16 v[112:115], a[224:227], v[4:7], v[112:115]\n"
"v_mul_f32 v184, v209, v184\n"
"v_mul_f32 v185, v209, v185\n"
"v_mfma_f32_16x16x32_bf16 v[108:111], a[224:227], v[0:3], 0\n"
"ds_read_b64_tr_b16 a[224:225], v195 offset:49664\n"
"ds_read_b64_tr_b16 a[226:227], v195 offset:51712\n"
"v_mfma_f32_16x16x32_bf16 v[108:111], a[228:231], v[12:15], v[108:111]\n"
"ds_read_b64_tr_b16 a[228:229], v195 offset:53760\n"
"ds_read_b64_tr_b16 a[230:231], v195 offset:55808\n"
"v_mfma_f32_16x16x32_bf16 v[108:111], a[232:235], v[24:27], v[108:111]\n"
"ds_read_b64_tr_b16 a[232:233], v195 offset:57856\n"
"ds_read_b64_tr_b16 a[234:235], v195 offset:59904\n"
"v_mfma_f32_16x16x32_bf16 v[108:111], a[236:239], v[36:39], v[108:111]\n"
"s_waitcnt lgkmcnt(12)\n"
"ds_read_b64_tr_b16 a[236:237], v195 offset:61952\n"
"ds_read_b64_tr_b16 a[238:239], v195 offset:64000\n"
"v_mfma_f32_16x16x32_bf16 v[120:123], a[208:211], v[0:3], 0\n"
"s_waitcnt lgkmcnt(12)\n"
"v_mul_f32 v186, v209, v186\n"
"v_mul_f32 v187, v209, v187\n"
"v_mfma_f32_16x16x32_bf16 v[120:123], a[212:215], v[12:15], v[120:123]\n"
"s_waitcnt lgkmcnt(10)\n"
"v_cvt_pk_bf16_f32 v242, v184, v185\n"
"v_cvt_pk_bf16_f32 v243, v186, v187\n"
"v_mfma_f32_16x16x32_bf16 v[120:123], a[216:219], v[24:27], v[120:123]\n"
"s_waitcnt lgkmcnt(8)\n"
"s_mov_b32 s66, 53248\n"
"v_mfma_f32_16x16x32_bf16 v[120:123], a[220:223], v[36:39], v[120:123]\n"
"buffer_store_dwordx4 v[240:243], v207, s[40:43], s66 offen sc1\n"
"v_mfma_f32_16x16x32_bf16 v[124:127], a[220:223], v[40:43], 0\n"
"v_mul_f32 v176, v210, v176\n"
"v_mul_f32 v177, v210, v177\n"
"v_mfma_f32_16x16x32_bf16 v[124:127], a[216:219], v[28:31], v[124:127]\n"
"v_mul_f32 v178, v210, v178\n"
"v_mul_f32 v179, v210, v179\n"
"v_mfma_f32_16x16x32_bf16 v[124:127], a[212:215], v[16:19], v[124:127]\n"
"v_cvt_pk_bf16_f32 v244, v176, v177\n"
"v_cvt_pk_bf16_f32 v245, v178, v179\n"
"v_mfma_f32_16x16x32_bf16 v[124:127], a[208:211], v[4:7], v[124:127]\n"
"v_mul_f32 v188, v210, v188\n"
"v_mul_f32 v189, v210, v189\n"
"v_mfma_f32_16x16x32_bf16 v[128:131], a[208:211], v[8:11], 0\n"
"ds_read_b64_tr_b16 a[208:209], v196 offset:0\n"
"ds_read_b64_tr_b16 a[210:211], v196 offset:2048\n"
"v_mfma_f32_16x16x32_bf16 v[128:131], a[212:215], v[20:23], v[128:131]\n"
"ds_read_b64_tr_b16 a[212:213], v196 offset:4096\n"
"ds_read_b64_tr_b16 a[214:215], v196 offset:6144\n"
"v_mfma_f32_16x16x32_bf16 v[128:131], a[216:219], v[32:35], v[128:131]\n"
"ds_read_b64_tr_b16 a[216:217], v196 offset:8192\n"
"ds_read_b64_tr_b16 a[218:219], v196 offset:10240\n"
"v_mfma_f32_16x16x32_bf16 v[128:131], a[220:223], v[44:47], v[128:131]\n"
"s_waitcnt lgkmcnt(12)\n"
"ds_read_b64_tr_b16 a[220:221], v196 offset:12288\n"
"ds_read_b64_tr_b16 a[222:223], v196 offset:14336\n"
"v_mfma_f32_16x16x32_bf16 v[140:143], a[224:227], v[8:11], 0\n"
"s_waitcnt lgkmcnt(12)\n"
"v_mul_f32 v190, v210, v190\n"
"v_mul_f32 v191, v210, v191\n"
"v_mfma_f32_16x16x32_bf16 v[140:143], a[228:231], v[20:23], v[140:143]\n"
"s_waitcnt lgkmcnt(10)\n"
"v_cvt_pk_bf16_f32 v246, v188, v189\n"
"v_cvt_pk_bf16_f32 v247, v190, v191\n"
"v_mfma_f32_16x16x32_bf16 v[140:143], a[232:235], v[32:35], v[140:143]\n"
"s_waitcnt lgkmcnt(8)\n"
"buffer_store_dwordx4 v[244:247], v207, s[40:43], s66 offen offset:1024 sc1\n"
"v_mfma_f32_16x16x32_bf16 v[140:143], a[236:239], v[44:47], v[140:143]\n"
"v_mfma_f32_16x16x32_bf16 v[136:139], a[236:239], v[40:43], 0\n"
"v_mfma_f32_16x16x32_bf16 v[136:139], a[232:235], v[28:31], v[136:139]\n"
"v_mfma_f32_16x16x32_bf16 v[136:139], a[228:231], v[16:19], v[136:139]\n"
"v_mfma_f32_16x16x32_bf16 v[136:139], a[224:227], v[4:7], v[136:139]\n"
"v_mfma_f32_16x16x32_bf16 v[132:135], a[224:227], v[0:3], 0\n"
"ds_read_b64_tr_b16 a[224:225], v197 offset:0\n"
"ds_read_b64_tr_b16 a[226:227], v197 offset:2048\n"
"v_mfma_f32_16x16x32_bf16 v[132:135], a[228:231], v[12:15], v[132:135]\n"
"ds_read_b64_tr_b16 a[228:229], v197 offset:4096\n"
"ds_read_b64_tr_b16 a[230:231], v197 offset:6144\n"
"v_mfma_f32_16x16x32_bf16 v[132:135], a[232:235], v[24:27], v[132:135]\n"
"ds_read_b64_tr_b16 a[232:233], v197 offset:8192\n"
"ds_read_b64_tr_b16 a[234:235], v197 offset:10240\n"
"v_mfma_f32_16x16x32_bf16 v[132:135], a[236:239], v[36:39], v[132:135]\n"
"s_waitcnt lgkmcnt(12)\n"
"ds_read_b64_tr_b16 a[236:237], v197 offset:12288\n"
"ds_read_b64_tr_b16 a[238:239], v197 offset:14336\n"
"v_mfma_f32_16x16x32_bf16 v[96:99], a[208:211], v[48:51], v[96:99]\n"
"s_waitcnt lgkmcnt(12)\n"
"v_mfma_f32_16x16x32_bf16 v[96:99], a[212:215], v[60:63], v[96:99]\n"
"s_waitcnt lgkmcnt(10)\n"
"v_mfma_f32_16x16x32_bf16 v[96:99], a[216:219], v[72:75], v[96:99]\n"
"s_waitcnt lgkmcnt(8)\n"
"v_mfma_f32_16x16x32_bf16 v[96:99], a[220:223], v[84:87], v[96:99]\n"
"v_mfma_f32_16x16x32_bf16 v[100:103], a[220:223], v[88:91], v[100:103]\n"
"v_mfma_f32_16x16x32_bf16 v[100:103], a[216:219], v[76:79], v[100:103]\n"
"v_mfma_f32_16x16x32_bf16 v[100:103], a[212:215], v[64:67], v[100:103]\n"
"s_nop 4\n"
"v_mul_f32 v96, v208, v96\n"
"v_mul_f32 v97, v208, v97\n"
"v_mfma_f32_16x16x32_bf16 v[100:103], a[208:211], v[52:55], v[100:103]\n"
"v_mul_f32 v98, v208, v98\n"
"v_mul_f32 v99, v208, v99\n"
"v_mfma_f32_16x16x32_bf16 v[104:107], a[208:211], v[56:59], v[104:107]\n"
"ds_read_b64_tr_b16 a[208:209], v196 offset:512\n"
"ds_read_b64_tr_b16 a[210:211], v196 offset:2560\n"
"v_mfma_f32_16x16x32_bf16 v[104:107], a[212:215], v[68:71], v[104:107]\n"
"ds_read_b64_tr_b16 a[212:213], v196 offset:4608\n"
"ds_read_b64_tr_b16 a[214:215], v196 offset:6656\n"
"v_mfma_f32_16x16x32_bf16 v[104:107], a[216:219], v[80:83], v[104:107]\n"
"ds_read_b64_tr_b16 a[216:217], v196 offset:8704\n"
"ds_read_b64_tr_b16 a[218:219], v196 offset:10752\n"
"v_mfma_f32_16x16x32_bf16 v[104:107], a[220:223], v[92:95], v[104:107]\n"
"s_waitcnt lgkmcnt(12)\n"
"ds_read_b64_tr_b16 a[220:221], v196 offset:12800\n"
"ds_read_b64_tr_b16 a[222:223], v196 offset:14848\n"
"v_mfma_f32_16x16x32_bf16 v[116:119], a[224:227], v[56:59], v[116:119]\n"
"s_waitcnt lgkmcnt(12)\n"
"v_cvt_pk_bf16_f32 v232, v96, v97\n"
"v_cvt_pk_bf16_f32 v233, v98, v99\n"
"v_mfma_f32_16x16x32_bf16 v[116:119], a[228:231], v[68:71], v[116:119]\n"
"s_waitcnt lgkmcnt(10)\n"
"v_mfma_f32_16x16x32_bf16 v[116:119], a[232:235], v[80:83], v[116:119]\n"
"s_waitcnt lgkmcnt(8)\n"
"v_mfma_f32_16x16x32_bf16 v[116:119], a[236:239], v[92:95], v[116:119]\n"
"v_mfma_f32_16x16x32_bf16 v[112:115], a[236:239], v[88:91], v[112:115]\n"
"v_mfma_f32_16x16x32_bf16 v[112:115], a[232:235], v[76:79], v[112:115]\n"
"v_mfma_f32_16x16x32_bf16 v[112:115], a[228:231], v[64:67], v[112:115]\n"
"v_mfma_f32_16x16x32_bf16 v[112:115], a[224:227], v[52:55], v[112:115]\n"
"v_mfma_f32_16x16x32_bf16 v[108:111], a[224:227], v[48:51], v[108:111]\n"
"ds_read_b64_tr_b16 a[224:225], v197 offset:512\n"
"ds_read_b64_tr_b16 a[226:227], v197 offset:2560\n"
"v_mfma_f32_16x16x32_bf16 v[108:111], a[228:231], v[60:63], v[108:111]\n"
"ds_read_b64_tr_b16 a[228:229], v197 offset:4608\n"
"ds_read_b64_tr_b16 a[230:231], v197 offset:6656\n"
"v_mfma_f32_16x16x32_bf16 v[108:111], a[232:235], v[72:75], v[108:111]\n"
"ds_read_b64_tr_b16 a[232:233], v197 offset:8704\n"
"ds_read_b64_tr_b16 a[234:235], v197 offset:10752\n"
"v_mfma_f32_16x16x32_bf16 v[108:111], a[236:239], v[84:87], v[108:111]\n"
"s_waitcnt lgkmcnt(12)\n"
"ds_read_b64_tr_b16 a[236:237], v197 offset:12800\n"
"ds_read_b64_tr_b16 a[238:239], v197 offset:14848\n"
"v_mfma_f32_16x16x32_bf16 v[120:123], a[208:211], v[48:51], v[120:123]\n"
"s_waitcnt lgkmcnt(12)\n"
"v_mfma_f32_16x16x32_bf16 v[120:123], a[212:215], v[60:63], v[120:123]\n"
"s_waitcnt lgkmcnt(10)\n"
"v_mfma_f32_16x16x32_bf16 v[120:123], a[216:219], v[72:75], v[120:123]\n"
"s_waitcnt lgkmcnt(8)\n"
"v_mul_f32 v108, v208, v108\n"
"v_mul_f32 v109, v208, v109\n"
"v_mfma_f32_16x16x32_bf16 v[120:123], a[220:223], v[84:87], v[120:123]\n"
"v_mul_f32 v110, v208, v110\n"
"v_mul_f32 v111, v208, v111\n"
"v_mfma_f32_16x16x32_bf16 v[124:127], a[220:223], v[88:91], v[124:127]\n"
"v_cvt_pk_bf16_f32 v234, v108, v109\n"
"v_cvt_pk_bf16_f32 v235, v110, v111\n"
"v_mfma_f32_16x16x32_bf16 v[124:127], a[216:219], v[76:79], v[124:127]\n"
"s_mov_b32 s66, 65536\n"
"buffer_store_dwordx4 v[232:235], v207, s[40:43], s66 offen sc1\n"
"v_mfma_f32_16x16x32_bf16 v[124:127], a[212:215], v[64:67], v[124:127]\n"
"v_mul_f32 v100, v209, v100\n"
"v_mul_f32 v101, v209, v101\n"
"v_mfma_f32_16x16x32_bf16 v[124:127], a[208:211], v[52:55], v[124:127]\n"
"v_mul_f32 v102, v209, v102\n"
"v_mul_f32 v103, v209, v103\n"
"v_mfma_f32_16x16x32_bf16 v[128:131], a[208:211], v[56:59], v[128:131]\n"
"ds_read_b64_tr_b16 a[208:209], v196 offset:16384\n"
"ds_read_b64_tr_b16 a[210:211], v196 offset:18432\n"
"v_mfma_f32_16x16x32_bf16 v[128:131], a[212:215], v[68:71], v[128:131]\n"
"ds_read_b64_tr_b16 a[212:213], v196 offset:20480\n"
"ds_read_b64_tr_b16 a[214:215], v196 offset:22528\n"
"v_mfma_f32_16x16x32_bf16 v[128:131], a[216:219], v[80:83], v[128:131]\n"
"ds_read_b64_tr_b16 a[216:217], v196 offset:24576\n"
"ds_read_b64_tr_b16 a[218:219], v196 offset:26624\n"
"v_mfma_f32_16x16x32_bf16 v[128:131], a[220:223], v[92:95], v[128:131]\n"
"s_waitcnt lgkmcnt(12)\n"
"ds_read_b64_tr_b16 a[220:221], v196 offset:28672\n"
"ds_read_b64_tr_b16 a[222:223], v196 offset:30720\n"
"v_mfma_f32_16x16x32_bf16 v[140:143], a[224:227], v[56:59], v[140:143]\n"
"s_waitcnt lgkmcnt(12)\n"
"v_cvt_pk_bf16_f32 v236, v100, v101\n"
"v_cvt_pk_bf16_f32 v237, v102, v103\n"
"v_mfma_f32_16x16x32_bf16 v[140:143], a[228:231], v[68:71], v[140:143]\n"
"s_waitcnt lgkmcnt(10)\n"
"v_mul_f32 v112, v209, v112\n"
"v_mul_f32 v113, v209, v113\n"
"v_mfma_f32_16x16x32_bf16 v[140:143], a[232:235], v[80:83], v[140:143]\n"
"s_waitcnt lgkmcnt(8)\n"
"v_mul_f32 v114, v209, v114\n"
"v_mul_f32 v115, v209, v115\n"
"v_mfma_f32_16x16x32_bf16 v[140:143], a[236:239], v[92:95], v[140:143]\n"
"v_cvt_pk_bf16_f32 v238, v112, v113\n"
"v_cvt_pk_bf16_f32 v239, v114, v115\n"
"v_mfma_f32_16x16x32_bf16 v[136:139], a[236:239], v[88:91], v[136:139]\n"
"buffer_store_dwordx4 v[236:239], v207, s[40:43], s66 offen offset:1024 sc1\n"
"v_mfma_f32_16x16x32_bf16 v[136:139], a[232:235], v[76:79], v[136:139]\n"
"v_mul_f32 v104, v210, v104\n"
"v_mul_f32 v105, v210, v105\n"
"v_mfma_f32_16x16x32_bf16 v[136:139], a[228:231], v[64:67], v[136:139]\n"
"v_mul_f32 v106, v210, v106\n"
"v_mul_f32 v107, v210, v107\n"
"v_mfma_f32_16x16x32_bf16 v[136:139], a[224:227], v[52:55], v[136:139]\n"
"v_cvt_pk_bf16_f32 v240, v104, v105\n"
"v_cvt_pk_bf16_f32 v241, v106, v107\n"
"v_mfma_f32_16x16x32_bf16 v[132:135], a[224:227], v[48:51], v[132:135]\n"
"ds_read_b64_tr_b16 a[224:225], v197 offset:16384\n"
"ds_read_b64_tr_b16 a[226:227], v197 offset:18432\n"
"v_mfma_f32_16x16x32_bf16 v[132:135], a[228:231], v[60:63], v[132:135]\n"
"ds_read_b64_tr_b16 a[228:229], v197 offset:20480\n"
"ds_read_b64_tr_b16 a[230:231], v197 offset:22528\n"
"v_mfma_f32_16x16x32_bf16 v[132:135], a[232:235], v[72:75], v[132:135]\n"
"ds_read_b64_tr_b16 a[232:233], v197 offset:24576\n"
"ds_read_b64_tr_b16 a[234:235], v197 offset:26624\n"
"v_mfma_f32_16x16x32_bf16 v[132:135], a[236:239], v[84:87], v[132:135]\n"
"s_waitcnt lgkmcnt(12)\n"
"ds_read_b64_tr_b16 a[236:237], v197 offset:28672\n"
"ds_read_b64_tr_b16 a[238:239], v197 offset:30720\n"
"v_mfma_f32_16x16x32_bf16 v[144:147], a[208:211], v[0:3], 0\n"
"s_waitcnt lgkmcnt(12)\n"
"v_mul_f32 v116, v210, v116\n"
"v_mul_f32 v117, v210, v117\n"
"v_mfma_f32_16x16x32_bf16 v[144:147], a[212:215], v[12:15], v[144:147]\n"
"s_waitcnt lgkmcnt(10)\n"
"v_mul_f32 v118, v210, v118\n"
"v_mul_f32 v119, v210, v119\n"
"v_mfma_f32_16x16x32_bf16 v[144:147], a[216:219], v[24:27], v[144:147]\n"
"s_waitcnt lgkmcnt(8)\n"
"v_cvt_pk_bf16_f32 v242, v116, v117\n"
"v_cvt_pk_bf16_f32 v243, v118, v119\n"
"v_mfma_f32_16x16x32_bf16 v[144:147], a[220:223], v[36:39], v[144:147]\n"
"buffer_store_dwordx4 v[240:243], v207, s[40:43], s66 offen offset:2048 sc1\n"
"v_mfma_f32_16x16x32_bf16 v[148:151], a[220:223], v[40:43], 0\n"
"v_mul_f32 v120, v208, v120\n"
"v_mul_f32 v121, v208, v121\n"
"v_mfma_f32_16x16x32_bf16 v[148:151], a[216:219], v[28:31], v[148:151]\n"
"v_mul_f32 v122, v208, v122\n"
"v_mul_f32 v123, v208, v123\n"
"v_mfma_f32_16x16x32_bf16 v[148:151], a[212:215], v[16:19], v[148:151]\n"
"v_cvt_pk_bf16_f32 v244, v120, v121\n"
"v_cvt_pk_bf16_f32 v245, v122, v123\n"
"v_mfma_f32_16x16x32_bf16 v[148:151], a[208:211], v[4:7], v[148:151]\n"
"v_mul_f32 v132, v208, v132\n"
"v_mul_f32 v133, v208, v133\n"
"v_mfma_f32_16x16x32_bf16 v[152:155], a[208:211], v[8:11], 0\n"
"ds_read_b64_tr_b16 a[208:209], v196 offset:16896\n"
"ds_read_b64_tr_b16 a[210:211], v196 offset:18944\n"
"v_mfma_f32_16x16x32_bf16 v[152:155], a[212:215], v[20:23], v[152:155]\n"
"ds_read_b64_tr_b16 a[212:213], v196 offset:20992\n"
"ds_read_b64_tr_b16 a[214:215], v196 offset:23040\n"
"v_mfma_f32_16x16x32_bf16 v[152:155], a[216:219], v[32:35], v[152:155]\n"
"ds_read_b64_tr_b16 a[216:217], v196 offset:25088\n"
"ds_read_b64_tr_b16 a[218:219], v196 offset:27136\n"
"v_mfma_f32_16x16x32_bf16 v[152:155], a[220:223], v[44:47], v[152:155]\n"
"s_waitcnt lgkmcnt(12)\n"
"ds_read_b64_tr_b16 a[220:221], v196 offset:29184\n"
"ds_read_b64_tr_b16 a[222:223], v196 offset:31232\n"
"v_mfma_f32_16x16x32_bf16 v[164:167], a[224:227], v[8:11], 0\n"
"s_waitcnt lgkmcnt(12)\n"
"v_mul_f32 v134, v208, v134\n"
"v_mul_f32 v135, v208, v135\n"
"v_mfma_f32_16x16x32_bf16 v[164:167], a[228:231], v[20:23], v[164:167]\n"
"s_waitcnt lgkmcnt(10)\n"
"v_cvt_pk_bf16_f32 v246, v132, v133\n"
"v_cvt_pk_bf16_f32 v247, v134, v135\n"
"v_mfma_f32_16x16x32_bf16 v[164:167], a[232:235], v[32:35], v[164:167]\n"
"s_waitcnt lgkmcnt(8)\n"
"buffer_store_dwordx4 v[244:247], v207, s[40:43], s66 offen offset:3072 sc1\n"
"v_mfma_f32_16x16x32_bf16 v[164:167], a[236:239], v[44:47], v[164:167]\n"
"v_mul_f32 v124, v209, v124\n"
"v_mul_f32 v125, v209, v125\n"
"v_mfma_f32_16x16x32_bf16 v[160:163], a[236:239], v[40:43], 0\n"
"v_mul_f32 v126, v209, v126\n"
"v_mul_f32 v127, v209, v127\n"
"v_mfma_f32_16x16x32_bf16 v[160:163], a[232:235], v[28:31], v[160:163]\n"
"v_cvt_pk_bf16_f32 v232, v124, v125\n"
"v_cvt_pk_bf16_f32 v233, v126, v127\n"
"v_mfma_f32_16x16x32_bf16 v[160:163], a[228:231], v[16:19], v[160:163]\n"
"v_mul_f32 v136, v209, v136\n"
"v_mul_f32 v137, v209, v137\n"
"v_mfma_f32_16x16x32_bf16 v[160:163], a[224:227], v[4:7], v[160:163]\n"
"v_mul_f32 v138, v209, v138\n"
"v_mul_f32 v139, v209, v139\n"
"v_mfma_f32_16x16x32_bf16 v[156:159], a[224:227], v[0:3], 0\n"
"ds_read_b64_tr_b16 a[224:225], v197 offset:16896\n"
"ds_read_b64_tr_b16 a[226:227], v197 offset:18944\n"
"v_mfma_f32_16x16x32_bf16 v[156:159], a[228:231], v[12:15], v[156:159]\n"
"ds_read_b64_tr_b16 a[228:229], v197 offset:20992\n"
"ds_read_b64_tr_b16 a[230:231], v197 offset:23040\n"
"v_mfma_f32_16x16x32_bf16 v[156:159], a[232:235], v[24:27], v[156:159]\n"
"ds_read_b64_tr_b16 a[232:233], v197 offset:25088\n"
"ds_read_b64_tr_b16 a[234:235], v197 offset:27136\n"
"v_mfma_f32_16x16x32_bf16 v[156:159], a[236:239], v[36:39], v[156:159]\n"
"s_waitcnt lgkmcnt(12)\n"
"ds_read_b64_tr_b16 a[236:237], v197 offset:29184\n"
"ds_read_b64_tr_b16 a[238:239], v197 offset:31232\n"
"v_mfma_f32_16x16x32_bf16 v[168:171], a[208:211], v[0:3], 0\n"
"s_waitcnt lgkmcnt(12)\n"
"v_cvt_pk_bf16_f32 v234, v136, v137\n"
"v_cvt_pk_bf16_f32 v235, v138, v139\n"
"v_mfma_f32_16x16x32_bf16 v[168:171], a[212:215], v[12:15], v[168:171]\n"
"s_waitcnt lgkmcnt(10)\n"
"s_mov_b32 s66, 69632\n"
"v_mfma_f32_16x16x32_bf16 v[168:171], a[216:219], v[24:27], v[168:171]\n"
"s_waitcnt lgkmcnt(8)\n"
"buffer_store_dwordx4 v[232:235], v207, s[40:43], s66 offen sc1\n"
"v_mfma_f32_16x16x32_bf16 v[168:171], a[220:223], v[36:39], v[168:171]\n"
"v_mul_f32 v128, v210, v128\n"
"v_mul_f32 v129, v210, v129\n"
"v_mfma_f32_16x16x32_bf16 v[172:175], a[220:223], v[40:43], 0\n"
"v_mul_f32 v130, v210, v130\n"
"v_mul_f32 v131, v210, v131\n"
"v_mfma_f32_16x16x32_bf16 v[172:175], a[216:219], v[28:31], v[172:175]\n"
"v_cvt_pk_bf16_f32 v236, v128, v129\n"
"v_cvt_pk_bf16_f32 v237, v130, v131\n"
"v_mfma_f32_16x16x32_bf16 v[172:175], a[212:215], v[16:19], v[172:175]\n"
"s_waitcnt vmcnt(5)\n"
"s_barrier\n"
"v_mov_b32 v254, s68\n"
"v_mfma_f32_16x16x32_bf16 v[172:175], a[208:211], v[4:7], v[172:175]\n"
"v_add_u32 v254, 4, v254\n"
"v_mul_f32 v140, v210, v140\n"
"v_mfma_f32_16x16x32_bf16 v[176:179], a[208:211], v[8:11], 0\n"
"buffer_store_dwordx2 v[254:255], v215, s[48:51], 0 offen sc1\n"
"v_mfma_f32_16x16x32_bf16 v[176:179], a[212:215], v[20:23], v[176:179]\n"
"ds_read_b64_tr_b16 a[208:209], v192 offset:0\n"
"ds_read_b64_tr_b16 a[210:211], v192 offset:2048\n"
"v_mfma_f32_16x16x32_bf16 v[176:179], a[216:219], v[32:35], v[176:179]\n"
"ds_read_b64_tr_b16 a[212:213], v192 offset:4096\n"
"ds_read_b64_tr_b16 a[214:215], v192 offset:6144\n"
"v_mfma_f32_16x16x32_bf16 v[176:179], a[220:223], v[44:47], v[176:179]\n"
"s_waitcnt lgkmcnt(10)\n"
"ds_read_b64_tr_b16 a[216:217], v192 offset:8192\n"
"ds_read_b64_tr_b16 a[218:219], v192 offset:10240\n"
"v_mfma_f32_16x16x32_bf16 v[188:191], a[224:227], v[8:11], 0\n"
"s_waitcnt lgkmcnt(10)\n"
"ds_read_b64_tr_b16 a[220:221], v192 offset:12288\n"
"ds_read_b64_tr_b16 a[222:223], v192 offset:14336\n"
"v_mfma_f32_16x16x32_bf16 v[188:191], a[228:231], v[20:23], v[188:191]\n"
"s_waitcnt lgkmcnt(10)\n"
"v_mul_f32 v141, v210, v141\n"
"v_mul_f32 v142, v210, v142\n"
"v_mfma_f32_16x16x32_bf16 v[188:191], a[232:235], v[32:35], v[188:191]\n"
"s_waitcnt lgkmcnt(8)\n"
"v_mul_f32 v143, v210, v143\n"
"v_cvt_pk_bf16_f32 v238, v140, v141\n"
"v_mfma_f32_16x16x32_bf16 v[188:191], a[236:239], v[44:47], v[188:191]\n"
"v_cvt_pk_bf16_f32 v239, v142, v143\n"
"v_mfma_f32_16x16x32_bf16 v[184:187], a[236:239], v[40:43], 0\n"
"buffer_store_dwordx4 v[236:239], v207, s[40:43], s66 offen offset:1024 sc1\n"
"v_mfma_f32_16x16x32_bf16 v[184:187], a[232:235], v[28:31], v[184:187]\n"
"v_mfma_f32_16x16x32_bf16 v[184:187], a[228:231], v[16:19], v[184:187]\n"
"v_mfma_f32_16x16x32_bf16 v[184:187], a[224:227], v[4:7], v[184:187]\n"
"v_mfma_f32_16x16x32_bf16 v[180:183], a[224:227], v[0:3], 0\n"
"ds_read_b64_tr_b16 a[224:225], v193 offset:0\n"
"ds_read_b64_tr_b16 a[226:227], v193 offset:2048\n"
"v_mfma_f32_16x16x32_bf16 v[180:183], a[228:231], v[12:15], v[180:183]\n"
"ds_read_b64_tr_b16 a[228:229], v193 offset:4096\n"
"ds_read_b64_tr_b16 a[230:231], v193 offset:6144\n"
"v_mfma_f32_16x16x32_bf16 v[180:183], a[232:235], v[24:27], v[180:183]\n"
"ds_read_b64_tr_b16 a[232:233], v193 offset:8192\n"
"ds_read_b64_tr_b16 a[234:235], v193 offset:10240\n"
"v_mfma_f32_16x16x32_bf16 v[180:183], a[236:239], v[36:39], v[180:183]\n"
"s_waitcnt lgkmcnt(12)\n"
"ds_read_b64_tr_b16 a[236:237], v193 offset:12288\n"
"ds_read_b64_tr_b16 a[238:239], v193 offset:14336\n"
"v_mfma_f32_16x16x32_bf16 v[144:147], a[208:211], v[48:51], v[144:147]\n"
"s_waitcnt lgkmcnt(12)\n"
"v_mfma_f32_16x16x32_bf16 v[144:147], a[212:215], v[60:63], v[144:147]\n"
"s_waitcnt lgkmcnt(10)\n"
"v_mfma_f32_16x16x32_bf16 v[144:147], a[216:219], v[72:75], v[144:147]\n"
"s_waitcnt lgkmcnt(8)\n"
"v_mfma_f32_16x16x32_bf16 v[144:147], a[220:223], v[84:87], v[144:147]\n"
"v_mfma_f32_16x16x32_bf16 v[148:151], a[220:223], v[88:91], v[148:151]\n"
"v_mfma_f32_16x16x32_bf16 v[148:151], a[216:219], v[76:79], v[148:151]\n"
"v_mfma_f32_16x16x32_bf16 v[148:151], a[212:215], v[64:67], v[148:151]\n"
"s_nop 4\n"
"v_mul_f32 v144, v208, v144\n"
"v_mul_f32 v145, v208, v145\n"
"v_mfma_f32_16x16x32_bf16 v[148:151], a[208:211], v[52:55], v[148:151]\n"
"v_mul_f32 v146, v208, v146\n"
"v_mul_f32 v147, v208, v147\n"
"v_mfma_f32_16x16x32_bf16 v[152:155], a[208:211], v[56:59], v[152:155]\n"
"ds_read_b64_tr_b16 a[208:209], v192 offset:512\n"
"ds_read_b64_tr_b16 a[210:211], v192 offset:2560\n"
"v_mfma_f32_16x16x32_bf16 v[152:155], a[212:215], v[68:71], v[152:155]\n"
"ds_read_b64_tr_b16 a[212:213], v192 offset:4608\n"
"ds_read_b64_tr_b16 a[214:215], v192 offset:6656\n"
"v_mfma_f32_16x16x32_bf16 v[152:155], a[216:219], v[80:83], v[152:155]\n"
"ds_read_b64_tr_b16 a[216:217], v192 offset:8704\n"
"ds_read_b64_tr_b16 a[218:219], v192 offset:10752\n"
"v_mfma_f32_16x16x32_bf16 v[152:155], a[220:223], v[92:95], v[152:155]\n"
"s_waitcnt lgkmcnt(12)\n"
"ds_read_b64_tr_b16 a[220:221], v192 offset:12800\n"
"ds_read_b64_tr_b16 a[222:223], v192 offset:14848\n"
"v_mfma_f32_16x16x32_bf16 v[164:167], a[224:227], v[56:59], v[164:167]\n"
"s_waitcnt lgkmcnt(12)\n"
"v_cvt_pk_bf16_f32 v240, v144, v145\n"
"v_cvt_pk_bf16_f32 v241, v146, v147\n"
"v_mfma_f32_16x16x32_bf16 v[164:167], a[228:231], v[68:71], v[164:167]\n"
"s_waitcnt lgkmcnt(10)\n"
"v_mfma_f32_16x16x32_bf16 v[164:167], a[232:235], v[80:83], v[164:167]\n"
"s_waitcnt lgkmcnt(8)\n"
"v_mfma_f32_16x16x32_bf16 v[164:167], a[236:239], v[92:95], v[164:167]\n"
"v_mfma_f32_16x16x32_bf16 v[160:163], a[236:239], v[88:91], v[160:163]\n"
"v_mfma_f32_16x16x32_bf16 v[160:163], a[232:235], v[76:79], v[160:163]\n"
"v_mfma_f32_16x16x32_bf16 v[160:163], a[228:231], v[64:67], v[160:163]\n"
"v_mfma_f32_16x16x32_bf16 v[160:163], a[224:227], v[52:55], v[160:163]\n"
"v_mfma_f32_16x16x32_bf16 v[156:159], a[224:227], v[48:51], v[156:159]\n"
"ds_read_b64_tr_b16 a[224:225], v193 offset:512\n"
"ds_read_b64_tr_b16 a[226:227], v193 offset:2560\n"
"v_mfma_f32_16x16x32_bf16 v[156:159], a[228:231], v[60:63], v[156:159]\n"
"ds_read_b64_tr_b16 a[228:229], v193 offset:4608\n"
"ds_read_b64_tr_b16 a[230:231], v193 offset:6656\n"
"v_mfma_f32_16x16x32_bf16 v[156:159], a[232:235], v[72:75], v[156:159]\n"
"ds_read_b64_tr_b16 a[232:233], v193 offset:8704\n"
"ds_read_b64_tr_b16 a[234:235], v193 offset:10752\n"
"v_mfma_f32_16x16x32_bf16 v[156:159], a[236:239], v[84:87], v[156:159]\n"
"s_waitcnt lgkmcnt(12)\n"
"ds_read_b64_tr_b16 a[236:237], v193 offset:12800\n"
"ds_read_b64_tr_b16 a[238:239], v193 offset:14848\n"
"v_mfma_f32_16x16x32_bf16 v[168:171], a[208:211], v[48:51], v[168:171]\n"
"s_waitcnt lgkmcnt(12)\n"
"v_mfma_f32_16x16x32_bf16 v[168:171], a[212:215], v[60:63], v[168:171]\n"
"s_waitcnt lgkmcnt(10)\n"
"v_mfma_f32_16x16x32_bf16 v[168:171], a[216:219], v[72:75], v[168:171]\n"
"s_waitcnt lgkmcnt(8)\n"
"v_mul_f32 v156, v208, v156\n"
"v_mul_f32 v157, v208, v157\n"
"v_mfma_f32_16x16x32_bf16 v[168:171], a[220:223], v[84:87], v[168:171]\n"
"v_mul_f32 v158, v208, v158\n"
"v_mul_f32 v159, v208, v159\n"
"v_mfma_f32_16x16x32_bf16 v[172:175], a[220:223], v[88:91], v[172:175]\n"
"v_cvt_pk_bf16_f32 v242, v156, v157\n"
"v_cvt_pk_bf16_f32 v243, v158, v159\n"
"v_mfma_f32_16x16x32_bf16 v[172:175], a[216:219], v[76:79], v[172:175]\n"
"s_mov_b32 s66, 81920\n"
"buffer_store_dwordx4 v[240:243], v207, s[40:43], s66 offen sc1\n"
"v_mfma_f32_16x16x32_bf16 v[172:175], a[212:215], v[64:67], v[172:175]\n"
"v_mul_f32 v148, v209, v148\n"
"v_mul_f32 v149, v209, v149\n"
"v_mfma_f32_16x16x32_bf16 v[172:175], a[208:211], v[52:55], v[172:175]\n"
"v_mul_f32 v150, v209, v150\n"
"v_mul_f32 v151, v209, v151\n"
"v_mfma_f32_16x16x32_bf16 v[176:179], a[208:211], v[56:59], v[176:179]\n"
"ds_read_b64_tr_b16 a[208:209], v192 offset:16384\n"
"ds_read_b64_tr_b16 a[210:211], v192 offset:18432\n"
"v_mfma_f32_16x16x32_bf16 v[176:179], a[212:215], v[68:71], v[176:179]\n"
"ds_read_b64_tr_b16 a[212:213], v192 offset:20480\n"
"ds_read_b64_tr_b16 a[214:215], v192 offset:22528\n"
"v_mfma_f32_16x16x32_bf16 v[176:179], a[216:219], v[80:83], v[176:179]\n"
"ds_read_b64_tr_b16 a[216:217], v192 offset:24576\n"
"ds_read_b64_tr_b16 a[218:219], v192 offset:26624\n"
"v_mfma_f32_16x16x32_bf16 v[176:179], a[220:223], v[92:95], v[176:179]\n"
"s_waitcnt lgkmcnt(12)\n"
"ds_read_b64_tr_b16 a[220:221], v192 offset:28672\n"
"ds_read_b64_tr_b16 a[222:223], v192 offset:30720\n"
"v_mfma_f32_16x16x32_bf16 v[188:191], a[224:227], v[56:59], v[188:191]\n"
"s_waitcnt lgkmcnt(12)\n"
"v_cvt_pk_bf16_f32 v244, v148, v149\n"
"v_cvt_pk_bf16_f32 v245, v150, v151\n"
"v_mfma_f32_16x16x32_bf16 v[188:191], a[228:231], v[68:71], v[188:191]\n"
"s_waitcnt lgkmcnt(10)\n"
"v_mul_f32 v160, v209, v160\n"
"v_mul_f32 v161, v209, v161\n"
"v_mfma_f32_16x16x32_bf16 v[188:191], a[232:235], v[80:83], v[188:191]\n"
"s_waitcnt lgkmcnt(8)\n"
"v_mul_f32 v162, v209, v162\n"
"v_mul_f32 v163, v209, v163\n"
"v_mfma_f32_16x16x32_bf16 v[188:191], a[236:239], v[92:95], v[188:191]\n"
"v_cvt_pk_bf16_f32 v246, v160, v161\n"
"v_cvt_pk_bf16_f32 v247, v162, v163\n"
"v_mfma_f32_16x16x32_bf16 v[184:187], a[236:239], v[88:91], v[184:187]\n"
"buffer_store_dwordx4 v[244:247], v207, s[40:43], s66 offen offset:1024 sc1\n"
"v_mfma_f32_16x16x32_bf16 v[184:187], a[232:235], v[76:79], v[184:187]\n"
"v_mul_f32 v152, v210, v152\n"
"v_mul_f32 v153, v210, v153\n"
"v_mfma_f32_16x16x32_bf16 v[184:187], a[228:231], v[64:67], v[184:187]\n"
"v_mul_f32 v154, v210, v154\n"
"v_mul_f32 v155, v210, v155\n"
"v_mfma_f32_16x16x32_bf16 v[184:187], a[224:227], v[52:55], v[184:187]\n"
"v_cvt_pk_bf16_f32 v232, v152, v153\n"
"v_cvt_pk_bf16_f32 v233, v154, v155\n"
"v_mfma_f32_16x16x32_bf16 v[180:183], a[224:227], v[48:51], v[180:183]\n"
"ds_read_b64_tr_b16 a[224:225], v193 offset:16384\n"
"ds_read_b64_tr_b16 a[226:227], v193 offset:18432\n"
"v_mfma_f32_16x16x32_bf16 v[180:183], a[228:231], v[60:63], v[180:183]\n"
"ds_read_b64_tr_b16 a[228:229], v193 offset:20480\n"
"ds_read_b64_tr_b16 a[230:231], v193 offset:22528\n"
"v_mfma_f32_16x16x32_bf16 v[180:183], a[232:235], v[72:75], v[180:183]\n"
"ds_read_b64_tr_b16 a[232:233], v193 offset:24576\n"
"ds_read_b64_tr_b16 a[234:235], v193 offset:26624\n"
"v_mfma_f32_16x16x32_bf16 v[180:183], a[236:239], v[84:87], v[180:183]\n"
"s_waitcnt lgkmcnt(12)\n"
"ds_read_b64_tr_b16 a[236:237], v193 offset:28672\n"
"ds_read_b64_tr_b16 a[238:239], v193 offset:30720\n"
"v_mfma_f32_16x16x32_bf16 v[96:99], a[208:211], v[0:3], 0\n"
"s_waitcnt lgkmcnt(12)\n"
"v_mul_f32 v164, v210, v164\n"
"v_mul_f32 v165, v210, v165\n"
"v_mfma_f32_16x16x32_bf16 v[96:99], a[212:215], v[12:15], v[96:99]\n"
"s_waitcnt lgkmcnt(10)\n"
"v_mul_f32 v166, v210, v166\n"
"v_mul_f32 v167, v210, v167\n"
"v_mfma_f32_16x16x32_bf16 v[96:99], a[216:219], v[24:27], v[96:99]\n"
"s_waitcnt lgkmcnt(8)\n"
"v_cvt_pk_bf16_f32 v234, v164, v165\n"
"v_cvt_pk_bf16_f32 v235, v166, v167\n"
"v_mfma_f32_16x16x32_bf16 v[96:99], a[220:223], v[36:39], v[96:99]\n"
"buffer_store_dwordx4 v[232:235], v207, s[40:43], s66 offen offset:2048 sc1\n"
"v_mfma_f32_16x16x32_bf16 v[100:103], a[220:223], v[40:43], 0\n"
"v_mul_f32 v168, v208, v168\n"
"v_mul_f32 v169, v208, v169\n"
"v_mfma_f32_16x16x32_bf16 v[100:103], a[216:219], v[28:31], v[100:103]\n"
"v_mul_f32 v170, v208, v170\n"
"v_mul_f32 v171, v208, v171\n"
"v_mfma_f32_16x16x32_bf16 v[100:103], a[212:215], v[16:19], v[100:103]\n"
"v_cvt_pk_bf16_f32 v236, v168, v169\n"
"v_cvt_pk_bf16_f32 v237, v170, v171\n"
"v_mfma_f32_16x16x32_bf16 v[100:103], a[208:211], v[4:7], v[100:103]\n"
"v_mul_f32 v180, v208, v180\n"
"v_mul_f32 v181, v208, v181\n"
"v_mfma_f32_16x16x32_bf16 v[104:107], a[208:211], v[8:11], 0\n"
"ds_read_b64_tr_b16 a[208:209], v192 offset:16896\n"
"ds_read_b64_tr_b16 a[210:211], v192 offset:18944\n"
"v_mfma_f32_16x16x32_bf16 v[104:107], a[212:215], v[20:23], v[104:107]\n"
"ds_read_b64_tr_b16 a[212:213], v192 offset:20992\n"
"ds_read_b64_tr_b16 a[214:215], v192 offset:23040\n"
"v_mfma_f32_16x16x32_bf16 v[104:107], a[216:219], v[32:35], v[104:107]\n"
"ds_read_b64_tr_b16 a[216:217], v192 offset:25088\n"
"ds_read_b64_tr_b16 a[218:219], v192 offset:27136\n"
"v_mfma_f32_16x16x32_bf16 v[104:107], a[220:223], v[44:47], v[104:107]\n"
"s_waitcnt lgkmcnt(12)\n"
"ds_read_b64_tr_b16 a[220:221], v192 offset:29184\n"
"ds_read_b64_tr_b16 a[222:223], v192 offset:31232\n"
"v_mfma_f32_16x16x32_bf16 v[116:119], a[224:227], v[8:11], 0\n"
"s_waitcnt lgkmcnt(12)\n"
"v_mul_f32 v182, v208, v182\n"
"v_mul_f32 v183, v208, v183\n"
"v_mfma_f32_16x16x32_bf16 v[116:119], a[228:231], v[20:23], v[116:119]\n"
"s_waitcnt lgkmcnt(10)\n"
"v_cvt_pk_bf16_f32 v238, v180, v181\n"
"v_cvt_pk_bf16_f32 v239, v182, v183\n"
"v_mfma_f32_16x16x32_bf16 v[116:119], a[232:235], v[32:35], v[116:119]\n"
"s_waitcnt lgkmcnt(8)\n"
"buffer_store_dwordx4 v[236:239], v207, s[40:43], s66 offen offset:3072 sc1\n"
"v_mfma_f32_16x16x32_bf16 v[116:119], a[236:239], v[44:47], v[116:119]\n"
"v_mul_f32 v172, v209, v172\n"
"v_mul_f32 v173, v209, v173\n"
"v_mfma_f32_16x16x32_bf16 v[112:115], a[236:239], v[40:43], 0\n"
"v_mul_f32 v174, v209, v174\n"
"v_mul_f32 v175, v209, v175\n"
"v_mfma_f32_16x16x32_bf16 v[112:115], a[232:235], v[28:31], v[112:115]\n"
"v_cvt_pk_bf16_f32 v240, v172, v173\n"
"v_cvt_pk_bf16_f32 v241, v174, v175\n"
"v_mfma_f32_16x16x32_bf16 v[112:115], a[228:231], v[16:19], v[112:115]\n"
"v_mul_f32 v184, v209, v184\n"
"v_mul_f32 v185, v209, v185\n"
"v_mfma_f32_16x16x32_bf16 v[112:115], a[224:227], v[4:7], v[112:115]\n"
"v_mul_f32 v186, v209, v186\n"
"v_mul_f32 v187, v209, v187\n"
"v_mfma_f32_16x16x32_bf16 v[108:111], a[224:227], v[0:3], 0\n"
"ds_read_b64_tr_b16 a[224:225], v193 offset:16896\n"
"ds_read_b64_tr_b16 a[226:227], v193 offset:18944\n"
"v_mfma_f32_16x16x32_bf16 v[108:111], a[228:231], v[12:15], v[108:111]\n"
"ds_read_b64_tr_b16 a[228:229], v193 offset:20992\n"
"ds_read_b64_tr_b16 a[230:231], v193 offset:23040\n"
"v_mfma_f32_16x16x32_bf16 v[108:111], a[232:235], v[24:27], v[108:111]\n"
"ds_read_b64_tr_b16 a[232:233], v193 offset:25088\n"
"ds_read_b64_tr_b16 a[234:235], v193 offset:27136\n"
"v_mfma_f32_16x16x32_bf16 v[108:111], a[236:239], v[36:39], v[108:111]\n"
"s_waitcnt lgkmcnt(12)\n"
"ds_read_b64_tr_b16 a[236:237], v193 offset:29184\n"
"ds_read_b64_tr_b16 a[238:239], v193 offset:31232\n"
"v_mfma_f32_16x16x32_bf16 v[120:123], a[208:211], v[0:3], 0\n"
"s_waitcnt lgkmcnt(12)\n"
"v_cvt_pk_bf16_f32 v242, v184, v185\n"
"v_cvt_pk_bf16_f32 v243, v186, v187\n"
"v_mfma_f32_16x16x32_bf16 v[120:123], a[212:215], v[12:15], v[120:123]\n"
"s_waitcnt lgkmcnt(10)\n"
"s_mov_b32 s66, 86016\n"
"v_mfma_f32_16x16x32_bf16 v[120:123], a[216:219], v[24:27], v[120:123]\n"
"s_waitcnt lgkmcnt(8)\n"
"buffer_store_dwordx4 v[240:243], v207, s[40:43], s66 offen sc1\n"
"v_mfma_f32_16x16x32_bf16 v[120:123], a[220:223], v[36:39], v[120:123]\n"
"v_mul_f32 v176, v210, v176\n"
"v_mul_f32 v177, v210, v177\n"
"v_mfma_f32_16x16x32_bf16 v[124:127], a[220:223], v[40:43], 0\n"
"v_mul_f32 v178, v210, v178\n"
"v_mul_f32 v179, v210, v179\n"
"v_mfma_f32_16x16x32_bf16 v[124:127], a[216:219], v[28:31], v[124:127]\n"
"v_cvt_pk_bf16_f32 v244, v176, v177\n"
"v_cvt_pk_bf16_f32 v245, v178, v179\n"
"v_mfma_f32_16x16x32_bf16 v[124:127], a[212:215], v[16:19], v[124:127]\n"
"v_mul_f32 v188, v210, v188\n"
"v_mul_f32 v189, v210, v189\n"
"v_mfma_f32_16x16x32_bf16 v[124:127], a[208:211], v[4:7], v[124:127]\n"
"v_mul_f32 v190, v210, v190\n"
"v_mul_f32 v191, v210, v191\n"
"v_mfma_f32_16x16x32_bf16 v[128:131], a[208:211], v[8:11], 0\n"
"ds_read_b64_tr_b16 a[208:209], v192 offset:32768\n"
"ds_read_b64_tr_b16 a[210:211], v192 offset:34816\n"
"v_mfma_f32_16x16x32_bf16 v[128:131], a[212:215], v[20:23], v[128:131]\n"
"ds_read_b64_tr_b16 a[212:213], v192 offset:36864\n"
"ds_read_b64_tr_b16 a[214:215], v192 offset:38912\n"
"v_mfma_f32_16x16x32_bf16 v[128:131], a[216:219], v[32:35], v[128:131]\n"
"ds_read_b64_tr_b16 a[216:217], v192 offset:40960\n"
"ds_read_b64_tr_b16 a[218:219], v192 offset:43008\n"
"v_mfma_f32_16x16x32_bf16 v[128:131], a[220:223], v[44:47], v[128:131]\n"
"s_waitcnt lgkmcnt(12)\n"
"ds_read_b64_tr_b16 a[220:221], v192 offset:45056\n"
"ds_read_b64_tr_b16 a[222:223], v192 offset:47104\n"
"v_mfma_f32_16x16x32_bf16 v[140:143], a[224:227], v[8:11], 0\n"
"s_waitcnt lgkmcnt(12)\n"
"v_cvt_pk_bf16_f32 v246, v188, v189\n"
"v_cvt_pk_bf16_f32 v247, v190, v191\n"
"v_mfma_f32_16x16x32_bf16 v[140:143], a[228:231], v[20:23], v[140:143]\n"
"s_waitcnt lgkmcnt(10)\n"
"buffer_store_dwordx4 v[244:247], v207, s[40:43], s66 offen offset:1024 sc1\n"
"v_mfma_f32_16x16x32_bf16 v[140:143], a[232:235], v[32:35], v[140:143]\n"
"s_waitcnt lgkmcnt(8)\n"
"v_mfma_f32_16x16x32_bf16 v[140:143], a[236:239], v[44:47], v[140:143]\n"
"v_mfma_f32_16x16x32_bf16 v[136:139], a[236:239], v[40:43], 0\n"
"v_mfma_f32_16x16x32_bf16 v[136:139], a[232:235], v[28:31], v[136:139]\n"
"v_mfma_f32_16x16x32_bf16 v[136:139], a[228:231], v[16:19], v[136:139]\n"
"v_mfma_f32_16x16x32_bf16 v[136:139], a[224:227], v[4:7], v[136:139]\n"
"buffer_load_dwordx2 v[212:213], v214, s[48:51], 0 offen sc1\n"
"v_mfma_f32_16x16x32_bf16 v[132:135], a[224:227], v[0:3], 0\n"
"ds_read_b64_tr_b16 a[224:225], v193 offset:32768\n"
"ds_read_b64_tr_b16 a[226:227], v193 offset:34816\n"
"v_mfma_f32_16x16x32_bf16 v[132:135], a[228:231], v[12:15], v[132:135]\n"
"ds_read_b64_tr_b16 a[228:229], v193 offset:36864\n"
"ds_read_b64_tr_b16 a[230:231], v193 offset:38912\n"
"v_mfma_f32_16x16x32_bf16 v[132:135], a[232:235], v[24:27], v[132:135]\n"
"ds_read_b64_tr_b16 a[232:233], v193 offset:40960\n"
"ds_read_b64_tr_b16 a[234:235], v193 offset:43008\n"
"v_mfma_f32_16x16x32_bf16 v[132:135], a[236:239], v[36:39], v[132:135]\n"
"s_waitcnt lgkmcnt(12)\n"
"ds_read_b64_tr_b16 a[236:237], v193 offset:45056\n"
"ds_read_b64_tr_b16 a[238:239], v193 offset:47104\n"
"v_mfma_f32_16x16x32_bf16 v[96:99], a[208:211], v[48:51], v[96:99]\n"
"s_waitcnt lgkmcnt(12)\n"
"v_mfma_f32_16x16x32_bf16 v[96:99], a[212:215], v[60:63], v[96:99]\n"
"s_waitcnt lgkmcnt(10)\n"
"v_mfma_f32_16x16x32_bf16 v[96:99], a[216:219], v[72:75], v[96:99]\n"
"s_waitcnt lgkmcnt(8)\n"
"v_mfma_f32_16x16x32_bf16 v[96:99], a[220:223], v[84:87], v[96:99]\n"
"v_mfma_f32_16x16x32_bf16 v[100:103], a[220:223], v[88:91], v[100:103]\n"
"v_mfma_f32_16x16x32_bf16 v[100:103], a[216:219], v[76:79], v[100:103]\n"
"v_mfma_f32_16x16x32_bf16 v[100:103], a[212:215], v[64:67], v[100:103]\n"
"s_nop 4\n"
"v_mul_f32 v96, v208, v96\n"
"v_mul_f32 v97, v208, v97\n"
"v_mfma_f32_16x16x32_bf16 v[100:103], a[208:211], v[52:55], v[100:103]\n"
"v_mul_f32 v98, v208, v98\n"
"v_mul_f32 v99, v208, v99\n"
"v_mfma_f32_16x16x32_bf16 v[104:107], a[208:211], v[56:59], v[104:107]\n"
"ds_read_b64_tr_b16 a[208:209], v192 offset:33280\n"
"ds_read_b64_tr_b16 a[210:211], v192 offset:35328\n"
"v_mfma_f32_16x16x32_bf16 v[104:107], a[212:215], v[68:71], v[104:107]\n"
"ds_read_b64_tr_b16 a[212:213], v192 offset:37376\n"
"ds_read_b64_tr_b16 a[214:215], v192 offset:39424\n"
"v_mfma_f32_16x16x32_bf16 v[104:107], a[216:219], v[80:83], v[104:107]\n"
"ds_read_b64_tr_b16 a[216:217], v192 offset:41472\n"
"ds_read_b64_tr_b16 a[218:219], v192 offset:43520\n"
"v_mfma_f32_16x16x32_bf16 v[104:107], a[220:223], v[92:95], v[104:107]\n"
"s_waitcnt lgkmcnt(12)\n"
"ds_read_b64_tr_b16 a[220:221], v192 offset:45568\n"
"ds_read_b64_tr_b16 a[222:223], v192 offset:47616\n"
"v_mfma_f32_16x16x32_bf16 v[116:119], a[224:227], v[56:59], v[116:119]\n"
"s_waitcnt lgkmcnt(12)\n"
"v_cvt_pk_bf16_f32 v232, v96, v97\n"
"v_cvt_pk_bf16_f32 v233, v98, v99\n"
"v_mfma_f32_16x16x32_bf16 v[116:119], a[228:231], v[68:71], v[116:119]\n"
"s_waitcnt lgkmcnt(10)\n"
"v_mfma_f32_16x16x32_bf16 v[116:119], a[232:235], v[80:83], v[116:119]\n"
"s_waitcnt lgkmcnt(8)\n"
"v_mfma_f32_16x16x32_bf16 v[116:119], a[236:239], v[92:95], v[116:119]\n"
"v_mfma_f32_16x16x32_bf16 v[112:115], a[236:239], v[88:91], v[112:115]\n"
"v_mfma_f32_16x16x32_bf16 v[112:115], a[232:235], v[76:79], v[112:115]\n"
"v_mfma_f32_16x16x32_bf16 v[112:115], a[228:231], v[64:67], v[112:115]\n"
"v_mfma_f32_16x16x32_bf16 v[112:115], a[224:227], v[52:55], v[112:115]\n"
"v_mfma_f32_16x16x32_bf16 v[108:111], a[224:227], v[48:51], v[108:111]\n"
"ds_read_b64_tr_b16 a[224:225], v193 offset:33280\n"
"ds_read_b64_tr_b16 a[226:227], v193 offset:35328\n"
"v_mfma_f32_16x16x32_bf16 v[108:111], a[228:231], v[60:63], v[108:111]\n"
"ds_read_b64_tr_b16 a[228:229], v193 offset:37376\n"
"ds_read_b64_tr_b16 a[230:231], v193 offset:39424\n"
"v_mfma_f32_16x16x32_bf16 v[108:111], a[232:235], v[72:75], v[108:111]\n"
"ds_read_b64_tr_b16 a[232:233], v193 offset:41472\n"
"ds_read_b64_tr_b16 a[234:235], v193 offset:43520\n"
"v_mfma_f32_16x16x32_bf16 v[108:111], a[236:239], v[84:87], v[108:111]\n"
"s_waitcnt lgkmcnt(12)\n"
"ds_read_b64_tr_b16 a[236:237], v193 offset:45568\n"
"ds_read_b64_tr_b16 a[238:239], v193 offset:47616\n"
"v_mfma_f32_16x16x32_bf16 v[120:123], a[208:211], v[48:51], v[120:123]\n"
"s_waitcnt lgkmcnt(12)\n"
"v_mfma_f32_16x16x32_bf16 v[120:123], a[212:215], v[60:63], v[120:123]\n"
"s_waitcnt lgkmcnt(10)\n"
"v_mfma_f32_16x16x32_bf16 v[120:123], a[216:219], v[72:75], v[120:123]\n"
"s_waitcnt lgkmcnt(8)\n"
"v_mul_f32 v108, v208, v108\n"
"v_mul_f32 v109, v208, v109\n"
"v_mfma_f32_16x16x32_bf16 v[120:123], a[220:223], v[84:87], v[120:123]\n"
"v_mul_f32 v110, v208, v110\n"
"v_mul_f32 v111, v208, v111\n"
"v_mfma_f32_16x16x32_bf16 v[124:127], a[220:223], v[88:91], v[124:127]\n"
"v_cvt_pk_bf16_f32 v234, v108, v109\n"
"v_cvt_pk_bf16_f32 v235, v110, v111\n"
"v_mfma_f32_16x16x32_bf16 v[124:127], a[216:219], v[76:79], v[124:127]\n"
"s_mov_b32 s66, 98304\n"
"buffer_store_dwordx4 v[232:235], v207, s[40:43], s66 offen sc1\n"
"v_mfma_f32_16x16x32_bf16 v[124:127], a[212:215], v[64:67], v[124:127]\n"
"v_mul_f32 v100, v209, v100\n"
"v_mul_f32 v101, v209, v101\n"
"v_mfma_f32_16x16x32_bf16 v[124:127], a[208:211], v[52:55], v[124:127]\n"
"v_mul_f32 v102, v209, v102\n"
"v_mul_f32 v103, v209, v103\n"
"v_mfma_f32_16x16x32_bf16 v[128:131], a[208:211], v[56:59], v[128:131]\n"
"v_cvt_pk_bf16_f32 v236, v100, v101\n"
"v_cvt_pk_bf16_f32 v237, v102, v103\n"
"v_mfma_f32_16x16x32_bf16 v[128:131], a[212:215], v[68:71], v[128:131]\n"
"v_mul_f32 v112, v209, v112\n"
"v_mul_f32 v113, v209, v113\n"
"v_mfma_f32_16x16x32_bf16 v[128:131], a[216:219], v[80:83], v[128:131]\n"
"v_mul_f32 v114, v209, v114\n"
"v_mul_f32 v115, v209, v115\n"
"v_mfma_f32_16x16x32_bf16 v[128:131], a[220:223], v[92:95], v[128:131]\n"
"s_waitcnt lgkmcnt(6)\n"
"v_cvt_pk_bf16_f32 v238, v112, v113\n"
"v_cvt_pk_bf16_f32 v239, v114, v115\n"
"v_mfma_f32_16x16x32_bf16 v[140:143], a[224:227], v[56:59], v[140:143]\n"
"s_waitcnt lgkmcnt(4)\n"
"buffer_store_dwordx4 v[236:239], v207, s[40:43], s66 offen offset:1024 sc1\n"
"v_mfma_f32_16x16x32_bf16 v[140:143], a[228:231], v[68:71], v[140:143]\n"
"s_waitcnt lgkmcnt(2)\n"
"v_mul_f32 v104, v210, v104\n"
"v_mul_f32 v105, v210, v105\n"
"v_mfma_f32_16x16x32_bf16 v[140:143], a[232:235], v[80:83], v[140:143]\n"
"s_waitcnt lgkmcnt(0)\n"
"v_mul_f32 v106, v210, v106\n"
"v_mul_f32 v107, v210, v107\n"
"v_mfma_f32_16x16x32_bf16 v[140:143], a[236:239], v[92:95], v[140:143]\n"
"v_cvt_pk_bf16_f32 v240, v104, v105\n"
"v_cvt_pk_bf16_f32 v241, v106, v107\n"
"v_mfma_f32_16x16x32_bf16 v[136:139], a[236:239], v[88:91], v[136:139]\n"
"v_mul_f32 v116, v210, v116\n"
"v_mul_f32 v117, v210, v117\n"
"v_mfma_f32_16x16x32_bf16 v[136:139], a[232:235], v[76:79], v[136:139]\n"
"v_mul_f32 v118, v210, v118\n"
"v_mul_f32 v119, v210, v119\n"
"v_mfma_f32_16x16x32_bf16 v[136:139], a[228:231], v[64:67], v[136:139]\n"
"v_cvt_pk_bf16_f32 v242, v116, v117\n"
"v_cvt_pk_bf16_f32 v243, v118, v119\n"
"v_mfma_f32_16x16x32_bf16 v[136:139], a[224:227], v[52:55], v[136:139]\n"
"buffer_store_dwordx4 v[240:243], v207, s[40:43], s66 offen offset:2048 sc1\n"
"v_mfma_f32_16x16x32_bf16 v[132:135], a[224:227], v[48:51], v[132:135]\n"
"v_mul_f32 v120, v208, v120\n"
"v_mul_f32 v121, v208, v121\n"
"v_mfma_f32_16x16x32_bf16 v[132:135], a[228:231], v[60:63], v[132:135]\n"
"v_mul_f32 v122, v208, v122\n"
"v_mul_f32 v123, v208, v123\n"
"v_mfma_f32_16x16x32_bf16 v[132:135], a[232:235], v[72:75], v[132:135]\n"
"v_cvt_pk_bf16_f32 v244, v120, v121\n"
"v_cvt_pk_bf16_f32 v245, v122, v123\n"
"v_mfma_f32_16x16x32_bf16 v[132:135], a[236:239], v[84:87], v[132:135]\n"
"s_nop 7\n"
"v_mul_f32 v132, v208, v132\n"
"v_mul_f32 v133, v208, v133\n"
"v_mul_f32 v134, v208, v134\n"
"v_mul_f32 v135, v208, v135\n"
"v_cvt_pk_bf16_f32 v246, v132, v133\n"
"v_cvt_pk_bf16_f32 v247, v134, v135\n"
"buffer_store_dwordx4 v[244:247], v207, s[40:43], s66 offen offset:3072 sc1\n"
"v_mul_f32 v124, v209, v124\n"
"v_mul_f32 v125, v209, v125\n"
"v_mul_f32 v126, v209, v126\n"
"v_mul_f32 v127, v209, v127\n"
"v_cvt_pk_bf16_f32 v232, v124, v125\n"
"v_cvt_pk_bf16_f32 v233, v126, v127\n"
"v_mul_f32 v136, v209, v136\n"
"v_mul_f32 v137, v209, v137\n"
"v_mul_f32 v138, v209, v138\n"
"v_mul_f32 v139, v209, v139\n"
"v_cvt_pk_bf16_f32 v234, v136, v137\n"
"v_cvt_pk_bf16_f32 v235, v138, v139\n"
"s_mov_b32 s66, 102400\n"
"buffer_store_dwordx4 v[232:235], v207, s[40:43], s66 offen sc1\n"
"v_mul_f32 v128, v210, v128\n"
"v_mul_f32 v129, v210, v129\n"
"v_mul_f32 v130, v210, v130\n"
"v_mul_f32 v131, v210, v131\n"
"v_cvt_pk_bf16_f32 v236, v128, v129\n"
"v_cvt_pk_bf16_f32 v237, v130, v131\n"
"v_mul_f32 v140, v210, v140\n"
"v_mul_f32 v141, v210, v141\n"
"v_mul_f32 v142, v210, v142\n"
"v_mul_f32 v143, v210, v143\n"
"v_cvt_pk_bf16_f32 v238, v140, v141\n"
"v_cvt_pk_bf16_f32 v239, v142, v143\n"
"buffer_store_dwordx4 v[236:239], v207, s[40:43], s66 offen offset:1024 sc1\n"
"Ltail:\n"
"s_waitcnt vmcnt(6)\n"
"v_cmp_ne_u32 s[58:59], s57, v213\n"
"v_and_b32 v213, 0xffff0007, v212\n"
"v_subrev_u32 v213, s56, v213\n"
"v_add_u32 v213, -4, v213\n"
"v_cmp_lt_u32 vcc, 3, v213\n"
"s_nop 0\n"
"s_or_b64 vcc, vcc, s[58:59]\n"
"s_cbranch_scc1 Lspin4a\n"
"Lok4a:\n"
"v_and_b32 v212, 8, v212\n"
"v_cmp_ne_u32 s[58:59], 0, v212\n"
"s_nop 1\n"
"v_lshrrev_b32 v212, v218, s58\n"
"v_bfe_u32 v198, v212, 0, 1\n"
"v_lshl_add_u32 v198, v198, 13, v216\n"
"v_bfe_u32 v199, v212, 2, 1\n"
"v_lshl_add_u32 v199, v199, 13, v216\n"
"v_bfe_u32 v200, v212, 4, 1\n"
"v_lshl_add_u32 v200, v200, 13, v216\n"
"v_bfe_u32 v201, v212, 6, 1\n"
"v_lshl_add_u32 v201, v201, 13, v216\n"
"v_bfe_u32 v202, v212, 8, 1\n"
"v_lshl_add_u32 v202, v202, 13, v216\n"
"v_bfe_u32 v203, v212, 10, 1\n"
"v_lshl_add_u32 v203, v203, 13, v216\n"
"v_bfe_u32 v204, v212, 12, 1\n"
"v_lshl_add_u32 v204, v204, 13, v216\n"
"v_bfe_u32 v205, v212, 14, 1\n"
"v_lshl_add_u32 v205, v205, 13, v216\n"
"s_mov_b32 s67, 0\n"
"buffer_load_dwordx4 a[0:3], v198, s[44:47], s67 offen sc1\n"
"s_mov_b32 s67, 1048576\n"
"buffer_load_dwordx4 a[4:7], v199, s[44:47], s67 offen sc1\n"
"s_mov_b32 s67, 2097152\n"
"buffer_load_dwordx4 a[8:11], v200, s[44:47], s67 offen sc1\n"
"s_mov_b32 s67, 3145728\n"
"buffer_load_dwordx4 a[12:15], v201, s[44:47], s67 offen sc1\n"
"s_mov_b32 s67, 4194304\n"
"buffer_load_dwordx4 a[16:19], v202, s[44:47], s67 offen sc1\n"
"s_mov_b32 s67, 5242880\n"
"buffer_load_dwordx4 a[20:23], v203, s[44:47], s67 offen sc1\n"
"s_mov_b32 s67, 6291456\n"
"buffer_load_dwordx4 a[24:27], v204, s[44:47], s67 offen sc1\n"
"s_mov_b32 s67, 7340032\n"
"buffer_load_dwordx4 a[28:31], v205, s[44:47], s67 offen sc1\n"
"s_waitcnt vmcnt(8)\n"
"s_barrier\n"
"v_mov_b32 v254, s68\n"
"v_add_u32 v254, 7, v254\n"
"buffer_store_dwordx2 v[254:255], v215, s[48:51], 0 offen sc1\n"
"s_mov_b32 s67, 16384\n"
"buffer_load_dwordx4 a[32:35], v198, s[44:47], s67 offen sc1\n"
"s_mov_b32 s67, 1064960\n"
"buffer_load_dwordx4 a[36:39], v199, s[44:47], s67 offen sc1\n"
"s_mov_b32 s67, 2113536\n"
"buffer_load_dwordx4 a[40:43], v200, s[44:47], s67 offen sc1\n"
"s_mov_b32 s67, 3162112\n"
"buffer_load_dwordx4 a[44:47], v201, s[44:47], s67 offen sc1\n"
"s_mov_b32 s67, 4210688\n"
"buffer_load_dwordx4 a[48:51], v202, s[44:47], s67 offen sc1\n"
"s_mov_b32 s67, 5259264\n"
"buffer_load_dwordx4 a[52:55], v203, s[44:47], s67 offen sc1\n"
"s_mov_b32 s67, 6307840\n"
"buffer_load_dwordx4 a[56:59], v204, s[44:47], s67 offen sc1\n"
"s_mov_b32 s67, 7356416\n"
"buffer_load_dwordx4 a[60:63], v205, s[44:47], s67 offen sc1\n"
"s_mov_b32 s67, 32768\n"
"buffer_load_dwordx4 a[64:67], v198, s[44:47], s67 offen sc1\n"
"s_mov_b32 s67, 1081344\n"
"buffer_load_dwordx4 a[68:71], v199, s[44:47], s67 offen sc1\n"
"s_mov_b32 s67, 2129920\n"
"buffer_load_dwordx4 a[72:75], v200, s[44:47], s67 offen sc1\n"
"s_mov_b32 s67, 3178496\n"
"buffer_load_dwordx4 a[76:79], v201, s[44:47], s67 offen sc1\n"
"s_mov_b32 s67, 4227072\n"
"buffer_load_dwordx4 a[80:83], v202, s[44:47], s67 offen sc1\n"
"s_mov_b32 s67, 5275648\n"
"buffer_load_dwordx4 a[84:87], v203, s[44:47], s67 offen sc1\n"
"s_mov_b32 s67, 6324224\n"
"buffer_load_dwordx4 a[88:91], v204, s[44:47], s67 offen sc1\n"
"s_mov_b32 s67, 7372800\n"
"buffer_load_dwordx4 a[92:95], v205, s[44:47], s67 offen sc1\n"
"s_waitcnt vmcnt(24)\n"
"v_mfma_f32_16x16x32_bf16 v[96:99], a[240:243], a[0:3], 0\n"
"v_mfma_f32_16x16x32_bf16 v[100:103], a[244:247], a[0:3], 0\n"
"s_waitcnt vmcnt(23)\n"
"v_mfma_f32_16x16x32_bf16 v[96:99], a[240:243], a[4:7], v[96:99]\n"
"v_mfma_f32_16x16x32_bf16 v[100:103], a[244:247], a[4:7], v[100:103]\n"
"s_waitcnt vmcnt(22)\n"
"v_mfma_f32_16x16x32_bf16 v[96:99], a[240:243], a[8:11], v[96:99]\n"
"v_mfma_f32_16x16x32_bf16 v[100:103], a[244:247], a[8:11], v[100:103]\n"
"s_waitcnt vmcnt(21)\n"
"v_mfma_f32_16x16x32_bf16 v[96:99], a[240:243], a[12:15], v[96:99]\n"
"v_mfma_f32_16x16x32_bf16 v[100:103], a[244:247], a[12:15], v[100:103]\n"
"s_waitcnt vmcnt(20)\n"
"v_mfma_f32_16x16x32_bf16 v[96:99], a[240:243], a[16:19], v[96:99]\n"
"v_mfma_f32_16x16x32_bf16 v[100:103], a[244:247], a[16:19], v[100:103]\n"
"s_waitcnt vmcnt(19)\n"
"v_mfma_f32_16x16x32_bf16 v[96:99], a[240:243], a[20:23], v[96:99]\n"
"v_mfma_f32_16x16x32_bf16 v[100:103], a[244:247], a[20:23], v[100:103]\n"
"s_waitcnt vmcnt(18)\n"
"v_mfma_f32_16x16x32_bf16 v[96:99], a[240:243], a[24:27], v[96:99]\n"
"v_mfma_f32_16x16x32_bf16 v[100:103], a[244:247], a[24:27], v[100:103]\n"
"s_waitcnt vmcnt(17)\n"
"v_mfma_f32_16x16x32_bf16 v[96:99], a[240:243], a[28:31], v[96:99]\n"
"v_mfma_f32_16x16x32_bf16 v[100:103], a[244:247], a[28:31], v[100:103]\n"
"s_nop 6\n"
"v_add_f32_dpp v96, v96, v96 row_ror:8 row_mask:0xf bank_mask:0xf\n"
"v_add_f32_dpp v97, v97, v97 row_ror:8 row_mask:0xf bank_mask:0xf\n"
"v_add_f32_dpp v98, v98, v98 row_ror:8 row_mask:0xf bank_mask:0xf\n"
"v_add_f32_dpp v99, v99, v99 row_ror:8 row_mask:0xf bank_mask:0xf\n"
"v_add_f32_dpp v100, v100, v100 row_ror:8 row_mask:0xf bank_mask:0xf\n"
"v_add_f32_dpp v101, v101, v101 row_ror:8 row_mask:0xf bank_mask:0xf\n"
"v_add_f32_dpp v102, v102, v102 row_ror:8 row_mask:0xf bank_mask:0xf\n"
"v_add_f32_dpp v103, v103, v103 row_ror:8 row_mask:0xf bank_mask:0xf\n"
"v_cvt_pk_bf16_f32 v160, v96, v97\n"
"v_cvt_pk_bf16_f32 v161, v98, v99\n"
"v_cvt_pk_bf16_f32 v162, v100, v101\n"
"v_cvt_pk_bf16_f32 v163, v102, v103\n"
"buffer_store_dwordx4 v[160:163], v217, s[52:55], s77 offen sc1\n"
"buffer_load_dwordx2 v[212:213], v214, s[48:51], 0 offen sc1\n"
"s_mov_b32 s67, 49152\n"
"buffer_load_dwordx4 a[96:99], v198, s[44:47], s67 offen sc1\n"
"s_mov_b32 s67, 1097728\n"
"buffer_load_dwordx4 a[100:103], v199, s[44:47], s67 offen sc1\n"
"s_mov_b32 s67, 2146304\n"
"buffer_load_dwordx4 a[104:107], v200, s[44:47], s67 offen sc1\n"
"s_mov_b32 s67, 3194880\n"
"buffer_load_dwordx4 a[108:111], v201, s[44:47], s67 offen sc1\n"
"s_mov_b32 s67, 4243456\n"
"buffer_load_dwordx4 a[112:115], v202, s[44:47], s67 offen sc1\n"
"s_mov_b32 s67, 5292032\n"
"buffer_load_dwordx4 a[116:119], v203, s[44:47], s67 offen sc1\n"
"s_mov_b32 s67, 6340608\n"
"buffer_load_dwordx4 a[120:123], v204, s[44:47], s67 offen sc1\n"
"s_mov_b32 s67, 7389184\n"
"buffer_load_dwordx4 a[124:127], v205, s[44:47], s67 offen sc1\n"
"s_waitcnt vmcnt(25)\n"
"v_mfma_f32_16x16x32_bf16 v[104:107], a[240:243], a[32:35], 0\n"
"v_mfma_f32_16x16x32_bf16 v[108:111], a[244:247], a[32:35], 0\n"
"s_waitcnt vmcnt(24)\n"
"v_mfma_f32_16x16x32_bf16 v[104:107], a[240:243], a[36:39], v[104:107]\n"
"v_mfma_f32_16x16x32_bf16 v[108:111], a[244:247], a[36:39], v[108:111]\n"
"s_waitcnt vmcnt(23)\n"
"v_mfma_f32_16x16x32_bf16 v[104:107], a[240:243], a[40:43], v[104:107]\n"
"v_mfma_f32_16x16x32_bf16 v[108:111], a[244:247], a[40:43], v[108:111]\n"
"s_waitcnt vmcnt(22)\n"
"v_mfma_f32_16x16x32_bf16 v[104:107], a[240:243], a[44:47], v[104:107]\n"
"v_mfma_f32_16x16x32_bf16 v[108:111], a[244:247], a[44:47], v[108:111]\n"
"s_waitcnt vmcnt(21)\n"
"v_mfma_f32_16x16x32_bf16 v[104:107], a[240:243], a[48:51], v[104:107]\n"
"v_mfma_f32_16x16x32_bf16 v[108:111], a[244:247], a[48:51], v[108:111]\n"
"s_waitcnt vmcnt(20)\n"
"v_mfma_f32_16x16x32_bf16 v[104:107], a[240:243], a[52:55], v[104:107]\n"
"v_mfma_f32_16x16x32_bf16 v[108:111], a[244:247], a[52:55], v[108:111]\n"
"s_waitcnt vmcnt(19)\n"
"v_mfma_f32_16x16x32_bf16 v[104:107], a[240:243], a[56:59], v[104:107]\n"
"v_mfma_f32_16x16x32_bf16 v[108:111], a[244:247], a[56:59], v[108:111]\n"
"s_waitcnt vmcnt(18)\n"
"v_mfma_f32_16x16x32_bf16 v[104:107], a[240:243], a[60:63], v[104:107]\n"
"v_mfma_f32_16x16x32_bf16 v[108:111], a[244:247], a[60:63], v[108:111]\n"
"s_nop 6\n"
"v_add_f32_dpp v104, v104, v104 row_ror:8 row_mask:0xf bank_mask:0xf\n"
"v_add_f32_dpp v105, v105, v105 row_ror:8 row_mask:0xf bank_mask:0xf\n"
"v_add_f32_dpp v106, v106, v106 row_ror:8 row_mask:0xf bank_mask:0xf\n"
"v_add_f32_dpp v107, v107, v107 row_ror:8 row_mask:0xf bank_mask:0xf\n"
"v_add_f32_dpp v108, v108, v108 row_ror:8 row_mask:0xf bank_mask:0xf\n"
"v_add_f32_dpp v109, v109, v109 row_ror:8 row_mask:0xf bank_mask:0xf\n"
"v_add_f32_dpp v110, v110, v110 row_ror:8 row_mask:0xf bank_mask:0xf\n"
"v_add_f32_dpp v111, v111, v111 row_ror:8 row_mask:0xf bank_mask:0xf\n"
"v_cvt_pk_bf16_f32 v164, v104, v105\n"
"v_cvt_pk_bf16_f32 v165, v106, v107\n"
"v_cvt_pk_bf16_f32 v166, v108, v109\n"
"v_cvt_pk_bf16_f32 v167, v110, v111\n"
"buffer_store_dwordx4 v[164:167], v217, s[52:55], s78 offen sc1\n"
"s_waitcnt vmcnt(9)\n"
"v_cmp_ne_u32 s[58:59], s57, v213\n"
"v_and_b32 v213, 0xffff0007, v212\n"
"v_subrev_u32 v213, s56, v213\n"
"v_add_u32 v213, -7, v213\n"
"v_cmp_lt_u32 vcc, 0, v213\n"
"s_nop 0\n"
"s_or_b64 vcc, vcc, s[58:59]\n"
"s_cbranch_scc1 Lspin7a\n"
"Lok7a:\n"
"s_mov_b32 s67, 65536\n"
"buffer_load_dwordx4 a[128:131], v198, s[44:47], s67 offen sc1\n"
"s_mov_b32 s67, 1114112\n"
"buffer_load_dwordx4 a[132:135], v199, s[44:47], s67 offen sc1\n"
"s_mov_b32 s67, 2162688\n"
"buffer_load_dwordx4 a[136:139], v200, s[44:47], s67 offen sc1\n"
"s_mov_b32 s67, 3211264\n"
"buffer_load_dwordx4 a[140:143], v201, s[44:47], s67 offen sc1\n"
"s_mov_b32 s67, 4259840\n"
"buffer_load_dwordx4 a[144:147], v202, s[44:47], s67 offen sc1\n"
"s_mov_b32 s67, 5308416\n"
"buffer_load_dwordx4 a[148:151], v203, s[44:47], s67 offen sc1\n"
"s_mov_b32 s67, 6356992\n"
"buffer_load_dwordx4 a[152:155], v204, s[44:47], s67 offen sc1\n"
"s_mov_b32 s67, 7405568\n"
"buffer_load_dwordx4 a[156:159], v205, s[44:47], s67 offen sc1\n"
"v_mfma_f32_16x16x32_bf16 v[112:115], a[240:243], a[64:67], 0\n"
"v_mfma_f32_16x16x32_bf16 v[116:119], a[244:247], a[64:67], 0\n"
"v_mfma_f32_16x16x32_bf16 v[112:115], a[240:243], a[68:71], v[112:115]\n"
"v_mfma_f32_16x16x32_bf16 v[116:119], a[244:247], a[68:71], v[116:119]\n"
"v_mfma_f32_16x16x32_bf16 v[112:115], a[240:243], a[72:75], v[112:115]\n"
"v_mfma_f32_16x16x32_bf16 v[116:119], a[244:247], a[72:75], v[116:119]\n"
"v_mfma_f32_16x16x32_bf16 v[112:115], a[240:243], a[76:79], v[112:115]\n"
"v_mfma_f32_16x16x32_bf16 v[116:119], a[244:247], a[76:79], v[116:119]\n"
"v_mfma_f32_16x16x32_bf16 v[112:115], a[240:243], a[80:83], v[112:115]\n"
"v_mfma_f32_16x16x32_bf16 v[116:119], a[244:247], a[80:83], v[116:119]\n"
"v_mfma_f32_16x16x32_bf16 v[112:115], a[240:243], a[84:87], v[112:115]\n"
"v_mfma_f32_16x16x32_bf16 v[116:119], a[244:247], a[84:87], v[116:119]\n"
"v_mfma_f32_16x16x32_bf16 v[112:115], a[240:243], a[88:91], v[112:115]\n"
"v_mfma_f32_16x16x32_bf16 v[116:119], a[244:247], a[88:91], v[116:119]\n"
"v_mfma_f32_16x16x32_bf16 v[112:115], a[240:243], a[92:95], v[112:115]\n"
"v_mfma_f32_16x16x32_bf16 v[116:119], a[244:247], a[92:95], v[116:119]\n"
"s_nop 6\n"
"v_add_f32_dpp v112, v112, v112 row_ror:8 row_mask:0xf bank_mask:0xf\n"
"v_add_f32_dpp v113, v113, v113 row_ror:8 row_mask:0xf bank_mask:0xf\n"
"v_add_f32_dpp v114, v114, v114 row_ror:8 row_mask:0xf bank_mask:0xf\n"
"v_add_f32_dpp v115, v115, v115 row_ror:8 row_mask:0xf bank_mask:0xf\n"
"v_add_f32_dpp v116, v116, v116 row_ror:8 row_mask:0xf bank_mask:0xf\n"
"v_add_f32_dpp v117, v117, v117 row_ror:8 row_mask:0xf bank_mask:0xf\n"
"v_add_f32_dpp v118, v118, v118 row_ror:8 row_mask:0xf bank_mask:0xf\n"
"v_add_f32_dpp v119, v119, v119 row_ror:8 row_mask:0xf bank_mask:0xf\n"
"v_cvt_pk_bf16_f32 v168, v112, v113\n"
"v_cvt_pk_bf16_f32 v169, v114, v115\n"
"v_cvt_pk_bf16_f32 v170, v116, v117\n"
"v_cvt_pk_bf16_f32 v171, v118, v119\n"
"buffer_store_dwordx4 v[168:171], v217, s[52:55], s79 offen sc1\n"
"s_mov_b32 s67, 81920\n"
"buffer_load_dwordx4 a[160:163], v198, s[44:47], s67 offen sc1\n"
"s_mov_b32 s67, 1130496\n"
"buffer_load_dwordx4 a[164:167], v199, s[44:47], s67 offen sc1\n"
"s_mov_b32 s67, 2179072\n"
"buffer_load_dwordx4 a[168:171], v200, s[44:47], s67 offen sc1\n"
"s_mov_b32 s67, 3227648\n"
"buffer_load_dwordx4 a[172:175], v201, s[44:47], s67 offen sc1\n"
"s_mov_b32 s67, 4276224\n"
"buffer_load_dwordx4 a[176:179], v202, s[44:47], s67 offen sc1\n"
"s_mov_b32 s67, 5324800\n"
"buffer_load_dwordx4 a[180:183], v203, s[44:47], s67 offen sc1\n"
"s_mov_b32 s67, 6373376\n"
"buffer_load_dwordx4 a[184:187], v204, s[44:47], s67 offen sc1\n"
"s_mov_b32 s67, 7421952\n"
"buffer_load_dwordx4 a[188:191], v205, s[44:47], s67 offen sc1\n"
"s_waitcnt vmcnt(25)\n"
"v_mfma_f32_16x16x32_bf16 v[120:123], a[240:243], a[96:99], 0\n"
"v_mfma_f32_16x16x32_bf16 v[124:127], a[244:247], a[96:99], 0\n"
"s_waitcnt vmcnt(24)\n"
"v_mfma_f32_16x16x32_bf16 v[120:123], a[240:243], a[100:103], v[120:123]\n"
"v_mfma_f32_16x16x32_bf16 v[124:127], a[244:247], a[100:103], v[124:127]\n"
"s_waitcnt vmcnt(23)\n"
"v_mfma_f32_16x16x32_bf16 v[120:123], a[240:243], a[104:107], v[120:123]\n"
"v_mfma_f32_16x16x32_bf16 v[124:127], a[244:247], a[104:107], v[124:127]\n"
"s_waitcnt vmcnt(22)\n"
"v_mfma_f32_16x16x32_bf16 v[120:123], a[240:243], a[108:111], v[120:123]\n"
"v_mfma_f32_16x16x32_bf16 v[124:127], a[244:247], a[108:111], v[124:127]\n"
"s_waitcnt vmcnt(21)\n"
"v_mfma_f32_16x16x32_bf16 v[120:123], a[240:243], a[112:115], v[120:123]\n"
"v_mfma_f32_16x16x32_bf16 v[124:127], a[244:247], a[112:115], v[124:127]\n"
"s_waitcnt vmcnt(20)\n"
"v_mfma_f32_16x16x32_bf16 v[120:123], a[240:243], a[116:119], v[120:123]\n"
"v_mfma_f32_16x16x32_bf16 v[124:127], a[244:247], a[116:119], v[124:127]\n"
"s_waitcnt vmcnt(19)\n"
"v_mfma_f32_16x16x32_bf16 v[120:123], a[240:243], a[120:123], v[120:123]\n"
"v_mfma_f32_16x16x32_bf16 v[124:127], a[244:247], a[120:123], v[124:127]\n"
"s_waitcnt vmcnt(18)\n"
"v_mfma_f32_16x16x32_bf16 v[120:123], a[240:243], a[124:127], v[120:123]\n"
"v_mfma_f32_16x16x32_bf16 v[124:127], a[244:247], a[124:127], v[124:127]\n"
"s_nop 6\n"
"v_add_f32_dpp v120, v120, v120 row_ror:8 row_mask:0xf bank_mask:0xf\n"
"v_add_f32_dpp v121, v121, v121 row_ror:8 row_mask:0xf bank_mask:0xf\n"
"v_add_f32_dpp v122, v122, v122 row_ror:8 row_mask:0xf bank_mask:0xf\n"
"v_add_f32_dpp v123, v123, v123 row_ror:8 row_mask:0xf bank_mask:0xf\n"
"v_add_f32_dpp v124, v124, v124 row_ror:8 row_mask:0xf bank_mask:0xf\n"
"v_add_f32_dpp v125, v125, v125 row_ror:8 row_mask:0xf bank_mask:0xf\n"
"v_add_f32_dpp v126, v126, v126 row_ror:8 row_mask:0xf bank_mask:0xf\n"
"v_add_f32_dpp v127, v127, v127 row_ror:8 row_mask:0xf bank_mask:0xf\n"
"v_cvt_pk_bf16_f32 v172, v120, v121\n"
"v_cvt_pk_bf16_f32 v173, v122, v123\n"
"v_cvt_pk_bf16_f32 v174, v124, v125\n"
"v_cvt_pk_bf16_f32 v175, v126, v127\n"
"buffer_store_dwordx4 v[172:175], v217, s[52:55], s80 offen sc1\n"
"s_mov_b32 s67, 98304\n"
"buffer_load_dwordx4 v[0:3], v198, s[44:47], s67 offen sc1\n"
"s_mov_b32 s67, 1146880\n"
"buffer_load_dwordx4 v[4:7], v199, s[44:47], s67 offen sc1\n"
"s_mov_b32 s67, 2195456\n"
"buffer_load_dwordx4 v[8:11], v200, s[44:47], s67 offen sc1\n"
"s_mov_b32 s67, 3244032\n"
"buffer_load_dwordx4 v[12:15], v201, s[44:47], s67 offen sc1\n"
"s_mov_b32 s67, 4292608\n"
"buffer_load_dwordx4 v[16:19], v202, s[44:47], s67 offen sc1\n"
"s_mov_b32 s67, 5341184\n"
"buffer_load_dwordx4 v[20:23], v203, s[44:47], s67 offen sc1\n"
"s_mov_b32 s67, 6389760\n"
"buffer_load_dwordx4 v[24:27], v204, s[44:47], s67 offen sc1\n"
"s_mov_b32 s67, 7438336\n"
"buffer_load_dwordx4 v[28:31], v205, s[44:47], s67 offen sc1\n"
"buffer_store_dwordx2 v[254:255], v219, s[48:51], 0 offen sc1\n"
"s_waitcnt vmcnt(26)\n"
"v_mfma_f32_16x16x32_bf16 v[128:131], a[240:243], a[128:131], 0\n"
"v_mfma_f32_16x16x32_bf16 v[132:135], a[244:247], a[128:131], 0\n"
"s_waitcnt vmcnt(25)\n"
"v_mfma_f32_16x16x32_bf16 v[128:131], a[240:243], a[132:135], v[128:131]\n"
"v_mfma_f32_16x16x32_bf16 v[132:135], a[244:247], a[132:135], v[132:135]\n"
"s_waitcnt vmcnt(24)\n"
"v_mfma_f32_16x16x32_bf16 v[128:131], a[240:243], a[136:139], v[128:131]\n"
"v_mfma_f32_16x16x32_bf16 v[132:135], a[244:247], a[136:139], v[132:135]\n"
"s_waitcnt vmcnt(23)\n"
"v_mfma_f32_16x16x32_bf16 v[128:131], a[240:243], a[140:143], v[128:131]\n"
"v_mfma_f32_16x16x32_bf16 v[132:135], a[244:247], a[140:143], v[132:135]\n"
"s_waitcnt vmcnt(22)\n"
"v_mfma_f32_16x16x32_bf16 v[128:131], a[240:243], a[144:147], v[128:131]\n"
"v_mfma_f32_16x16x32_bf16 v[132:135], a[244:247], a[144:147], v[132:135]\n"
"s_waitcnt vmcnt(21)\n"
"v_mfma_f32_16x16x32_bf16 v[128:131], a[240:243], a[148:151], v[128:131]\n"
"v_mfma_f32_16x16x32_bf16 v[132:135], a[244:247], a[148:151], v[132:135]\n"
"s_waitcnt vmcnt(20)\n"
"v_mfma_f32_16x16x32_bf16 v[128:131], a[240:243], a[152:155], v[128:131]\n"
"v_mfma_f32_16x16x32_bf16 v[132:135], a[244:247], a[152:155], v[132:135]\n"
"s_waitcnt vmcnt(19)\n"
"v_mfma_f32_16x16x32_bf16 v[128:131], a[240:243], a[156:159], v[128:131]\n"
"v_mfma_f32_16x16x32_bf16 v[132:135], a[244:247], a[156:159], v[132:135]\n"
"s_waitcnt vmcnt(17)\n"
"v_mfma_f32_16x16x32_bf16 v[136:139], a[240:243], a[160:163], 0\n"
"v_mfma_f32_16x16x32_bf16 v[140:143], a[244:247], a[160:163], 0\n"
"s_waitcnt vmcnt(16)\n"
"v_mfma_f32_16x16x32_bf16 v[136:139], a[240:243], a[164:167], v[136:139]\n"
"v_mfma_f32_16x16x32_bf16 v[140:143], a[244:247], a[164:167], v[140:143]\n"
"s_nop 0\n"
"v_add_f32_dpp v128, v128, v128 row_ror:8 row_mask:0xf bank_mask:0xf\n"
"s_waitcnt vmcnt(15)\n"
"v_mfma_f32_16x16x32_bf16 v[136:139], a[240:243], a[168:171], v[136:139]\n"
"v_add_f32_dpp v129, v129, v129 row_ror:8 row_mask:0xf bank_mask:0xf\n"
"v_mfma_f32_16x16x32_bf16 v[140:143], a[244:247], a[168:171], v[140:143]\n"
"v_add_f32_dpp v130, v130, v130 row_ror:8 row_mask:0xf bank_mask:0xf\n"
"s_waitcnt vmcnt(14)\n"
"v_mfma_f32_16x16x32_bf16 v[136:139], a[240:243], a[172:175], v[136:139]\n"
"v_add_f32_dpp v131, v131, v131 row_ror:8 row_mask:0xf bank_mask:0xf\n"
"v_mfma_f32_16x16x32_bf16 v[140:143], a[244:247], a[172:175], v[140:143]\n"
"v_add_f32_dpp v132, v132, v132 row_ror:8 row_mask:0xf bank_mask:0xf\n"
"s_waitcnt vmcnt(13)\n"
"v_mfma_f32_16x16x32_bf16 v[136:139], a[240:243], a[176:179], v[136:139]\n"
"v_add_f32_dpp v133, v133, v133 row_ror:8 row_mask:0xf bank_mask:0xf\n"
"v_mfma_f32_16x16x32_bf16 v[140:143], a[244:247], a[176:179], v[140:143]\n"
"v_add_f32_dpp v134, v134, v134 row_ror:8 row_mask:0xf bank_mask:0xf\n"
"s_waitcnt vmcnt(12)\n"
"v_mfma_f32_16x16x32_bf16 v[136:139], a[240:243], a[180:183], v[136:139]\n"
"v_add_f32_dpp v135, v135, v135 row_ror:8 row_mask:0xf bank_mask:0xf\n"
"v_mfma_f32_16x16x32_bf16 v[140:143], a[244:247], a[180:183], v[140:143]\n"
"v_cvt_pk_bf16_f32 v176, v128, v129\n"
"s_waitcnt vmcnt(11)\n"
"v_mfma_f32_16x16x32_bf16 v[136:139], a[240:243], a[184:187], v[136:139]\n"
"v_cvt_pk_bf16_f32 v177, v130, v131\n"
"v_mfma_f32_16x16x32_bf16 v[140:143], a[244:247], a[184:187], v[140:143]\n"
"v_cvt_pk_bf16_f32 v178, v132, v133\n"
"s_waitcnt vmcnt(10)\n"
"v_mfma_f32_16x16x32_bf16 v[136:139], a[240:243], a[188:191], v[136:139]\n"
"v_cvt_pk_bf16_f32 v179, v134, v135\n"
"v_mfma_f32_16x16x32_bf16 v[140:143], a[244:247], a[188:191], v[140:143]\n"
"buffer_store_dwordx4 v[176:179], v217, s[52:55], s81 offen sc1\n"
"s_waitcnt vmcnt(9)\n"
"v_mfma_f32_16x16x32_bf16 v[144:147], a[240:243], v[0:3], 0\n"
"v_mfma_f32_16x16x32_bf16 v[148:151], a[244:247], v[0:3], 0\n"
"s_waitcnt vmcnt(8)\n"
"v_mfma_f32_16x16x32_bf16 v[144:147], a[240:243], v[4:7], v[144:147]\n"
"v_mfma_f32_16x16x32_bf16 v[148:151], a[244:247], v[4:7], v[148:151]\n"
"v_add_f32_dpp v136, v136, v136 row_ror:8 row_mask:0xf bank_mask:0xf\n"
"s_waitcnt vmcnt(7)\n"
"v_mfma_f32_16x16x32_bf16 v[144:147], a[240:243], v[8:11], v[144:147]\n"
"v_add_f32_dpp v137, v137, v137 row_ror:8 row_mask:0xf bank_mask:0xf\n"
"v_mfma_f32_16x16x32_bf16 v[148:151], a[244:247], v[8:11], v[148:151]\n"
"v_add_f32_dpp v138, v138, v138 row_ror:8 row_mask:0xf bank_mask:0xf\n"
"s_waitcnt vmcnt(6)\n"
"v_mfma_f32_16x16x32_bf16 v[144:147], a[240:243], v[12:15], v[144:147]\n"
"v_add_f32_dpp v139, v139, v139 row_ror:8 row_mask:0xf bank_mask:0xf\n"
"v_mfma_f32_16x16x32_bf16 v[148:151], a[244:247], v[12:15], v[148:151]\n"
"v_add_f32_dpp v140, v140, v140 row_ror:8 row_mask:0xf bank_mask:0xf\n"
"s_waitcnt vmcnt(5)\n"
"v_mfma_f32_16x16x32_bf16 v[144:147], a[240:243], v[16:19], v[144:147]\n"
"v_add_f32_dpp v141, v141, v141 row_ror:8 row_mask:0xf bank_mask:0xf\n"
"v_mfma_f32_16x16x32_bf16 v[148:151], a[244:247], v[16:19], v[148:151]\n"
"v_add_f32_dpp v142, v142, v142 row_ror:8 row_mask:0xf bank_mask:0xf\n"
"s_waitcnt vmcnt(4)\n"
"v_mfma_f32_16x16x32_bf16 v[144:147], a[240:243], v[20:23], v[144:147]\n"
"v_add_f32_dpp v143, v143, v143 row_ror:8 row_mask:0xf bank_mask:0xf\n"
"v_mfma_f32_16x16x32_bf16 v[148:151], a[244:247], v[20:23], v[148:151]\n"
"v_cvt_pk_bf16_f32 v180, v136, v137\n"
"s_waitcnt vmcnt(3)\n"
"v_mfma_f32_16x16x32_bf16 v[144:147], a[240:243], v[24:27], v[144:147]\n"
"v_cvt_pk_bf16_f32 v181, v138, v139\n"
"v_mfma_f32_16x16x32_bf16 v[148:151], a[244:247], v[24:27], v[148:151]\n"
"v_cvt_pk_bf16_f32 v182, v140, v141\n"
"s_waitcnt vmcnt(2)\n"
"v_mfma_f32_16x16x32_bf16 v[144:147], a[240:243], v[28:31], v[144:147]\n"
"v_cvt_pk_bf16_f32 v183, v142, v143\n"
"v_mfma_f32_16x16x32_bf16 v[148:151], a[244:247], v[28:31], v[148:151]\n"
"buffer_store_dwordx4 v[180:183], v217, s[52:55], s82 offen sc1\n"
"s_nop 4\n"
"v_add_f32_dpp v144, v144, v144 row_ror:8 row_mask:0xf bank_mask:0xf\n"
"v_add_f32_dpp v145, v145, v145 row_ror:8 row_mask:0xf bank_mask:0xf\n"
"v_add_f32_dpp v146, v146, v146 row_ror:8 row_mask:0xf bank_mask:0xf\n"
"v_add_f32_dpp v147, v147, v147 row_ror:8 row_mask:0xf bank_mask:0xf\n"
"v_add_f32_dpp v148, v148, v148 row_ror:8 row_mask:0xf bank_mask:0xf\n"
"v_add_f32_dpp v149, v149, v149 row_ror:8 row_mask:0xf bank_mask:0xf\n"
"v_add_f32_dpp v150, v150, v150 row_ror:8 row_mask:0xf bank_mask:0xf\n"
"v_add_f32_dpp v151, v151, v151 row_ror:8 row_mask:0xf bank_mask:0xf\n"
"v_cvt_pk_bf16_f32 v184, v144, v145\n"
"v_cvt_pk_bf16_f32 v185, v146, v147\n"
"v_cvt_pk_bf16_f32 v186, v148, v149\n"
"v_cvt_pk_bf16_f32 v187, v150, v151\n"
"buffer_store_dwordx4 v[184:187], v217, s[52:55], s83 offen sc1\n"
"Lfin:\n"
"s_endpgm\n"
"Lspin4a:\n"
"s_mov_b32 s61, 0x40000\n"
"Lspl4a:\n"
"s_sleep 2\n"
"buffer_load_dwordx2 v[212:213], v214, s[48:51], 0 offen sc1\n"
"s_sub_u32 s61, s61, 1\n"
"s_cbranch_scc1 Lok4a\n"
"s_waitcnt vmcnt(0)\n"
"v_cmp_ne_u32 s[58:59], s57, v213\n"
"v_and_b32 v213, 0xffff0007, v212\n"
"v_subrev_u32 v213, s56, v213\n"
"v_add_u32 v213, -4, v213\n"
"v_cmp_lt_u32 vcc, 3, v213\n"
"s_nop 0\n"
"s_or_b64 vcc, vcc, s[58:59]\n"
"s_cbranch_scc1 Lspl4a\n"
"s_branch Lok4a\n"
"Lspin7a:\n"
"s_mov_b32 s61, 0x40000\n"
"Lspl7a:\n"
"s_sleep 2\n"
"buffer_load_dwordx2 v[212:213], v214, s[48:51], 0 offen sc1\n"
"s_sub_u32 s61, s61, 1\n"
"s_cbranch_scc1 Lok7a\n"
"s_waitcnt vmcnt(0)\n"
"v_cmp_ne_u32 s[58:59], s57, v213\n"
"v_and_b32 v213, 0xffff0007, v212\n"
"v_subrev_u32 v213, s56, v213\n"
"v_add_u32 v213, -7, v213\n"
"v_cmp_lt_u32 vcc, 0, v213\n"
"s_nop 0\n"
"s_or_b64 vcc, vcc, s[58:59]\n"
"s_cbranch_scc1 Lspl7a\n"
"s_branch Lok7a\n"
"Lstub1:\n"
"s_call_b64 s[64:65], Lfix1\n"
"s_branch Lback1\n"
"Lstub2:\n"
"s_call_b64 s[64:65], Lfix0\n"
"s_branch Lback2\n"
"Lstub3:\n"
"s_call_b64 s[64:65], Lfix1\n"
"s_branch Lback3\n"
"Lstub4:\n"
"s_call_b64 s[64:65], Lfix0\n"
"s_branch Lback4\n"
"Lstub5:\n"
"s_call_b64 s[64:65], Lfix1\n"
"s_branch Lback5\n"
"Lstub6:\n"
"s_call_b64 s[64:65], Lfix0\n"
"s_branch Lback6\n"
"Lstub7:\n"
"s_call_b64 s[64:65], Lfix1\n"
"s_branch Lback7\n"
"Lstub8:\n"
"s_call_b64 s[64:65], Lfix0\n"
"s_branch Lback8\n"
"Lstub9:\n"
"s_call_b64 s[64:65], Lfix1\n"
"s_branch Lback9\n"
"Lstub10:\n"
"s_call_b64 s[64:65], Lfix0\n"
"s_branch Lback10\n"
"Lstub11:\n"
"s_call_b64 s[64:65], Lfix1\n"
"s_branch Lback11\n"
"Lstub12:\n"
"s_call_b64 s[64:65], Lfix0\n"
"s_branch Lback12\n"
"Lstub13:\n"
"s_call_b64 s[64:65], Lfix1\n"
"s_branch Lback13\n"
"Lstub14:\n"
"s_call_b64 s[64:65], Lfix0\n"
"s_branch Lback14\n"
"Lstub15:\n"
"s_call_b64 s[64:65], Lfix1\n"
"s_branch Lback15\n"
"Lcold:\n"
"Lfix0:\n"
"s_nop 7\n"
"s_nop 7\n"
"v_max3_f32 v223, v96, v97, v98\n"
"v_max3_f32 v224, v108, v109, v110\n"
"v_max3_f32 v223, v223, v99, v111\n"
"v_max_f32 v223, v223, v224\n"
"v_mov_b32 v224, v223\n"
"s_nop 1\n"
"v_permlane32_swap_b32 v223, v224\n"
"v_max_f32 v223, v223, v224\n"
"v_mov_b32 v224, v223\n"
"s_nop 1\n"
"v_permlane16_swap_b32 v223, v224\n"
"v_max_f32 v223, v223, v224\n"
"v_add_f32 v223, 0x41000000, v223\n"
"v_max_f32 v223, 0, v223\n"
"v_exp_f32 v252, -v223\n"
"v_sub_f32 v208, v208, v223\n"
"v_sub_f32 v209, v209, v223\n"
"v_sub_f32 v210, v210, v223\n"
"v_sub_f32 v211, v211, v223\n"
"v_sub_f32 v96, v96, v223\n"
"v_sub_f32 v97, v97, v223\n"
"v_sub_f32 v98, v98, v223\n"
"v_sub_f32 v99, v99, v223\n"
"v_sub_f32 v108, v108, v223\n"
"v_sub_f32 v109, v109, v223\n"
"v_sub_f32 v110, v110, v223\n"
"v_sub_f32 v111, v111, v223\n"
"v_sub_f32 v120, v120, v223\n"
"v_sub_f32 v121, v121, v223\n"
"v_sub_f32 v122, v122, v223\n"
"v_sub_f32 v123, v123, v223\n"
"v_sub_f32 v132, v132, v223\n"
"v_sub_f32 v133, v133, v223\n"
"v_sub_f32 v134, v134, v223\n"
"v_sub_f32 v135, v135, v223\n"
"v_accvgpr_read_b32 v225, a192\n"
"v_mul_f32 v225, v252, v225\n"
"v_accvgpr_write_b32 a192, v225\n"
"v_accvgpr_read_b32 v226, a193\n"
"v_mul_f32 v226, v252, v226\n"
"v_accvgpr_write_b32 a193, v226\n"
"v_accvgpr_read_b32 v227, a194\n"
"v_mul_f32 v227, v252, v227\n"
"v_accvgpr_write_b32 a194, v227\n"
"v_accvgpr_read_b32 v220, a195\n"
"v_mul_f32 v220, v252, v220\n"
"v_accvgpr_write_b32 a195, v220\n"
"v_max3_f32 v221, v100, v101, v102\n"
"v_max3_f32 v222, v112, v113, v114\n"
"v_max3_f32 v221, v221, v103, v115\n"
"v_max_f32 v221, v221, v222\n"
"v_mov_b32 v222, v221\n"
"s_nop 1\n"
"v_permlane32_swap_b32 v221, v222\n"
"v_max_f32 v221, v221, v222\n"
"v_mov_b32 v222, v221\n"
"s_nop 1\n"
"v_permlane16_swap_b32 v221, v222\n"
"v_max_f32 v221, v221, v222\n"
"v_add_f32 v221, 0x41000000, v221\n"
"v_max_f32 v221, 0, v221\n"
"v_exp_f32 v253, -v221\n"
"v_sub_f32 v212, v212, v221\n"
"v_sub_f32 v213, v213, v221\n"
"v_sub_f32 v214, v214, v221\n"
"v_sub_f32 v215, v215, v221\n"
"v_sub_f32 v100, v100, v221\n"
"v_sub_f32 v101, v101, v221\n"
"v_sub_f32 v102, v102, v221\n"
"v_sub_f32 v103, v103, v221\n"
"v_sub_f32 v112, v112, v221\n"
"v_sub_f32 v113, v113, v221\n"
"v_sub_f32 v114, v114, v221\n"
"v_sub_f32 v115, v115, v221\n"
"v_sub_f32 v124, v124, v221\n"
"v_sub_f32 v125, v125, v221\n"
"v_sub_f32 v126, v126, v221\n"
"v_sub_f32 v127, v127, v221\n"
"v_sub_f32 v136, v136, v221\n"
"v_sub_f32 v137, v137, v221\n"
"v_sub_f32 v138, v138, v221\n"
"v_sub_f32 v139, v139, v221\n"
"v_accvgpr_read_b32 v223, a196\n"
"v_mul_f32 v223, v253, v223\n"
"v_accvgpr_write_b32 a196, v223\n"
"v_accvgpr_read_b32 v224, a197\n"
"v_mul_f32 v224, v253, v224\n"
"v_accvgpr_write_b32 a197, v224\n"
"v_accvgpr_read_b32 v225, a198\n"
"v_mul_f32 v225, v253, v225\n"
"v_accvgpr_write_b32 a198, v225\n"
"v_accvgpr_read_b32 v226, a199\n"
"v_mul_f32 v226, v253, v226\n"
"v_accvgpr_write_b32 a199, v226\n"
"v_max3_f32 v227, v104, v105, v106\n"
"v_max3_f32 v220, v116, v117, v118\n"
"v_max3_f32 v227, v227, v107, v119\n"
"v_max_f32 v227, v227, v220\n"
"v_mov_b32 v220, v227\n"
"s_nop 1\n"
"v_permlane32_swap_b32 v227, v220\n"
"v_max_f32 v227, v227, v220\n"
"v_mov_b32 v220, v227\n"
"s_nop 1\n"
"v_permlane16_swap_b32 v227, v220\n"
"v_max_f32 v227, v227, v220\n"
"v_add_f32 v227, 0x41000000, v227\n"
"v_max_f32 v227, 0, v227\n"
"v_exp_f32 v254, -v227\n"
"v_sub_f32 v216, v216, v227\n"
"v_sub_f32 v217, v217, v227\n"
"v_sub_f32 v218, v218, v227\n"
"v_sub_f32 v219, v219, v227\n"
"v_sub_f32 v104, v104, v227\n"
"v_sub_f32 v105, v105, v227\n"
"v_sub_f32 v106, v106, v227\n"
"v_sub_f32 v107, v107, v227\n"
"v_sub_f32 v116, v116, v227\n"
"v_sub_f32 v117, v117, v227\n"
"v_sub_f32 v118, v118, v227\n"
"v_sub_f32 v119, v119, v227\n"
"v_sub_f32 v128, v128, v227\n"
"v_sub_f32 v129, v129, v227\n"
"v_sub_f32 v130, v130, v227\n"
"v_sub_f32 v131, v131, v227\n"
"v_sub_f32 v140, v140, v227\n"
"v_sub_f32 v141, v141, v227\n"
"v_sub_f32 v142, v142, v227\n"
"v_sub_f32 v143, v143, v227\n"
"v_accvgpr_read_b32 v221, a200\n"
"v_mul_f32 v221, v254, v221\n"
"v_accvgpr_write_b32 a200, v221\n"
"v_accvgpr_read_b32 v222, a201\n"
"v_mul_f32 v222, v254, v222\n"
"v_accvgpr_write_b32 a201, v222\n"
"v_accvgpr_read_b32 v223, a202\n"
"v_mul_f32 v223, v254, v223\n"
"v_accvgpr_write_b32 a202, v223\n"
"v_accvgpr_read_b32 v224, a203\n"
"v_mul_f32 v224, v254, v224\n"
"v_accvgpr_write_b32 a203, v224\n"
"v_exp_f32 v225, v96\n"
"v_exp_f32 v226, v97\n"
"s_nop 0\n"
"v_cvt_pk_bf16_f32 v144, v225, v226\n"
"v_exp_f32 v227, v98\n"
"v_exp_f32 v220, v99\n"
"s_nop 0\n"
"v_cvt_pk_bf16_f32 v145, v227, v220\n"
"v_exp_f32 v221, v100\n"
"v_exp_f32 v222, v101\n"
"s_nop 0\n"
"v_cvt_pk_bf16_f32 v148, v221, v222\n"
"v_exp_f32 v223, v102\n"
"v_exp_f32 v224, v103\n"
"s_nop 0\n"
"v_cvt_pk_bf16_f32 v149, v223, v224\n"
"v_exp_f32 v225, v104\n"
"v_exp_f32 v226, v105\n"
"s_nop 0\n"
"v_cvt_pk_bf16_f32 v152, v225, v226\n"
"v_exp_f32 v227, v106\n"
"v_exp_f32 v220, v107\n"
"s_nop 0\n"
"v_cvt_pk_bf16_f32 v153, v227, v220\n"
"v_exp_f32 v221, v108\n"
"v_exp_f32 v222, v109\n"
"s_nop 0\n"
"v_cvt_pk_bf16_f32 v146, v221, v222\n"
"v_exp_f32 v223, v110\n"
"v_exp_f32 v224, v111\n"
"s_nop 0\n"
"v_cvt_pk_bf16_f32 v147, v223, v224\n"
"v_exp_f32 v225, v112\n"
"v_exp_f32 v226, v113\n"
"s_nop 0\n"
"v_cvt_pk_bf16_f32 v150, v225, v226\n"
"v_exp_f32 v227, v114\n"
"v_exp_f32 v220, v115\n"
"s_nop 0\n"
"v_cvt_pk_bf16_f32 v151, v227, v220\n"
"v_exp_f32 v221, v116\n"
"v_exp_f32 v222, v117\n"
"s_nop 0\n"
"v_cvt_pk_bf16_f32 v154, v221, v222\n"
"v_exp_f32 v223, v118\n"
"v_exp_f32 v224, v119\n"
"s_nop 0\n"
"v_cvt_pk_bf16_f32 v155, v223, v224\n"
"v_mfma_f32_16x16x32_bf16 a[0:3], a[204:207], v[144:147], a[192:195]\n"
"v_mfma_f32_16x16x32_bf16 a[4:7], a[204:207], v[148:151], a[196:199]\n"
"v_mfma_f32_16x16x32_bf16 a[8:11], a[204:207], v[152:155], a[200:203]\n"
"s_branch Lresc\n"
"Lfix1:\n"
"s_nop 7\n"
"s_nop 7\n"
"v_max3_f32 v225, v120, v121, v122\n"
"v_max3_f32 v226, v132, v133, v134\n"
"v_max3_f32 v225, v225, v123, v135\n"
"v_max_f32 v225, v225, v226\n"
"v_mov_b32 v226, v225\n"
"s_nop 1\n"
"v_permlane32_swap_b32 v225, v226\n"
"v_max_f32 v225, v225, v226\n"
"v_mov_b32 v226, v225\n"
"s_nop 1\n"
"v_permlane16_swap_b32 v225, v226\n"
"v_max_f32 v225, v225, v226\n"
"v_add_f32 v225, 0x41000000, v225\n"
"v_max_f32 v225, 0, v225\n"
"v_exp_f32 v252, -v225\n"
"v_sub_f32 v208, v208, v225\n"
"v_sub_f32 v209, v209, v225\n"
"v_sub_f32 v210, v210, v225\n"
"v_sub_f32 v211, v211, v225\n"
"v_sub_f32 v96, v96, v225\n"
"v_sub_f32 v97, v97, v225\n"
"v_sub_f32 v98, v98, v225\n"
"v_sub_f32 v99, v99, v225\n"
"v_sub_f32 v108, v108, v225\n"
"v_sub_f32 v109, v109, v225\n"
"v_sub_f32 v110, v110, v225\n"
"v_sub_f32 v111, v111, v225\n"
"v_sub_f32 v120, v120, v225\n"
"v_sub_f32 v121, v121, v225\n"
"v_sub_f32 v122, v122, v225\n"
"v_sub_f32 v123, v123, v225\n"
"v_sub_f32 v132, v132, v225\n"
"v_sub_f32 v133, v133, v225\n"
"v_sub_f32 v134, v134, v225\n"
"v_sub_f32 v135, v135, v225\n"
"v_accvgpr_read_b32 v227, a0\n"
"v_mul_f32 v227, v252, v227\n"
"v_accvgpr_write_b32 a0, v227\n"
"v_accvgpr_read_b32 v220, a1\n"
"v_mul_f32 v220, v252, v220\n"
"v_accvgpr_write_b32 a1, v220\n"
"v_accvgpr_read_b32 v221, a2\n"
"v_mul_f32 v221, v252, v221\n"
"v_accvgpr_write_b32 a2, v221\n"
"v_accvgpr_read_b32 v222, a3\n"
"v_mul_f32 v222, v252, v222\n"
"v_accvgpr_write_b32 a3, v222\n"
"v_max3_f32 v223, v124, v125, v126\n"
"v_max3_f32 v224, v136, v137, v138\n"
"v_max3_f32 v223, v223, v127, v139\n"
"v_max_f32 v223, v223, v224\n"
"v_mov_b32 v224, v223\n"
"s_nop 1\n"
"v_permlane32_swap_b32 v223, v224\n"
"v_max_f32 v223, v223, v224\n"
"v_mov_b32 v224, v223\n"
"s_nop 1\n"
"v_permlane16_swap_b32 v223, v224\n"
"v_max_f32 v223, v223, v224\n"
"v_add_f32 v223, 0x41000000, v223\n"
"v_max_f32 v223, 0, v223\n"
"v_exp_f32 v253, -v223\n"
"v_sub_f32 v212, v212, v223\n"
"v_sub_f32 v213, v213, v223\n"
"v_sub_f32 v214, v214, v223\n"
"v_sub_f32 v215, v215, v223\n"
"v_sub_f32 v100, v100, v223\n"
"v_sub_f32 v101, v101, v223\n"
"v_sub_f32 v102, v102, v223\n"
"v_sub_f32 v103, v103, v223\n"
"v_sub_f32 v112, v112, v223\n"
"v_sub_f32 v113, v113, v223\n"
"v_sub_f32 v114, v114, v223\n"
"v_sub_f32 v115, v115, v223\n"
"v_sub_f32 v124, v124, v223\n"
"v_sub_f32 v125, v125, v223\n"
"v_sub_f32 v126, v126, v223\n"
"v_sub_f32 v127, v127, v223\n"
"v_sub_f32 v136, v136, v223\n"
"v_sub_f32 v137, v137, v223\n"
"v_sub_f32 v138, v138, v223\n"
"v_sub_f32 v139, v139, v223\n"
"v_accvgpr_read_b32 v225, a4\n"
"v_mul_f32 v225, v253, v225\n"
"v_accvgpr_write_b32 a4, v225\n"
"v_accvgpr_read_b32 v226, a5\n"
"v_mul_f32 v226, v253, v226\n"
"v_accvgpr_write_b32 a5, v226\n"
"v_accvgpr_read_b32 v227, a6\n"
"v_mul_f32 v227, v253, v227\n"
"v_accvgpr_write_b32 a6, v227\n"
"v_accvgpr_read_b32 v220, a7\n"
"v_mul_f32 v220, v253, v220\n"
"v_accvgpr_write_b32 a7, v220\n"
"v_max3_f32 v221, v128, v129, v130\n"
"v_max3_f32 v222, v140, v141, v142\n"
"v_max3_f32 v221, v221, v131, v143\n"
"v_max_f32 v221, v221, v222\n"
"v_mov_b32 v222, v221\n"
"s_nop 1\n"
"v_permlane32_swap_b32 v221, v222\n"
"v_max_f32 v221, v221, v222\n"
"v_mov_b32 v222, v221\n"
"s_nop 1\n"
"v_permlane16_swap_b32 v221, v222\n"
"v_max_f32 v221, v221, v222\n"
"v_add_f32 v221, 0x41000000, v221\n"
"v_max_f32 v221, 0, v221\n"
"v_exp_f32 v254, -v221\n"
"v_sub_f32 v216, v216, v221\n"
"v_sub_f32 v217, v217, v221\n"
"v_sub_f32 v218, v218, v221\n"
"v_sub_f32 v219, v219, v221\n"
"v_sub_f32 v104, v104, v221\n"
"v_sub_f32 v105, v105, v221\n"
"v_sub_f32 v106, v106, v221\n"
"v_sub_f32 v107, v107, v221\n"
"v_sub_f32 v116, v116, v221\n"
"v_sub_f32 v117, v117, v221\n"
"v_sub_f32 v118, v118, v221\n"
"v_sub_f32 v119, v119, v221\n"
"v_sub_f32 v128, v128, v221\n"
"v_sub_f32 v129, v129, v221\n"
"v_sub_f32 v130, v130, v221\n"
"v_sub_f32 v131, v131, v221\n"
"v_sub_f32 v140, v140, v221\n"
"v_sub_f32 v141, v141, v221\n"
"v_sub_f32 v142, v142, v221\n"
"v_sub_f32 v143, v143, v221\n"
"v_accvgpr_read_b32 v223, a8\n"
"v_mul_f32 v223, v254, v223\n"
"v_accvgpr_write_b32 a8, v223\n"
"v_accvgpr_read_b32 v224, a9\n"
"v_mul_f32 v224, v254, v224\n"
"v_accvgpr_write_b32 a9, v224\n"
"v_accvgpr_read_b32 v225, a10\n"
"v_mul_f32 v225, v254, v225\n"
"v_accvgpr_write_b32 a10, v225\n"
"v_accvgpr_read_b32 v226, a11\n"
"v_mul_f32 v226, v254, v226\n"
"v_accvgpr_write_b32 a11, v226\n"
"v_exp_f32 v227, v120\n"
"v_exp_f32 v220, v121\n"
"s_nop 0\n"
"v_cvt_pk_bf16_f32 v156, v227, v220\n"
"v_exp_f32 v221, v122\n"
"v_exp_f32 v222, v123\n"
"s_nop 0\n"
"v_cvt_pk_bf16_f32 v157, v221, v222\n"
"v_exp_f32 v223, v124\n"
"v_exp_f32 v224, v125\n"
"s_nop 0\n"
"v_cvt_pk_bf16_f32 v160, v223, v224\n"
"v_exp_f32 v225, v126\n"
"v_exp_f32 v226, v127\n"
"s_nop 0\n"
"v_cvt_pk_bf16_f32 v161, v225, v226\n"
"v_exp_f32 v227, v128\n"
"v_exp_f32 v220, v129\n"
"s_nop 0\n"
"v_cvt_pk_bf16_f32 v164, v227, v220\n"
"v_exp_f32 v221, v130\n"
"v_exp_f32 v222, v131\n"
"s_nop 0\n"
"v_cvt_pk_bf16_f32 v165, v221, v222\n"
"v_exp_f32 v223, v132\n"
"v_exp_f32 v224, v133\n"
"s_nop 0\n"
"v_cvt_pk_bf16_f32 v158, v223, v224\n"
"v_exp_f32 v225, v134\n"
"v_exp_f32 v226, v135\n"
"s_nop 0\n"
"v_cvt_pk_bf16_f32 v159, v225, v226\n"
"v_exp_f32 v227, v136\n"
"v_exp_f32 v220, v137\n"
"s_nop 0\n"
"v_cvt_pk_bf16_f32 v162, v227, v220\n"
"v_exp_f32 v221, v138\n"
"v_exp_f32 v222, v139\n"
"s_nop 0\n"
"v_cvt_pk_bf16_f32 v163, v221, v222\n"
"v_exp_f32 v223, v140\n"
"v_exp_f32 v224, v141\n"
"s_nop 0\n"
"v_cvt_pk_bf16_f32 v166, v223, v224\n"
"v_exp_f32 v225, v142\n"
"v_exp_f32 v226, v143\n"
"s_nop 0\n"
"v_cvt_pk_bf16_f32 v167, v225, v226\n"
"v_mfma_f32_16x16x32_bf16 a[192:195], a[204:207], v[156:159], a[0:3]\n"
"v_mfma_f32_16x16x32_bf16 a[196:199], a[204:207], v[160:163], a[4:7]\n"
"v_mfma_f32_16x16x32_bf16 a[200:203], a[204:207], v[164:167], a[8:11]\n"
"Lresc:\n"
"v_mul_f32 v168, v252, v168\n"
"v_mul_f32 v169, v252, v169\n"
"v_mul_f32 v170, v252, v170\n"
"v_mul_f32 v171, v252, v171\n"
"v_mul_f32 v172, v253, v172\n"
"v_mul_f32 v173, v253, v173\n"
"v_mul_f32 v174, v253, v174\n"
"v_mul_f32 v175, v253, v175\n"
"v_mul_f32 v176, v254, v176\n"
"v_mul_f32 v177, v254, v177\n"
"v_mul_f32 v178, v254, v178\n"
"v_mul_f32 v179, v254, v179\n"
"v_mul_f32 v180, v252, v180\n"
"v_mul_f32 v181, v252, v181\n"
"v_mul_f32 v182, v252, v182\n"
"v_mul_f32 v183, v252, v183\n"
"v_mul_f32 v184, v253, v184\n"
"v_mul_f32 v185, v253, v185\n"
"v_mul_f32 v186, v253, v186\n"
"v_mul_f32 v187, v253, v187\n"
"v_mul_f32 v188, v254, v188\n"
"v_mul_f32 v189, v254, v189\n"
"v_mul_f32 v190, v254, v190\n"
"v_mul_f32 v191, v254, v191\n"
"v_mul_f32 v228, v252, v228\n"
"v_mul_f32 v229, v252, v229\n"
"v_mul_f32 v230, v252, v230\n"
"v_mul_f32 v231, v252, v231\n"
"v_mul_f32 v232, v253, v232\n"
"v_mul_f32 v233, v253, v233\n"
"v_mul_f32 v234, v253, v234\n"
"v_mul_f32 v235, v253, v235\n"
"v_mul_f32 v236, v254, v236\n"
"v_mul_f32 v237, v254, v237\n"
"v_mul_f32 v238, v254, v238\n"
"v_mul_f32 v239, v254, v239\n"
"v_mul_f32 v240, v252, v240\n"
"v_mul_f32 v241, v252, v241\n"
"v_mul_f32 v242, v252, v242\n"
"v_mul_f32 v243, v252, v243\n"
"v_mul_f32 v244, v253, v244\n"
"v_mul_f32 v245, v253, v245\n"
"v_mul_f32 v246, v253, v246\n"
"v_mul_f32 v247, v253, v247\n"
"v_mul_f32 v248, v254, v248\n"
"v_mul_f32 v249, v254, v249\n"
"v_mul_f32 v250, v254, v250\n"
"v_mul_f32 v251, v254, v251\n"
"v_accvgpr_read_b32 v227, a48\n"
"v_mul_f32 v227, v252, v227\n"
"v_accvgpr_write_b32 a48, v227\n"
"v_accvgpr_read_b32 v220, a49\n"
"v_mul_f32 v220, v252, v220\n"
"v_accvgpr_write_b32 a49, v220\n"
"v_accvgpr_read_b32 v221, a50\n"
"v_mul_f32 v221, v252, v221\n"
"v_accvgpr_write_b32 a50, v221\n"
"v_accvgpr_read_b32 v222, a51\n"
"v_mul_f32 v222, v252, v222\n"
"v_accvgpr_write_b32 a51, v222\n"
"v_accvgpr_read_b32 v223, a52\n"
"v_mul_f32 v223, v253, v223\n"
"v_accvgpr_write_b32 a52, v223\n"
"v_accvgpr_read_b32 v224, a53\n"
"v_mul_f32 v224, v253, v224\n"
"v_accvgpr_write_b32 a53, v224\n"
"v_accvgpr_read_b32 v225, a54\n"
"v_mul_f32 v225, v253, v225\n"
"v_accvgpr_write_b32 a54, v225\n"
"v_accvgpr_read_b32 v226, a55\n"
"v_mul_f32 v226, v253, v226\n"
"v_accvgpr_write_b32 a55, v226\n"
"v_accvgpr_read_b32 v227, a56\n"
"v_mul_f32 v227, v254, v227\n"
"v_accvgpr_write_b32 a56, v227\n"
"v_accvgpr_read_b32 v220, a57\n"
"v_mul_f32 v220, v254, v220\n"
"v_accvgpr_write_b32 a57, v220\n"
"v_accvgpr_read_b32 v221, a58\n"
"v_mul_f32 v221, v254, v221\n"
"v_accvgpr_write_b32 a58, v221\n"
"v_accvgpr_read_b32 v222, a59\n"
"v_mul_f32 v222, v254, v222\n"
"v_accvgpr_write_b32 a59, v222\n"
"v_accvgpr_read_b32 v223, a60\n"
"v_mul_f32 v223, v252, v223\n"
"v_accvgpr_write_b32 a60, v223\n"
"v_accvgpr_read_b32 v224, a61\n"
"v_mul_f32 v224, v252, v224\n"
"v_accvgpr_write_b32 a61, v224\n"
"v_accvgpr_read_b32 v225, a62\n"
"v_mul_f32 v225, v252, v225\n"
"v_accvgpr_write_b32 a62, v225\n"
"v_accvgpr_read_b32 v226, a63\n"
"v_mul_f32 v226, v252, v226\n"
"v_accvgpr_write_b32 a63, v226\n"
"v_accvgpr_read_b32 v227, a64\n"
"v_mul_f32 v227, v253, v227\n"
"v_accvgpr_write_b32 a64, v227\n"
"v_accvgpr_read_b32 v220, a65\n"
"v_mul_f32 v220, v253, v220\n"
"v_accvgpr_write_b32 a65, v220\n"
"v_accvgpr_read_b32 v221, a66\n"
"v_mul_f32 v221, v253, v221\n"
"v_accvgpr_write_b32 a66, v221\n"
"v_accvgpr_read_b32 v222, a67\n"
"v_mul_f32 v222, v253, v222\n"
"v_accvgpr_write_b32 a67, v222\n"
"v_accvgpr_read_b32 v223, a68\n"
"v_mul_f32 v223, v254, v223\n"
"v_accvgpr_write_b32 a68, v223\n"
"v_accvgpr_read_b32 v224, a69\n"
"v_mul_f32 v224, v254, v224\n"
"v_accvgpr_write_b32 a69, v224\n"
"v_accvgpr_read_b32 v225, a70\n"
"v_mul_f32 v225, v254, v225\n"
"v_accvgpr_write_b32 a70, v225\n"
"v_accvgpr_read_b32 v226, a71\n"
"v_mul_f32 v226, v254, v226\n"
"v_accvgpr_write_b32 a71, v226\n"
"v_accvgpr_read_b32 v227, a72\n"
"v_mul_f32 v227, v252, v227\n"
"v_accvgpr_write_b32 a72, v227\n"
"v_accvgpr_read_b32 v220, a73\n"
"v_mul_f32 v220, v252, v220\n"
"v_accvgpr_write_b32 a73, v220\n"
"v_accvgpr_read_b32 v221, a74\n"
"v_mul_f32 v221, v252, v221\n"
"v_accvgpr_write_b32 a74, v221\n"
"v_accvgpr_read_b32 v222, a75\n"
"v_mul_f32 v222, v252, v222\n"
"v_accvgpr_write_b32 a75, v222\n"
"v_accvgpr_read_b32 v223, a76\n"
"v_mul_f32 v223, v253, v223\n"
"v_accvgpr_write_b32 a76, v223\n"
"v_accvgpr_read_b32 v224, a77\n"
"v_mul_f32 v224, v253, v224\n"
"v_accvgpr_write_b32 a77, v224\n"
"v_accvgpr_read_b32 v225, a78\n"
"v_mul_f32 v225, v253, v225\n"
"v_accvgpr_write_b32 a78, v225\n"
"v_accvgpr_read_b32 v226, a79\n"
"v_mul_f32 v226, v253, v226\n"
"v_accvgpr_write_b32 a79, v226\n"
"v_accvgpr_read_b32 v227, a80\n"
"v_mul_f32 v227, v254, v227\n"
"v_accvgpr_write_b32 a80, v227\n"
"v_accvgpr_read_b32 v220, a81\n"
"v_mul_f32 v220, v254, v220\n"
"v_accvgpr_write_b32 a81, v220\n"
"v_accvgpr_read_b32 v221, a82\n"
"v_mul_f32 v221, v254, v221\n"
"v_accvgpr_write_b32 a82, v221\n"
"v_accvgpr_read_b32 v222, a83\n"
"v_mul_f32 v222, v254, v222\n"
"v_accvgpr_write_b32 a83, v222\n"
"v_accvgpr_read_b32 v223, a84\n"
"v_mul_f32 v223, v252, v223\n"
"v_accvgpr_write_b32 a84, v223\n"
"v_accvgpr_read_b32 v224, a85\n"
"v_mul_f32 v224, v252, v224\n"
"v_accvgpr_write_b32 a85, v224\n"
"v_accvgpr_read_b32 v225, a86\n"
"v_mul_f32 v225, v252, v225\n"
"v_accvgpr_write_b32 a86, v225\n"
"v_accvgpr_read_b32 v226, a87\n"
"v_mul_f32 v226, v252, v226\n"
"v_accvgpr_write_b32 a87, v226\n"
"v_accvgpr_read_b32 v227, a88\n"
"v_mul_f32 v227, v253, v227\n"
"v_accvgpr_write_b32 a88, v227\n"
"v_accvgpr_read_b32 v220, a89\n"
"v_mul_f32 v220, v253, v220\n"
"v_accvgpr_write_b32 a89, v220\n"
"v_accvgpr_read_b32 v221, a90\n"
"v_mul_f32 v221, v253, v221\n"
"v_accvgpr_write_b32 a90, v221\n"
"v_accvgpr_read_b32 v222, a91\n"
"v_mul_f32 v222, v253, v222\n"
"v_accvgpr_write_b32 a91, v222\n"
"v_accvgpr_read_b32 v223, a92\n"
"v_mul_f32 v223, v254, v223\n"
"v_accvgpr_write_b32 a92, v223\n"
"v_accvgpr_read_b32 v224, a93\n"
"v_mul_f32 v224, v254, v224\n"
"v_accvgpr_write_b32 a93, v224\n"
"v_accvgpr_read_b32 v225, a94\n"
"v_mul_f32 v225, v254, v225\n"
"v_accvgpr_write_b32 a94, v225\n"
"v_accvgpr_read_b32 v226, a95\n"
"v_mul_f32 v226, v254, v226\n"
"v_accvgpr_write_b32 a95, v226\n"
"v_accvgpr_read_b32 v227, a96\n"
"v_mul_f32 v227, v252, v227\n"
"v_accvgpr_write_b32 a96, v227\n"
"v_accvgpr_read_b32 v220, a97\n"
"v_mul_f32 v220, v252, v220\n"
"v_accvgpr_write_b32 a97, v220\n"
"v_accvgpr_read_b32 v221, a98\n"
"v_mul_f32 v221, v252, v221\n"
"v_accvgpr_write_b32 a98, v221\n"
"v_accvgpr_read_b32 v222, a99\n"
"v_mul_f32 v222, v252, v222\n"
"v_accvgpr_write_b32 a99, v222\n"
"v_accvgpr_read_b32 v223, a100\n"
"v_mul_f32 v223, v253, v223\n"
"v_accvgpr_write_b32 a100, v223\n"
"v_accvgpr_read_b32 v224, a101\n"
"v_mul_f32 v224, v253, v224\n"
"v_accvgpr_write_b32 a101, v224\n"
"v_accvgpr_read_b32 v225, a102\n"
"v_mul_f32 v225, v253, v225\n"
"v_accvgpr_write_b32 a102, v225\n"
"v_accvgpr_read_b32 v226, a103\n"
"v_mul_f32 v226, v253, v226\n"
"v_accvgpr_write_b32 a103, v226\n"
"v_accvgpr_read_b32 v227, a104\n"
"v_mul_f32 v227, v254, v227\n"
"v_accvgpr_write_b32 a104, v227\n"
"v_accvgpr_read_b32 v220, a105\n"
"v_mul_f32 v220, v254, v220\n"
"v_accvgpr_write_b32 a105, v220\n"
"v_accvgpr_read_b32 v221, a106\n"
"v_mul_f32 v221, v254, v221\n"
"v_accvgpr_write_b32 a106, v221\n"
"v_accvgpr_read_b32 v222, a107\n"
"v_mul_f32 v222, v254, v222\n"
"v_accvgpr_write_b32 a107, v222\n"
"v_accvgpr_read_b32 v223, a108\n"
"v_mul_f32 v223, v252, v223\n"
"v_accvgpr_write_b32 a108, v223\n"
"v_accvgpr_read_b32 v224, a109\n"
"v_mul_f32 v224, v252, v224\n"
"v_accvgpr_write_b32 a109, v224\n"
"v_accvgpr_read_b32 v225, a110\n"
"v_mul_f32 v225, v252, v225\n"
"v_accvgpr_write_b32 a110, v225\n"
"v_accvgpr_read_b32 v226, a111\n"
"v_mul_f32 v226, v252, v226\n"
"v_accvgpr_write_b32 a111, v226\n"
"v_accvgpr_read_b32 v227, a112\n"
"v_mul_f32 v227, v253, v227\n"
"v_accvgpr_write_b32 a112, v227\n"
"v_accvgpr_read_b32 v220, a113\n"
"v_mul_f32 v220, v253, v220\n"
"v_accvgpr_write_b32 a113, v220\n"
"v_accvgpr_read_b32 v221, a114\n"
"v_mul_f32 v221, v253, v221\n"
"v_accvgpr_write_b32 a114, v221\n"
"v_accvgpr_read_b32 v222, a115\n"
"v_mul_f32 v222, v253, v222\n"
"v_accvgpr_write_b32 a115, v222\n"
"v_accvgpr_read_b32 v223, a116\n"
"v_mul_f32 v223, v254, v223\n"
"v_accvgpr_write_b32 a116, v223\n"
"v_accvgpr_read_b32 v224, a117\n"
"v_mul_f32 v224, v254, v224\n"
"v_accvgpr_write_b32 a117, v224\n"
"v_accvgpr_read_b32 v225, a118\n"
"v_mul_f32 v225, v254, v225\n"
"v_accvgpr_write_b32 a118, v225\n"
"v_accvgpr_read_b32 v226, a119\n"
"v_mul_f32 v226, v254, v226\n"
"v_accvgpr_write_b32 a119, v226\n"
"v_accvgpr_read_b32 v227, a120\n"
"v_mul_f32 v227, v252, v227\n"
"v_accvgpr_write_b32 a120, v227\n"
"v_accvgpr_read_b32 v220, a121\n"
"v_mul_f32 v220, v252, v220\n"
"v_accvgpr_write_b32 a121, v220\n"
"v_accvgpr_read_b32 v221, a122\n"
"v_mul_f32 v221, v252, v221\n"
"v_accvgpr_write_b32 a122, v221\n"
"v_accvgpr_read_b32 v222, a123\n"
"v_mul_f32 v222, v252, v222\n"
"v_accvgpr_write_b32 a123, v222\n"
"v_accvgpr_read_b32 v223, a124\n"
"v_mul_f32 v223, v253, v223\n"
"v_accvgpr_write_b32 a124, v223\n"
"v_accvgpr_read_b32 v224, a125\n"
"v_mul_f32 v224, v253, v224\n"
"v_accvgpr_write_b32 a125, v224\n"
"v_accvgpr_read_b32 v225, a126\n"
"v_mul_f32 v225, v253, v225\n"
"v_accvgpr_write_b32 a126, v225\n"
"v_accvgpr_read_b32 v226, a127\n"
"v_mul_f32 v226, v253, v226\n"
"v_accvgpr_write_b32 a127, v226\n"
"v_accvgpr_read_b32 v227, a128\n"
"v_mul_f32 v227, v254, v227\n"
"v_accvgpr_write_b32 a128, v227\n"
"v_accvgpr_read_b32 v220, a129\n"
"v_mul_f32 v220, v254, v220\n"
"v_accvgpr_write_b32 a129, v220\n"
"v_accvgpr_read_b32 v221, a130\n"
"v_mul_f32 v221, v254, v221\n"
"v_accvgpr_write_b32 a130, v221\n"
"v_accvgpr_read_b32 v222, a131\n"
"v_mul_f32 v222, v254, v222\n"
"v_accvgpr_write_b32 a131, v222\n"
"v_accvgpr_read_b32 v223, a132\n"
"v_mul_f32 v223, v252, v223\n"
"v_accvgpr_write_b32 a132, v223\n"
"v_accvgpr_read_b32 v224, a133\n"
"v_mul_f32 v224, v252, v224\n"
"v_accvgpr_write_b32 a133, v224\n"
"v_accvgpr_read_b32 v225, a134\n"
"v_mul_f32 v225, v252, v225\n"
"v_accvgpr_write_b32 a134, v225\n"
"v_accvgpr_read_b32 v226, a135\n"
"v_mul_f32 v226, v252, v226\n"
"v_accvgpr_write_b32 a135, v226\n"
"v_accvgpr_read_b32 v227, a136\n"
"v_mul_f32 v227, v253, v227\n"
"v_accvgpr_write_b32 a136, v227\n"
"v_accvgpr_read_b32 v220, a137\n"
"v_mul_f32 v220, v253, v220\n"
"v_accvgpr_write_b32 a137, v220\n"
"v_accvgpr_read_b32 v221, a138\n"
"v_mul_f32 v221, v253, v221\n"
"v_accvgpr_write_b32 a138, v221\n"
"v_accvgpr_read_b32 v222, a139\n"
"v_mul_f32 v222, v253, v222\n"
"v_accvgpr_write_b32 a139, v222\n"
"v_accvgpr_read_b32 v223, a140\n"
"v_mul_f32 v223, v254, v223\n"
"v_accvgpr_write_b32 a140, v223\n"
"v_accvgpr_read_b32 v224, a141\n"
"v_mul_f32 v224, v254, v224\n"
"v_accvgpr_write_b32 a141, v224\n"
"v_accvgpr_read_b32 v225, a142\n"
"v_mul_f32 v225, v254, v225\n"
"v_accvgpr_write_b32 a142, v225\n"
"v_accvgpr_read_b32 v226, a143\n"
"v_mul_f32 v226, v254, v226\n"
"v_accvgpr_write_b32 a143, v226\n"
"v_accvgpr_read_b32 v227, a144\n"
"v_mul_f32 v227, v252, v227\n"
"v_accvgpr_write_b32 a144, v227\n"
"v_accvgpr_read_b32 v220, a145\n"
"v_mul_f32 v220, v252, v220\n"
"v_accvgpr_write_b32 a145, v220\n"
"v_accvgpr_read_b32 v221, a146\n"
"v_mul_f32 v221, v252, v221\n"
"v_accvgpr_write_b32 a146, v221\n"
"v_accvgpr_read_b32 v222, a147\n"
"v_mul_f32 v222, v252, v222\n"
"v_accvgpr_write_b32 a147, v222\n"
"v_accvgpr_read_b32 v223, a148\n"
"v_mul_f32 v223, v253, v223\n"
"v_accvgpr_write_b32 a148, v223\n"
"v_accvgpr_read_b32 v224, a149\n"
"v_mul_f32 v224, v253, v224\n"
"v_accvgpr_write_b32 a149, v224\n"
"v_accvgpr_read_b32 v225, a150\n"
"v_mul_f32 v225, v253, v225\n"
"v_accvgpr_write_b32 a150, v225\n"
"v_accvgpr_read_b32 v226, a151\n"
"v_mul_f32 v226, v253, v226\n"
"v_accvgpr_write_b32 a151, v226\n"
"v_accvgpr_read_b32 v227, a152\n"
"v_mul_f32 v227, v254, v227\n"
"v_accvgpr_write_b32 a152, v227\n"
"v_accvgpr_read_b32 v220, a153\n"
"v_mul_f32 v220, v254, v220\n"
"v_accvgpr_write_b32 a153, v220\n"
"v_accvgpr_read_b32 v221, a154\n"
"v_mul_f32 v221, v254, v221\n"
"v_accvgpr_write_b32 a154, v221\n"
"v_accvgpr_read_b32 v222, a155\n"
"v_mul_f32 v222, v254, v222\n"
"v_accvgpr_write_b32 a155, v222\n"
"v_accvgpr_read_b32 v223, a156\n"
"v_mul_f32 v223, v252, v223\n"
"v_accvgpr_write_b32 a156, v223\n"
"v_accvgpr_read_b32 v224, a157\n"
"v_mul_f32 v224, v252, v224\n"
"v_accvgpr_write_b32 a157, v224\n"
"v_accvgpr_read_b32 v225, a158\n"
"v_mul_f32 v225, v252, v225\n"
"v_accvgpr_write_b32 a158, v225\n"
"v_accvgpr_read_b32 v226, a159\n"
"v_mul_f32 v226, v252, v226\n"
"v_accvgpr_write_b32 a159, v226\n"
"v_accvgpr_read_b32 v227, a160\n"
"v_mul_f32 v227, v253, v227\n"
"v_accvgpr_write_b32 a160, v227\n"
"v_accvgpr_read_b32 v220, a161\n"
"v_mul_f32 v220, v253, v220\n"
"v_accvgpr_write_b32 a161, v220\n"
"v_accvgpr_read_b32 v221, a162\n"
"v_mul_f32 v221, v253, v221\n"
"v_accvgpr_write_b32 a162, v221\n"
"v_accvgpr_read_b32 v222, a163\n"
"v_mul_f32 v222, v253, v222\n"
"v_accvgpr_write_b32 a163, v222\n"
"v_accvgpr_read_b32 v223, a164\n"
"v_mul_f32 v223, v254, v223\n"
"v_accvgpr_write_b32 a164, v223\n"
"v_accvgpr_read_b32 v224, a165\n"
"v_mul_f32 v224, v254, v224\n"
"v_accvgpr_write_b32 a165, v224\n"
"v_accvgpr_read_b32 v225, a166\n"
"v_mul_f32 v225, v254, v225\n"
"v_accvgpr_write_b32 a166, v225\n"
"v_accvgpr_read_b32 v226, a167\n"
"v_mul_f32 v226, v254, v226\n"
"v_accvgpr_write_b32 a167, v226\n"
"v_accvgpr_read_b32 v227, a168\n"
"v_mul_f32 v227, v252, v227\n"
"v_accvgpr_write_b32 a168, v227\n"
"v_accvgpr_read_b32 v220, a169\n"
"v_mul_f32 v220, v252, v220\n"
"v_accvgpr_write_b32 a169, v220\n"
"v_accvgpr_read_b32 v221, a170\n"
"v_mul_f32 v221, v252, v221\n"
"v_accvgpr_write_b32 a170, v221\n"
"v_accvgpr_read_b32 v222, a171\n"
"v_mul_f32 v222, v252, v222\n"
"v_accvgpr_write_b32 a171, v222\n"
"v_accvgpr_read_b32 v223, a172\n"
"v_mul_f32 v223, v253, v223\n"
"v_accvgpr_write_b32 a172, v223\n"
"v_accvgpr_read_b32 v224, a173\n"
"v_mul_f32 v224, v253, v224\n"
"v_accvgpr_write_b32 a173, v224\n"
"v_accvgpr_read_b32 v225, a174\n"
"v_mul_f32 v225, v253, v225\n"
"v_accvgpr_write_b32 a174, v225\n"
"v_accvgpr_read_b32 v226, a175\n"
"v_mul_f32 v226, v253, v226\n"
"v_accvgpr_write_b32 a175, v226\n"
"v_accvgpr_read_b32 v227, a176\n"
"v_mul_f32 v227, v254, v227\n"
"v_accvgpr_write_b32 a176, v227\n"
"v_accvgpr_read_b32 v220, a177\n"
"v_mul_f32 v220, v254, v220\n"
"v_accvgpr_write_b32 a177, v220\n"
"v_accvgpr_read_b32 v221, a178\n"
"v_mul_f32 v221, v254, v221\n"
"v_accvgpr_write_b32 a178, v221\n"
"v_accvgpr_read_b32 v222, a179\n"
"v_mul_f32 v222, v254, v222\n"
"v_accvgpr_write_b32 a179, v222\n"
"v_accvgpr_read_b32 v223, a180\n"
"v_mul_f32 v223, v252, v223\n"
"v_accvgpr_write_b32 a180, v223\n"
"v_accvgpr_read_b32 v224, a181\n"
"v_mul_f32 v224, v252, v224\n"
"v_accvgpr_write_b32 a181, v224\n"
"v_accvgpr_read_b32 v225, a182\n"
"v_mul_f32 v225, v252, v225\n"
"v_accvgpr_write_b32 a182, v225\n"
"v_accvgpr_read_b32 v226, a183\n"
"v_mul_f32 v226, v252, v226\n"
"v_accvgpr_write_b32 a183, v226\n"
"v_accvgpr_read_b32 v227, a184\n"
"v_mul_f32 v227, v253, v227\n"
"v_accvgpr_write_b32 a184, v227\n"
"v_accvgpr_read_b32 v220, a185\n"
"v_mul_f32 v220, v253, v220\n"
"v_accvgpr_write_b32 a185, v220\n"
"v_accvgpr_read_b32 v221, a186\n"
"v_mul_f32 v221, v253, v221\n"
"v_accvgpr_write_b32 a186, v221\n"
"v_accvgpr_read_b32 v222, a187\n"
"v_mul_f32 v222, v253, v222\n"
"v_accvgpr_write_b32 a187, v222\n"
"v_accvgpr_read_b32 v223, a188\n"
"v_mul_f32 v223, v254, v223\n"
"v_accvgpr_write_b32 a188, v223\n"
"v_accvgpr_read_b32 v224, a189\n"
"v_mul_f32 v224, v254, v224\n"
"v_accvgpr_write_b32 a189, v224\n"
"v_accvgpr_read_b32 v225, a190\n"
"v_mul_f32 v225, v254, v225\n"
"v_accvgpr_write_b32 a190, v225\n"
"v_accvgpr_read_b32 v226, a191\n"
"v_mul_f32 v226, v254, v226\n"
"v_accvgpr_write_b32 a191, v226\n"
"s_nop 3\n"
"s_setpc_b64 s[64:65]\n"
  :: "s"(__builtin_amdgcn_kernarg_segment_ptr()), "s"(blockIdx.x), "s"(w)
  : "v0","v1","v2","v3","v4","v5","v6","v7","v8","v9","v10","v11","v12","v13","v14","v15","v16","v17","v18","v19","v20","v21","v22","v23","v24","v25","v26","v27","v28","v29","v30","v31","v32","v33","v34","v35","v36","v37","v38","v39","v40","v41","v42","v43","v44","v45","v46","v47","v48","v49","v50","v51","v52","v53","v54","v55","v56","v57","v58","v59","v60","v61","v62","v63","v64","v65","v66","v67","v68","v69","v70","v71","v72","v73","v74","v75","v76","v77","v78","v79","v80","v81","v82","v83","v84","v85","v86","v87","v88","v89","v90","v91","v92","v93","v94","v95","v96","v97","v98","v99","v100","v101","v102","v103","v104","v105","v106","v107","v108","v109","v110","v111","v112","v113","v114","v115","v116","v117","v118","v119","v120","v121","v122","v123","v124","v125","v126","v127","v128","v129","v130","v131","v132","v133","v134","v135","v136","v137","v138","v139","v140","v141","v142","v143","v144","v145","v146","v147","v148","v149","v150","v151","v152","v153","v154","v155","v156","v157","v158","v159","v160","v161","v162","v163","v164","v165","v166","v167","v168","v169","v170","v171","v172","v173","v174","v175","v176","v177","v178","v179","v180","v181","v182","v183","v184","v185","v186","v187","v188","v189","v190","v191","v192","v193","v194","v195","v196","v197","v198","v199","v200","v201","v202","v203","v204","v205","v206","v207","v208","v209","v210","v211","v212","v213","v214","v215","v216","v217","v218","v219","v220","v221","v222","v223","v224","v225","v226","v227","v228","v229","v230","v231","v232","v233","v234","v235","v236","v237","v238","v239","v240","v241","v242","v243","v244","v245","v246","v247","v248","v249","v250","v251","v252","v253","v254","v255","a0","a1","a2","a3","a4","a5","a6","a7","a8","a9","a10","a11","a12","a13","a14","a15","a16","a17","a18","a19","a20","a21","a22","a23","a24","a25","a26","a27","a28","a29","a30","a31","a32","a33","a34","a35","a36","a37","a38","a39","a40","a41","a42","a43","a44","a45","a46","a47","a48","a49","a50","a51","a52","a53","a54","a55","a56","a57","a58","a59","a60","a61","a62","a63","a64","a65","a66","a67","a68","a69","a70","a71","a72","a73","a74","a75","a76","a77","a78","a79","a80","a81","a82","a83","a84","a85","a86","a87","a88","a89","a90","a91","a92","a93","a94","a95","a96","a97","a98","a99","a100","a101","a102","a103","a104","a105","a106","a107","a108","a109","a110","a111","a112","a113","a114","a115","a116","a117","a118","a119","a120","a121","a122","a123","a124","a125","a126","a127","a128","a129","a130","a131","a132","a133","a134","a135","a136","a137","a138","a139","a140","a141","a142","a143","a144","a145","a146","a147","a148","a149","a150","a151","a152","a153","a154","a155","a156","a157","a158","a159","a160","a161","a162","a163","a164","a165","a166","a167","a168","a169","a170","a171","a172","a173","a174","a175","a176","a177","a178","a179","a180","a181","a182","a183","a184","a185","a186","a187","a188","a189","a190","a191","a192","a193","a194","a195","a196","a197","a198","a199","a200","a201","a202","a203","a204","a205","a206","a207","a208","a209","a210","a211","a212","a213","a214","a215","a216","a217","a218","a219","a220","a221","a222","a223","a224","a225","a226","a227","a228","a229","a230","a231","a232","a233","a234","a235","a236","a237","a238","a239","a240","a241","a242","a243","a244","a245","a246","a247","a248","a249","a250","a251","a252","a253","a254","a255","s4","s5","s6","s7","s8","s9","s10","s11","s12","s13","s14","s15","s16","s17","s18","s19","s20","s21","s22","s23","s24","s25","s26","s27","s28","s29","s30","s31","s32","s33","s34","s35","s36","s37","s38","s39","s40","s41","s42","s43","s44","s45","s46","s47","s48","s49","s50","s51","s52","s53","s54","s55","s56","s57","s58","s59","s60","s61","s62","s63","s64","s65","s66","s67","s68","s69","s70","s71","s72","s73","s74","s75","s76","s77","s78","s79","s80","s81","s82","s83","s84","s85","s86","s87","s88","s89","s90","s91","s92","s93","s94","s95","s96","s97","s98","s99","s100","s101","vcc","scc","memory");
}

extern "C" void kernel_launch(void* const* d_in, const int* in_sizes, int n_in, void* d_out, int out_size, void* d_ws, size_t ws_size,
                              hipStream_t stream) {
  mha_main<<<256, 256, 0, stream>>>((const char*)d_in[0], (const char*)d_in[1], (const char*)d_in[2], (const char*)d_in[3],
                                    (const char*)d_in[4], (char*)d_ws, (char*)d_out);
}
```
